```python
import torch

def _softmax(x, axis=-1):
    return torch.softmax(x, dim=axis)
def _gelu(x):
    return torch.nn.functional.gelu(x, approximate="tanh")
def _silu(x):
    return torch.nn.functional.silu(x)
def _sigmoid(x):
    return torch.sigmoid(x)
def _layernorm(x, axis=-1, eps=1e-5):
    m = x.mean(dim=axis, keepdim=True)
    v = x.var(dim=axis, keepdim=True, unbiased=False)
    return (x - m) / torch.sqrt(v + eps)
def _scatter_add(tgt, idx, src, axis=0):
    return tgt.clone().index_add_(axis, idx, src)
def _segment_sum(src, seg, n):
    out = torch.zeros((n,) + src.shape[1:], dtype=src.dtype)
    return out.index_add_(0, seg, src)
def _pad(x, pw):
    flat = tuple(v for pair in reversed(pw) for v in pair)
    return torch.nn.functional.pad(x, flat)

kernel_name = 'standalone__gqa_softmax__near_ridge'

def setup_inputs():

    _w = (lambda a: (lambda t: t.to(torch.bfloat16) if t.is_floating_point() else t)(torch.as_tensor(a)))
    B, Hq, Hkv, Sq, Sk, D = 8, 32, 32, 608, 1024, 128
    return {
        "q": _w((lambda *s: torch.randn(*s, dtype=torch.bfloat16) * 0.8)(B, Hq, Sq, D)),
        "k": _w((lambda *s: torch.randn(*s, dtype=torch.bfloat16) * 0.8)(B, Hkv, Sk, D)),
        "v": _w((lambda *s: torch.randn(*s, dtype=torch.bfloat16) * 0.05)(B, Hkv, Sk, D)),
    }

def reference(q, k, v):
    B, Hq, Sq, D = q.shape
    Hkv = k.shape[1]
    rep = Hq // Hkv
    k = (lambda x, n, axis: torch.repeat_interleave(x, n, dim=axis))(k, rep, axis=1)
    v = (lambda x, n, axis: torch.repeat_interleave(x, n, dim=axis))(v, rep, axis=1)
    scale = D ** -0.5
    scores = torch.einsum("bhqd,bhkd->bhqk", q, k) * scale
    p = _softmax(scores, axis=-1)
    return torch.einsum("bhqk,bhkd->bhqd", p, v)
```

```cpp
#include <hip/hip_runtime.h>
#include <hip/hip_bf16.h>
using bf16 = __hip_bfloat16;
constexpr int   D = 128, NW = 8, QBLK = 32, KVBLK = 64;
constexpr float SCALE = 0.088388347648318440f;
constexpr float THR = 8.f;
constexpr int SDEPTH = 2;
constexpr int B = 8, HQ = 32, HKV = 32, S = 608, SKV = 1024;
constexpr int LDQ = D, LDK = D, LDO = D;
constexpr size_t SHM_V = KVBLK * D * 2, SHM_K = KVBLK * D * 2, SHM_ATTN = 2 * SHM_V + 2 * SHM_K + NW * 64 * 4;
using bf16x8 = __attribute__((ext_vector_type(8))) short;
using s16x4  = __attribute__((ext_vector_type(4))) short;
using f32x16 = __attribute__((ext_vector_type(16))) float;
using u32x4  = __attribute__((ext_vector_type(4))) unsigned;
#define KSWZ(row, colB) ((row) * 256 + ((colB) ^ (((row) & 7) << 4)))
#define SBAR() __builtin_amdgcn_sched_barrier(0)
__device__ __forceinline__ int crow(int r, int hi) { return (r & 3) + 8 * (r >> 2) + 4 * hi; }
__device__ __forceinline__ unsigned cvtpk(float lo, float hi) {
  unsigned r; asm volatile("v_cvt_pk_bf16_f32 %0, %1, %2" : "=v"(r) : "v"(lo), "v"(hi)); return r;
}
__device__ __forceinline__ bf16x8 ld8(const bf16* p) { return *reinterpret_cast<const bf16x8*>(p); }
__device__ __forceinline__ void partialSM(f32x16& p0, f32x16& p1, float& m_reg, float& mn, float& alpha) {
  constexpr float C = SCALE * 1.4426950408889634f;
  float pmax = p0[0]; for (int r = 1; r < 16; ++r) pmax = fmaxf(pmax, p0[r]); for (int r = 0; r < 16; ++r) pmax = fmaxf(pmax, p1[r]);
  { auto rr = __builtin_amdgcn_permlane32_swap(__float_as_uint(pmax), __float_as_uint(pmax), false, false);
    pmax = fmaxf(__uint_as_float(rr[0]), __uint_as_float(rr[1])); }
  if (__builtin_expect(__all(pmax - m_reg <= THR / SCALE), 1)) { mn = m_reg; alpha = 1.f; }
  else { mn = fmaxf(m_reg, pmax); alpha = __builtin_amdgcn_exp2f((m_reg - mn) * C); m_reg = mn; }
  float mnC = -mn * C;
  for (int r = 0; r < 16; ++r) p0[r] = fmaf(p0[r], C, mnC); for (int r = 0; r < 16; ++r) p1[r] = fmaf(p1[r], C, mnC);
  for (int r = 0; r < 16; ++r) p0[r] = __builtin_amdgcn_exp2f(p0[r]);
}
__device__ __forceinline__ void finishSM(f32x16& p0, f32x16& p1, float alpha, float& l_reg, bf16x8& pa0, bf16x8& pa1, bf16x8& pa2, bf16x8& pa3) {
  for (int r = 0; r < 16; ++r) p1[r] = __builtin_amdgcn_exp2f(p1[r]);
  float ps = 0; for (int r = 0; r < 16; ++r) ps += p0[r]; for (int r = 0; r < 16; ++r) ps += p1[r];
  { auto rr = __builtin_amdgcn_permlane32_swap(__float_as_uint(ps), __float_as_uint(ps), false, false);
    ps = __uint_as_float(rr[0]) + __uint_as_float(rr[1]); }
  l_reg = l_reg * alpha + ps;
#define PK4(P, BASE, OUT) do { unsigned a0 = cvtpk(P[BASE + 0], P[BASE + 1]), a1 = cvtpk(P[BASE + 2], P[BASE + 3]);   \
    unsigned b0 = cvtpk(P[BASE + 4], P[BASE + 5]), b1 = cvtpk(P[BASE + 6], P[BASE + 7]);                              \
    auto r0 = __builtin_amdgcn_permlane32_swap(a0, b0, false, false); auto r1 = __builtin_amdgcn_permlane32_swap(a1, b1, false, false); \
    u32x4 w = {r0[0], r1[0], r0[1], r1[1]}; OUT = *reinterpret_cast<bf16x8*>(&w); } while (0)
  PK4(p0, 0, pa0); PK4(p0, 8, pa1); PK4(p1, 0, pa2); PK4(p1, 8, pa3);
#undef PK4
}
__device__ __forceinline__ void qkt(f32x16& p0, f32x16& p1, const bf16* Ks, const bf16x8* qr, int r32, int hi) {
  p0 = f32x16{}; p1 = f32x16{};
  for (int d0 = 0; d0 < 8; ++d0) { int cb = (d0 * 16 + hi * 8) * 2;
    bf16x8 b0 = *reinterpret_cast<const bf16x8*>((const char*)Ks + KSWZ(r32, cb));
    bf16x8 b1 = *reinterpret_cast<const bf16x8*>((const char*)Ks + KSWZ(32 + r32, cb));
    p0 = __builtin_amdgcn_mfma_f32_32x32x16_bf16(b0, qr[d0], p0, 0, 0, 0);
    p1 = __builtin_amdgcn_mfma_f32_32x32x16_bf16(b1, qr[d0], p1, 0, 0, 0); }
}
__device__ __forceinline__ int v_st(int k, int c) { const int kk = (k & ~0xC) | ((k & 4) << 1) | ((k & 8) >> 1); return ((kk >> 3) * 4 + (c >> 5)) * 512 + ((kk & 7) * 32 + (c & 31)) * 2; }
__device__ __forceinline__ int v_rd_base(int lane) { return ((lane & 3) << 3) | (((lane >> 2) & 3) << 6) | (((lane >> 4) & 1) << 5) | (((lane >> 5) & 1) << 8); }
constexpr int v_rd_off(int d0, int ks, int half) { return d0 * 512 + ks * 4096 + half * 2048; }
template <int OFF> __device__ __forceinline__ s16x4 tr_read(int vb) {
  s16x4 r; asm volatile("ds_read_b64_tr_b16 %0, %1 offset:%2" : "=&v"(r) : "v"(vb), "i"(OFF) : "memory"); return r;
}
template <int D0> __device__ __forceinline__ void pv_one(f32x16& od, int vb, bf16x8 pa0, bf16x8 pa1, bf16x8 pa2, bf16x8 pa3) {
  const s16x4 l0 = tr_read<v_rd_off(D0, 0, 0)>(vb), h0 = tr_read<v_rd_off(D0, 0, 1)>(vb), l1 = tr_read<v_rd_off(D0, 1, 0)>(vb), h1 = tr_read<v_rd_off(D0, 1, 1)>(vb);
  const s16x4 l2 = tr_read<v_rd_off(D0, 2, 0)>(vb), h2 = tr_read<v_rd_off(D0, 2, 1)>(vb), l3 = tr_read<v_rd_off(D0, 3, 0)>(vb), h3 = tr_read<v_rd_off(D0, 3, 1)>(vb);
  asm volatile("s_waitcnt lgkmcnt(0)" ::: "memory"); SBAR();
#define PK(L, H) (bf16x8){L[0], L[1], L[2], L[3], H[0], H[1], H[2], H[3]}
  od = __builtin_amdgcn_mfma_f32_32x32x16_bf16(pa0, PK(l0, h0), od, 0, 0, 0);
  od = __builtin_amdgcn_mfma_f32_32x32x16_bf16(pa1, PK(l1, h1), od, 0, 0, 0);
  od = __builtin_amdgcn_mfma_f32_32x32x16_bf16(pa2, PK(l2, h2), od, 0, 0, 0);
  od = __builtin_amdgcn_mfma_f32_32x32x16_bf16(pa3, PK(l3, h3), od, 0, 0, 0);
#undef PK
}
__device__ __forceinline__ void pv_d0(f32x16* o, int vb, bf16x8 pa0, bf16x8 pa1, bf16x8 pa2, bf16x8 pa3) {
  pv_one<0>(o[0], vb, pa0, pa1, pa2, pa3); pv_one<1>(o[1], vb, pa0, pa1, pa2, pa3); pv_one<2>(o[2], vb, pa0, pa1, pa2, pa3); pv_one<3>(o[3], vb, pa0, pa1, pa2, pa3);
}
__device__ __forceinline__ void attn_dense_body(const bf16* __restrict__ Qh, int row0, const bf16* __restrict__ Kh, const bf16* __restrict__ Vh,
                                                bf16* __restrict__ Oh, int seq, char* lds) {
  const int tid = threadIdx.x, wid = tid >> 6, lane = tid & 63, r32 = lane & 31, hi = lane >> 5;
  bf16* V_lds = (bf16*)lds; bf16* K_lds = (bf16*)(lds + 2 * SHM_V);
  float* ws = (float*)(lds + 2 * SHM_V + 2 * SHM_K) + wid * 64; float* li_l = ws; float* al_l = ws + 32;
  float m_reg = -1e30f, l_reg = 0; f32x16 o[4] = {}; bf16x8 qr[8];
  int qrow = row0 + wid * QBLK + r32; if (qrow > S - 1) qrow = S - 1;
  const bf16* Qw = Qh + (long)qrow * LDQ + hi * 8;
#pragma unroll
  for (int d0 = 0; d0 < 8; ++d0) qr[d0] = ld8(Qw + d0 * 16);
  const int sr = tid >> 4, sc = (tid & 15) * 8, vst0 = v_st(sr, sc), vst1 = v_st(32 + sr, sc);
  const int vb0 = (int)(uintptr_t)V_lds + v_rd_base(lane);
  struct { bf16x8 vs0, vs1, ks0, ks1; } sr_[SDEPTH];
#define SLOAD(i, k0) do { sr_[i].vs0 = ld8(&Vh[(long)((k0) + sr) * LDK + sc]); sr_[i].vs1 = ld8(&Vh[(long)((k0) + 32 + sr) * LDK + sc]); \
    sr_[i].ks0 = ld8(&Kh[(long)((k0) + sr) * LDK + sc]); sr_[i].ks1 = ld8(&Kh[(long)((k0) + 32 + sr) * LDK + sc]); } while (0)
#define SWRITE(b, i) do { *(bf16x8*)((char*)V_lds + (b) * SHM_V + vst0) = sr_[i].vs0;          \
    *(bf16x8*)((char*)V_lds + (b) * SHM_V + vst1) = sr_[i].vs1; int kc = sc * 2;               \
    *(bf16x8*)((char*)K_lds + (b) * SHM_K + KSWZ(sr, kc)) = sr_[i].ks0;                       \
    *(bf16x8*)((char*)K_lds + (b) * SHM_K + KSWZ(32 + sr, kc)) = sr_[i].ks1; } while (0)
#define SWAIT() do { asm volatile("s_waitcnt vmcnt(4)" ::: "memory"); } while (0)
#define RESC(a) do { if (__any((a) < 1.f)) { if (hi == 0) al_l[r32] = (a); asm volatile("s_waitcnt lgkmcnt(0)" ::: "memory"); \
    for (int d = 0; d < 4; ++d) for (int r = 0; r < 16; ++r) o[d][r] *= al_l[crow(r, hi)]; } } while (0)
  f32x16 pA0, pA1, pB0, pB1; float mnA, mnB, alA, alB; bf16x8 pa0, pa1, pa2, pa3; const int NT = seq / KVBLK;
  constexpr int SE = 0, SO = SDEPTH - 1;
  SLOAD(SE, 0); asm volatile("s_waitcnt vmcnt(0)" ::: "memory"); SWRITE(0, SE); __syncthreads();
  qkt(pA0, pA1, K_lds, qr, r32, hi); partialSM(pA0, pA1, m_reg, mnA, alA);
  SLOAD(SO, KVBLK); if (2 < NT) SLOAD(SE, 2 * KVBLK);
  SWAIT(); SWRITE(1, SO); __syncthreads();
  for (int j = 1; j + 1 < NT; j += 2) {
    SBAR(); qkt(pB0, pB1, (bf16*)((char*)K_lds + SHM_K), qr, r32, hi);
    finishSM(pA0, pA1, alA, l_reg, pa0, pa1, pa2, pa3); SBAR();
    SLOAD(SO, (j + SDEPTH) * KVBLK); SBAR();
    pv_d0(o, vb0, pa0, pa1, pa2, pa3); partialSM(pB0, pB1, m_reg, mnB, alB);
    __syncthreads(); SWAIT(); SWRITE(0, SE);
    RESC(alB); __syncthreads();
    SBAR(); qkt(pA0, pA1, K_lds, qr, r32, hi);
    finishSM(pB0, pB1, alB, l_reg, pa0, pa1, pa2, pa3); SBAR();
    if (j + 3 < NT) SLOAD(SE, (j + 1 + SDEPTH) * KVBLK); SBAR();
    pv_d0(o, vb0 + (int)SHM_V, pa0, pa1, pa2, pa3); partialSM(pA0, pA1, m_reg, mnA, alA);
    __syncthreads(); SWAIT(); SWRITE(1, SO);
    RESC(alA); __syncthreads();
  }
  SBAR(); qkt(pB0, pB1, (bf16*)((char*)K_lds + SHM_K), qr, r32, hi);
  finishSM(pA0, pA1, alA, l_reg, pa0, pa1, pa2, pa3); SBAR();
  pv_d0(o, vb0, pa0, pa1, pa2, pa3); partialSM(pB0, pB1, m_reg, mnB, alB);
  __syncthreads(); RESC(alB);
  finishSM(pB0, pB1, alB, l_reg, pa0, pa1, pa2, pa3); SBAR();
  pv_d0(o, vb0 + (int)SHM_V, pa0, pa1, pa2, pa3);
  if (hi == 0) li_l[r32] = l_reg; asm volatile("s_waitcnt lgkmcnt(0)" ::: "memory");
  float rli[16];
#pragma unroll
  for (int r = 0; r < 16; ++r) rli[r] = __builtin_amdgcn_rcpf(li_l[crow(r, hi)]);
  if (row0 + wid * QBLK < S) {
  bf16* Ow = Oh + (long)(row0 + wid * QBLK) * LDO;
#pragma unroll
  for (int r = 0; r < 16; ++r) { int orow = crow(r, hi);
    for (int d0 = 0; d0 < 4; ++d0) Ow[(long)orow * LDO + d0 * 32 + r32] = __float2bfloat16(o[d0][r] * rli[r]); }
  }
}
__global__ __launch_bounds__(64 * NW, 1)
void attn_fwd_dense(const bf16* __restrict__ Q, const bf16* __restrict__ K, const bf16* __restrict__ V, bf16* __restrict__ O) {
  extern __shared__ __attribute__((aligned(16))) char shm[];
  const int bid = blockIdx.x;
  const int xcd = bid & 7, idx = bid >> 3; const int bh = xcd * 32 + idx / 3, qb = idx % 3;
  attn_dense_body(Q + (long)bh * S * D, qb * 256, K + (long)bh * SKV * D, V + (long)bh * SKV * D, O + (long)bh * S * D, SKV, shm);
}
extern "C" void kernel_launch(void* const* d_in, const int* in_sizes, int n_in, void* d_out, int out_size, void* d_ws, size_t ws_size, hipStream_t stream) {
  hipFuncSetAttribute((const void*)attn_fwd_dense, hipFuncAttributeMaxDynamicSharedMemorySize, (int)SHM_ATTN);
  hipLaunchKernelGGL(attn_fwd_dense, dim3(768), dim3(64 * NW), SHM_ATTN, stream,
                     (const bf16*)d_in[0], (const bf16*)d_in[1], (const bf16*)d_in[2], (bf16*)d_out);
}
```

```cpp
#include <hip/hip_runtime.h>
#include <hip/hip_bf16.h>
typedef __attribute__((ext_vector_type(8))) short bf16x8;
typedef __attribute__((ext_vector_type(4))) float f32x4;
typedef __attribute__((ext_vector_type(4))) unsigned u32x4;
constexpr int SQ = 608, SK = 1024, HD = 128, LDS_BYTES = 163840;

typedef __attribute__((ext_vector_type(2))) float f32x2;
typedef __attribute__((ext_vector_type(2))) __bf16 bf16x2;
__device__ __forceinline__ unsigned cvtpk(float lo, float hi) { f32x2 v = {lo, hi}; return __builtin_bit_cast(unsigned, __builtin_convertvector(v, bf16x2)); }

__device__ __forceinline__ void true_max(const unsigned short* Qp, const unsigned short* Kp, float* wsw, int wave, int lane) {
  const int n = lane & 15, g = lane >> 4;
  for (int pass = 0; pass < 2; ++pass) {
    bf16x8 qf[5][4]; float m[5];
#pragma unroll
    for (int qt = 0; qt < 5; ++qt) {
      int row = wave * 80 + qt * 16 + n; if (row >= 304) row = 0;
      const unsigned short* qrow = Qp + ((long)pass * SQ + row) * HD + g * 8;
#pragma unroll
      for (int ds = 0; ds < 4; ++ds) {
        u32x4 w = *(const u32x4*)(qrow + ds * 32);
#pragma unroll
        for (int k = 0; k < 4; ++k) { float lo = __uint_as_float(w[k] << 16) * 0.12751743082f, hi = __uint_as_float(w[k] & 0xffff0000u) * 0.12751743082f; w[k] = cvtpk(lo, hi); }
        qf[qt][ds] = __builtin_bit_cast(bf16x8, w);
      }
      m[qt] = -INFINITY;
    }
#pragma unroll 2
    for (int kb = 0; kb < SK / 16; ++kb) {
      const unsigned short* krow = Kp + ((long)pass * SK + kb * 16 + n) * HD + g * 8;
      bf16x8 kf[4];
#pragma unroll
      for (int ds = 0; ds < 4; ++ds) kf[ds] = *(const bf16x8*)(krow + ds * 32);
#pragma unroll
      for (int qt = 0; qt < 5; ++qt) {
        f32x4 s = {0.f, 0.f, 0.f, 0.f};
#pragma unroll
        for (int ds = 0; ds < 4; ++ds) s = __builtin_amdgcn_mfma_f32_16x16x32_bf16(kf[ds], qf[qt][ds], s, 0, 0, 0);
        m[qt] = fmaxf(fmaxf(m[qt], fmaxf(s[0], s[1])), fmaxf(s[2], s[3]));
      }
    }
#pragma unroll
    for (int qt = 0; qt < 5; ++qt) {
      float x = m[qt]; x = fmaxf(x, __shfl_xor(x, 16)); x = fmaxf(x, __shfl_xor(x, 32));
      wsw[(pass * 5 + qt) * 64 + lane] = -x;
    }
  }
}

__device__ __forceinline__ unsigned attn_body(const unsigned short* Kp, const unsigned short* Vp, const unsigned short* Qp, unsigned short* Op,
                                              unsigned wave, unsigned mode, float* wsw, unsigned slot) {
  unsigned fail;
  asm volatile(
"s_mov_b32 s64, %5\n"
"s_mov_b32 s66, %6\n"
"s_mov_b64 s[68:69], %7\n"
"s_mov_b64 s[36:37], %1\n"
"s_and_b32 s37, s37, 0xffff\n"
"s_mov_b32 s38, 524288\n"
"s_mov_b32 s39, 0x20000\n"
"s_mov_b64 s[40:41], %2\n"
"s_sub_u32 s40, s40, 0x4000\n"
"s_subb_u32 s41, s41, 0\n"
"s_and_b32 s41, s41, 0xffff\n"
"s_mov_b32 s42, 540672\n"
"s_mov_b32 s43, 0x20000\n"
"s_mov_b64 s[44:45], %3\n"
"s_and_b32 s45, s45, 0xffff\n"
"s_mov_b32 s46, 77824\n"
"s_mov_b32 s47, 0x20000\n"
"s_mov_b64 s[48:49], %4\n"
"s_and_b32 s49, s49, 0xffff\n"
"s_mov_b32 s50, 77824\n"
"s_mov_b32 s51, 0x20000\n"
"s_lshl_b32 s53, s64, 12\n"
"s_or_b32 s54, s53, 0x10000\n"
"v_mbcnt_lo_u32_b32 v0, -1, 0\n"
"v_mbcnt_hi_u32_b32 v0, -1, v0\n"
"v_and_b32 v1, 15, v0\n"
"v_lshrrev_b32 v2, 4, v0\n"
"v_lshrrev_b32 v3, 2, v1\n"
"v_and_b32 v4, 3, v1\n"
"v_lshlrev_b32 v9, 8, v3\n"
"v_lshl_add_u32 v9, v2, 6, v9\n"
"v_lshl_add_u32 v183, v4, 4, v9\n"
"v_xor_b32 v184, 32, v183\n"
"s_add_u32 s63, s53, 0x0\n"
"s_add_u32 m0, s53, 0x0\n"
"s_nop 0\n"
"buffer_load_dwordx4 v183, s[36:39], s63 offen offset:0 lds\n"
"buffer_load_dwordx4 v184, s[36:39], s63 offen offset:1024 lds\n"
"buffer_load_dwordx4 v183, s[36:39], s63 offen offset:2048 lds\n"
"buffer_load_dwordx4 v184, s[36:39], s63 offen offset:3072 lds\n"
"s_getpc_b64 s[62:63]\n"
"s_mul_i32 s55, %8, 640\n"
"v_min_u32 v10, 4, v0\n"
"v_lshlrev_b32 v10, 7, v10\n"
"v_add_u32 v10, s55, v10\n"
"global_load_dword v255, v10, s[62:63]\n"
"s_mul_i32 s70, s64, 20480\n"
"s_add_u32 s71, s70, 4096\n"
"s_add_u32 s72, s70, 8192\n"
"s_add_u32 s73, s70, 12288\n"
"s_add_u32 s74, s70, 16384\n"
"s_mul_i32 s62, s64, 20480\n"
"s_add_u32 s62, s62, 98304\n"
"s_cmp_eq_u32 s64, 3\n"
"s_cselect_b32 s62, 49152, s62\n"
"s_add_u32 m0, s62, 0\n"
"s_nop 0\n"
"buffer_load_dwordx4 v183, s[44:47], s70 offen offset:0 nt lds\n"
"buffer_load_dwordx4 v184, s[44:47], s70 offen offset:1024 nt lds\n"
"buffer_load_dwordx4 v183, s[44:47], s70 offen offset:2048 nt lds\n"
"buffer_load_dwordx4 v184, s[44:47], s70 offen offset:3072 nt lds\n"
"s_add_u32 m0, s62, 4096\n"
"s_nop 0\n"
"buffer_load_dwordx4 v183, s[44:47], s71 offen offset:0 nt lds\n"
"buffer_load_dwordx4 v184, s[44:47], s71 offen offset:1024 nt lds\n"
"buffer_load_dwordx4 v183, s[44:47], s71 offen offset:2048 nt lds\n"
"buffer_load_dwordx4 v184, s[44:47], s71 offen offset:3072 nt lds\n"
"v_lshlrev_b32 v185, 8, v1\n"
"v_lshl_add_u32 v185, v2, 4, v185\n"
"s_add_u32 s52, s53, 0x10000\n"
"s_mov_b32 s56, 0x3e0293ee\n"
"s_mov_b32 s57, 0x3e0293ee\n"
"s_mov_b64 s[60:61], 0\n"
"s_mov_b32 s65, 0x5f800000\n"
"v_and_b32 v5, 1, v3\n"
"v_lshlrev_b32 v5, 1, v5\n"
"v_xor_b32 v6, v5, v2\n"
"v_lshlrev_b32 v180, 10, v3\n"
"v_lshl_add_u32 v180, v4, 6, v180\n"
"v_lshl_add_u32 v180, v6, 4, v180\n"
"v_lshlrev_b32 v7, 10, v2\n"
"v_lshl_add_u32 v7, v3, 6, v7\n"
"v_lshl_add_u32 v7, v4, 3, v7\n"
"v_and_b32 v8, 1, v2\n"
"v_lshl_add_u32 v181, v8, 5, v7\n"
"v_add_u32 v181, 0x10000, v181\n"
"v_xor_b32 v182, 32, v181\n"
"v_mov_b32 v5, 0x3f803f80\n"
"v_cmp_eq_u32 vcc, 0, v1\n"
"s_nop 1\n"
"v_cndmask_b32 v176, 0, v5, vcc\n"
"v_cndmask_b32 v177, 0, v5, vcc\n"
"v_cndmask_b32 v178, 0, v5, vcc\n"
"v_cndmask_b32 v179, 0, v5, vcc\n"
"v_add_u32 v186, s62, v180\n"
"s_cmp_eq_u32 s66, 0\n"
"s_cbranch_scc1 L_nogiven_%=\n"
"v_lshlrev_b32 v187, 2, v0\n"
"global_load_dword v136, v187, s[68:69] offset:0\n"
"global_load_dword v140, v187, s[68:69] offset:256\n"
"global_load_dword v144, v187, s[68:69] offset:512\n"
"global_load_dword v148, v187, s[68:69] offset:768\n"
"global_load_dword v152, v187, s[68:69] offset:1024\n"
"s_waitcnt vmcnt(0)\n"
"L_nogiven_%=:\n"
"s_waitcnt vmcnt(8)\n"
"s_barrier\n"
"ds_read_b128 v[192:195], v180 offset:0\n"
"ds_read_b128 v[196:199], v180 offset:256\n"
"ds_read_b128 v[200:203], v180 offset:512\n"
"ds_read_b128 v[204:207], v180 offset:768\n"
"ds_read_b128 v[208:211], v180 offset:4096\n"
"ds_read_b128 v[212:215], v180 offset:4352\n"
"ds_read_b128 v[216:219], v180 offset:4608\n"
"ds_read_b128 v[220:223], v180 offset:4864\n"
"ds_read_b128 v[224:227], v180 offset:8192\n"
"ds_read_b128 v[228:231], v180 offset:8448\n"
"ds_read_b128 v[232:235], v180 offset:8704\n"
"ds_read_b128 v[236:239], v180 offset:8960\n"
"ds_read_b128 v[240:243], v180 offset:12288\n"
"ds_read_b128 v[244:247], v180 offset:12544\n"
"ds_read_b128 v[248:251], v180 offset:12800\n"
"ds_read_b128 v[252:255], v180 offset:13056\n"
"s_add_u32 m0, s62, 8192\n"
"s_nop 0\n"
"buffer_load_dwordx4 v183, s[44:47], s72 offen offset:0 nt lds\n"
"buffer_load_dwordx4 v184, s[44:47], s72 offen offset:1024 nt lds\n"
"buffer_load_dwordx4 v183, s[44:47], s72 offen offset:2048 nt lds\n"
"buffer_load_dwordx4 v184, s[44:47], s72 offen offset:3072 nt lds\n"
"s_cmp_eq_u32 s64, 3\n"
"s_cbranch_scc1 L_w3_%=\n"
"s_waitcnt vmcnt(8)\n"
"ds_read_b128 v[56:59], v186 offset:0\n"
"ds_read_b128 v[60:63], v186 offset:256\n"
"ds_read_b128 v[64:67], v186 offset:512\n"
"ds_read_b128 v[68:71], v186 offset:768\n"
"s_waitcnt lgkmcnt(0)\n"
"v_lshlrev_b32 v88, 16, v56\n"
"v_and_b32 v89, 0xffff0000, v56\n"
"v_pk_mul_f32 v[88:89], v[88:89], s[56:57]\n"
"v_cvt_pk_bf16_f32 v90, v88, v89\n"
"v_accvgpr_write_b32 a160, v90\n"
"v_lshlrev_b32 v92, 16, v57\n"
"v_and_b32 v93, 0xffff0000, v57\n"
"v_pk_mul_f32 v[92:93], v[92:93], s[56:57]\n"
"v_cvt_pk_bf16_f32 v94, v92, v93\n"
"v_accvgpr_write_b32 a161, v94\n"
"v_lshlrev_b32 v88, 16, v58\n"
"v_and_b32 v89, 0xffff0000, v58\n"
"v_pk_mul_f32 v[88:89], v[88:89], s[56:57]\n"
"v_cvt_pk_bf16_f32 v90, v88, v89\n"
"v_accvgpr_write_b32 a162, v90\n"
"v_lshlrev_b32 v92, 16, v59\n"
"v_and_b32 v93, 0xffff0000, v59\n"
"v_pk_mul_f32 v[92:93], v[92:93], s[56:57]\n"
"v_cvt_pk_bf16_f32 v94, v92, v93\n"
"v_accvgpr_write_b32 a163, v94\n"
"v_lshlrev_b32 v88, 16, v60\n"
"v_and_b32 v89, 0xffff0000, v60\n"
"v_pk_mul_f32 v[88:89], v[88:89], s[56:57]\n"
"v_cvt_pk_bf16_f32 v90, v88, v89\n"
"v_accvgpr_write_b32 a164, v90\n"
"v_lshlrev_b32 v92, 16, v61\n"
"v_and_b32 v93, 0xffff0000, v61\n"
"v_pk_mul_f32 v[92:93], v[92:93], s[56:57]\n"
"v_cvt_pk_bf16_f32 v94, v92, v93\n"
"v_accvgpr_write_b32 a165, v94\n"
"v_lshlrev_b32 v88, 16, v62\n"
"v_and_b32 v89, 0xffff0000, v62\n"
"v_pk_mul_f32 v[88:89], v[88:89], s[56:57]\n"
"v_cvt_pk_bf16_f32 v90, v88, v89\n"
"v_accvgpr_write_b32 a166, v90\n"
"v_lshlrev_b32 v92, 16, v63\n"
"v_and_b32 v93, 0xffff0000, v63\n"
"v_pk_mul_f32 v[92:93], v[92:93], s[56:57]\n"
"v_cvt_pk_bf16_f32 v94, v92, v93\n"
"v_accvgpr_write_b32 a167, v94\n"
"v_mfma_f32_16x16x32_bf16 v[0:3], v[192:195], a[160:163], 0\n"
"v_mfma_f32_16x16x32_bf16 v[4:7], v[208:211], a[160:163], 0\n"
"v_mfma_f32_16x16x32_bf16 v[8:11], v[224:227], a[160:163], 0\n"
"v_mfma_f32_16x16x32_bf16 v[12:15], v[240:243], a[160:163], 0\n"
"v_lshlrev_b32 v88, 16, v64\n"
"v_and_b32 v89, 0xffff0000, v64\n"
"v_pk_mul_f32 v[88:89], v[88:89], s[56:57]\n"
"v_cvt_pk_bf16_f32 v90, v88, v89\n"
"v_accvgpr_write_b32 a168, v90\n"
"v_lshlrev_b32 v92, 16, v65\n"
"v_and_b32 v93, 0xffff0000, v65\n"
"v_pk_mul_f32 v[92:93], v[92:93], s[56:57]\n"
"v_cvt_pk_bf16_f32 v94, v92, v93\n"
"v_accvgpr_write_b32 a169, v94\n"
"v_lshlrev_b32 v88, 16, v66\n"
"v_and_b32 v89, 0xffff0000, v66\n"
"v_pk_mul_f32 v[88:89], v[88:89], s[56:57]\n"
"v_cvt_pk_bf16_f32 v90, v88, v89\n"
"v_accvgpr_write_b32 a170, v90\n"
"v_lshlrev_b32 v92, 16, v67\n"
"v_and_b32 v93, 0xffff0000, v67\n"
"v_pk_mul_f32 v[92:93], v[92:93], s[56:57]\n"
"v_cvt_pk_bf16_f32 v94, v92, v93\n"
"v_accvgpr_write_b32 a171, v94\n"
"v_mfma_f32_16x16x32_bf16 v[0:3], v[196:199], a[164:167], v[0:3]\n"
"v_mfma_f32_16x16x32_bf16 v[4:7], v[212:215], a[164:167], v[4:7]\n"
"v_mfma_f32_16x16x32_bf16 v[8:11], v[228:231], a[164:167], v[8:11]\n"
"v_mfma_f32_16x16x32_bf16 v[12:15], v[244:247], a[164:167], v[12:15]\n"
"v_lshlrev_b32 v88, 16, v68\n"
"v_and_b32 v89, 0xffff0000, v68\n"
"v_pk_mul_f32 v[88:89], v[88:89], s[56:57]\n"
"v_cvt_pk_bf16_f32 v90, v88, v89\n"
"v_accvgpr_write_b32 a172, v90\n"
"v_lshlrev_b32 v92, 16, v69\n"
"v_and_b32 v93, 0xffff0000, v69\n"
"v_pk_mul_f32 v[92:93], v[92:93], s[56:57]\n"
"v_cvt_pk_bf16_f32 v94, v92, v93\n"
"v_accvgpr_write_b32 a173, v94\n"
"v_lshlrev_b32 v88, 16, v70\n"
"v_and_b32 v89, 0xffff0000, v70\n"
"v_pk_mul_f32 v[88:89], v[88:89], s[56:57]\n"
"v_cvt_pk_bf16_f32 v90, v88, v89\n"
"v_accvgpr_write_b32 a174, v90\n"
"v_lshlrev_b32 v92, 16, v71\n"
"v_and_b32 v93, 0xffff0000, v71\n"
"v_pk_mul_f32 v[92:93], v[92:93], s[56:57]\n"
"v_cvt_pk_bf16_f32 v94, v92, v93\n"
"v_accvgpr_write_b32 a175, v94\n"
"v_mfma_f32_16x16x32_bf16 v[0:3], v[200:203], a[168:171], v[0:3]\n"
"v_mfma_f32_16x16x32_bf16 v[4:7], v[216:219], a[168:171], v[4:7]\n"
"v_mfma_f32_16x16x32_bf16 v[8:11], v[232:235], a[168:171], v[8:11]\n"
"v_mfma_f32_16x16x32_bf16 v[12:15], v[248:251], a[168:171], v[12:15]\n"
"s_nop 1\n"
"v_mfma_f32_16x16x32_bf16 v[0:3], v[204:207], a[172:175], v[0:3]\n"
"v_mfma_f32_16x16x32_bf16 v[4:7], v[220:223], a[172:175], v[4:7]\n"
"v_mfma_f32_16x16x32_bf16 v[8:11], v[236:239], a[172:175], v[8:11]\n"
"v_mfma_f32_16x16x32_bf16 v[12:15], v[252:255], a[172:175], v[12:15]\n"
"s_nop 3\n"
"s_cmp_lg_u32 s66, 0\n"
"s_cbranch_scc1 L_gm0_a_%=\n"
"v_max3_f32 v136, v0, v1, v2\n"
"v_max3_f32 v137, v3, v4, v5\n"
"v_max3_f32 v136, v136, v6, v7\n"
"v_max3_f32 v137, v137, v8, v9\n"
"v_max3_f32 v136, v136, v10, v11\n"
"v_max3_f32 v137, v137, v12, v13\n"
"v_max3_f32 v136, v136, v14, v15\n"
"v_max_f32 v138, v136, v137\n"
"v_max_f32 v139, v136, v137\n"
"s_nop 1\n"
"v_permlane32_swap_b32 v138, v139\n"
"s_nop 1\n"
"v_max_f32 v136, v138, v139\n"
"v_max_f32 v137, v138, v139\n"
"s_nop 1\n"
"v_permlane16_swap_b32 v136, v137\n"
"s_nop 1\n"
"v_max_f32 v136, v136, v137\n"
"v_sub_f32 v136, 0, v136\n"
"L_gm0_a_%=:\n"
"v_mov_b32 v137, v136\n"
"v_mov_b32 v138, v136\n"
"v_mov_b32 v139, v136\n"
"v_pk_add_f32 v[0:1], v[0:1], v[136:137]\n"
"v_pk_add_f32 v[2:3], v[2:3], v[136:137]\n"
"v_pk_add_f32 v[4:5], v[4:5], v[136:137]\n"
"v_pk_add_f32 v[6:7], v[6:7], v[136:137]\n"
"v_pk_add_f32 v[8:9], v[8:9], v[136:137]\n"
"v_pk_add_f32 v[10:11], v[10:11], v[136:137]\n"
"v_pk_add_f32 v[12:13], v[12:13], v[136:137]\n"
"v_pk_add_f32 v[14:15], v[14:15], v[136:137]\n"
"v_exp_f32 v0, v0\n"
"v_exp_f32 v1, v1\n"
"v_exp_f32 v2, v2\n"
"v_exp_f32 v3, v3\n"
"v_exp_f32 v4, v4\n"
"v_exp_f32 v5, v5\n"
"v_exp_f32 v6, v6\n"
"v_exp_f32 v7, v7\n"
"v_cvt_pk_bf16_f32 v16, v0, v1\n"
"v_cvt_pk_bf16_f32 v17, v2, v3\n"
"v_exp_f32 v8, v8\n"
"v_exp_f32 v9, v9\n"
"v_exp_f32 v10, v10\n"
"v_exp_f32 v11, v11\n"
"v_cvt_pk_bf16_f32 v18, v4, v5\n"
"v_cvt_pk_bf16_f32 v19, v6, v7\n"
"v_exp_f32 v12, v12\n"
"v_exp_f32 v13, v13\n"
"v_exp_f32 v14, v14\n"
"v_exp_f32 v15, v15\n"
"v_cvt_pk_bf16_f32 v20, v8, v9\n"
"v_cvt_pk_bf16_f32 v21, v10, v11\n"
"s_nop 0\n"
"v_cvt_pk_bf16_f32 v22, v12, v13\n"
"v_cvt_pk_bf16_f32 v23, v14, v15\n"
"s_add_u32 m0, s62, 12288\n"
"s_nop 0\n"
"buffer_load_dwordx4 v183, s[44:47], s73 offen offset:0 nt lds\n"
"buffer_load_dwordx4 v184, s[44:47], s73 offen offset:1024 nt lds\n"
"buffer_load_dwordx4 v183, s[44:47], s73 offen offset:2048 nt lds\n"
"buffer_load_dwordx4 v184, s[44:47], s73 offen offset:3072 nt lds\n"
"s_waitcnt vmcnt(8)\n"
"ds_read_b128 v[72:75], v186 offset:4096\n"
"ds_read_b128 v[76:79], v186 offset:4352\n"
"ds_read_b128 v[80:83], v186 offset:4608\n"
"ds_read_b128 v[84:87], v186 offset:4864\n"
"s_waitcnt lgkmcnt(0)\n"
"v_lshlrev_b32 v88, 16, v72\n"
"v_and_b32 v89, 0xffff0000, v72\n"
"v_pk_mul_f32 v[88:89], v[88:89], s[56:57]\n"
"v_cvt_pk_bf16_f32 v90, v88, v89\n"
"v_accvgpr_write_b32 a176, v90\n"
"v_lshlrev_b32 v92, 16, v73\n"
"v_and_b32 v93, 0xffff0000, v73\n"
"v_pk_mul_f32 v[92:93], v[92:93], s[56:57]\n"
"v_cvt_pk_bf16_f32 v94, v92, v93\n"
"v_accvgpr_write_b32 a177, v94\n"
"v_lshlrev_b32 v88, 16, v74\n"
"v_and_b32 v89, 0xffff0000, v74\n"
"v_pk_mul_f32 v[88:89], v[88:89], s[56:57]\n"
"v_cvt_pk_bf16_f32 v90, v88, v89\n"
"v_accvgpr_write_b32 a178, v90\n"
"v_lshlrev_b32 v92, 16, v75\n"
"v_and_b32 v93, 0xffff0000, v75\n"
"v_pk_mul_f32 v[92:93], v[92:93], s[56:57]\n"
"v_cvt_pk_bf16_f32 v94, v92, v93\n"
"v_accvgpr_write_b32 a179, v94\n"
"v_lshlrev_b32 v88, 16, v76\n"
"v_and_b32 v89, 0xffff0000, v76\n"
"v_pk_mul_f32 v[88:89], v[88:89], s[56:57]\n"
"v_cvt_pk_bf16_f32 v90, v88, v89\n"
"v_accvgpr_write_b32 a180, v90\n"
"v_lshlrev_b32 v92, 16, v77\n"
"v_and_b32 v93, 0xffff0000, v77\n"
"v_pk_mul_f32 v[92:93], v[92:93], s[56:57]\n"
"v_cvt_pk_bf16_f32 v94, v92, v93\n"
"v_accvgpr_write_b32 a181, v94\n"
"v_lshlrev_b32 v88, 16, v78\n"
"v_and_b32 v89, 0xffff0000, v78\n"
"v_pk_mul_f32 v[88:89], v[88:89], s[56:57]\n"
"v_cvt_pk_bf16_f32 v90, v88, v89\n"
"v_accvgpr_write_b32 a182, v90\n"
"v_lshlrev_b32 v92, 16, v79\n"
"v_and_b32 v93, 0xffff0000, v79\n"
"v_pk_mul_f32 v[92:93], v[92:93], s[56:57]\n"
"v_cvt_pk_bf16_f32 v94, v92, v93\n"
"v_accvgpr_write_b32 a183, v94\n"
"v_mfma_f32_16x16x32_bf16 v[0:3], v[192:195], a[176:179], 0\n"
"v_mfma_f32_16x16x32_bf16 v[4:7], v[208:211], a[176:179], 0\n"
"v_mfma_f32_16x16x32_bf16 v[8:11], v[224:227], a[176:179], 0\n"
"v_mfma_f32_16x16x32_bf16 v[12:15], v[240:243], a[176:179], 0\n"
"v_lshlrev_b32 v88, 16, v80\n"
"v_and_b32 v89, 0xffff0000, v80\n"
"v_pk_mul_f32 v[88:89], v[88:89], s[56:57]\n"
"v_cvt_pk_bf16_f32 v90, v88, v89\n"
"v_accvgpr_write_b32 a184, v90\n"
"v_lshlrev_b32 v92, 16, v81\n"
"v_and_b32 v93, 0xffff0000, v81\n"
"v_pk_mul_f32 v[92:93], v[92:93], s[56:57]\n"
"v_cvt_pk_bf16_f32 v94, v92, v93\n"
"v_accvgpr_write_b32 a185, v94\n"
"v_lshlrev_b32 v88, 16, v82\n"
"v_and_b32 v89, 0xffff0000, v82\n"
"v_pk_mul_f32 v[88:89], v[88:89], s[56:57]\n"
"v_cvt_pk_bf16_f32 v90, v88, v89\n"
"v_accvgpr_write_b32 a186, v90\n"
"v_lshlrev_b32 v92, 16, v83\n"
"v_and_b32 v93, 0xffff0000, v83\n"
"v_pk_mul_f32 v[92:93], v[92:93], s[56:57]\n"
"v_cvt_pk_bf16_f32 v94, v92, v93\n"
"v_accvgpr_write_b32 a187, v94\n"
"v_mfma_f32_16x16x32_bf16 v[0:3], v[196:199], a[180:183], v[0:3]\n"
"v_mfma_f32_16x16x32_bf16 v[4:7], v[212:215], a[180:183], v[4:7]\n"
"v_mfma_f32_16x16x32_bf16 v[8:11], v[228:231], a[180:183], v[8:11]\n"
"v_mfma_f32_16x16x32_bf16 v[12:15], v[244:247], a[180:183], v[12:15]\n"
"v_lshlrev_b32 v88, 16, v84\n"
"v_and_b32 v89, 0xffff0000, v84\n"
"v_pk_mul_f32 v[88:89], v[88:89], s[56:57]\n"
"v_cvt_pk_bf16_f32 v90, v88, v89\n"
"v_accvgpr_write_b32 a188, v90\n"
"v_lshlrev_b32 v92, 16, v85\n"
"v_and_b32 v93, 0xffff0000, v85\n"
"v_pk_mul_f32 v[92:93], v[92:93], s[56:57]\n"
"v_cvt_pk_bf16_f32 v94, v92, v93\n"
"v_accvgpr_write_b32 a189, v94\n"
"v_lshlrev_b32 v88, 16, v86\n"
"v_and_b32 v89, 0xffff0000, v86\n"
"v_pk_mul_f32 v[88:89], v[88:89], s[56:57]\n"
"v_cvt_pk_bf16_f32 v90, v88, v89\n"
"v_accvgpr_write_b32 a190, v90\n"
"v_lshlrev_b32 v92, 16, v87\n"
"v_and_b32 v93, 0xffff0000, v87\n"
"v_pk_mul_f32 v[92:93], v[92:93], s[56:57]\n"
"v_cvt_pk_bf16_f32 v94, v92, v93\n"
"v_accvgpr_write_b32 a191, v94\n"
"v_mfma_f32_16x16x32_bf16 v[0:3], v[200:203], a[184:187], v[0:3]\n"
"v_mfma_f32_16x16x32_bf16 v[4:7], v[216:219], a[184:187], v[4:7]\n"
"v_mfma_f32_16x16x32_bf16 v[8:11], v[232:235], a[184:187], v[8:11]\n"
"v_mfma_f32_16x16x32_bf16 v[12:15], v[248:251], a[184:187], v[12:15]\n"
"s_nop 1\n"
"v_mfma_f32_16x16x32_bf16 v[0:3], v[204:207], a[188:191], v[0:3]\n"
"v_mfma_f32_16x16x32_bf16 v[4:7], v[220:223], a[188:191], v[4:7]\n"
"v_mfma_f32_16x16x32_bf16 v[8:11], v[236:239], a[188:191], v[8:11]\n"
"v_mfma_f32_16x16x32_bf16 v[12:15], v[252:255], a[188:191], v[12:15]\n"
"s_nop 3\n"
"s_cmp_lg_u32 s66, 0\n"
"s_cbranch_scc1 L_gm1_a_%=\n"
"v_max3_f32 v140, v0, v1, v2\n"
"v_max3_f32 v141, v3, v4, v5\n"
"v_max3_f32 v140, v140, v6, v7\n"
"v_max3_f32 v141, v141, v8, v9\n"
"v_max3_f32 v140, v140, v10, v11\n"
"v_max3_f32 v141, v141, v12, v13\n"
"v_max3_f32 v140, v140, v14, v15\n"
"v_max_f32 v142, v140, v141\n"
"v_max_f32 v143, v140, v141\n"
"s_nop 1\n"
"v_permlane32_swap_b32 v142, v143\n"
"s_nop 1\n"
"v_max_f32 v140, v142, v143\n"
"v_max_f32 v141, v142, v143\n"
"s_nop 1\n"
"v_permlane16_swap_b32 v140, v141\n"
"s_nop 1\n"
"v_max_f32 v140, v140, v141\n"
"v_sub_f32 v140, 0, v140\n"
"L_gm1_a_%=:\n"
"v_mov_b32 v141, v140\n"
"v_mov_b32 v142, v140\n"
"v_mov_b32 v143, v140\n"
"v_pk_add_f32 v[0:1], v[0:1], v[140:141]\n"
"v_pk_add_f32 v[2:3], v[2:3], v[140:141]\n"
"v_pk_add_f32 v[4:5], v[4:5], v[140:141]\n"
"v_pk_add_f32 v[6:7], v[6:7], v[140:141]\n"
"v_pk_add_f32 v[8:9], v[8:9], v[140:141]\n"
"v_pk_add_f32 v[10:11], v[10:11], v[140:141]\n"
"v_pk_add_f32 v[12:13], v[12:13], v[140:141]\n"
"v_pk_add_f32 v[14:15], v[14:15], v[140:141]\n"
"v_exp_f32 v0, v0\n"
"v_exp_f32 v1, v1\n"
"v_exp_f32 v2, v2\n"
"v_exp_f32 v3, v3\n"
"v_exp_f32 v4, v4\n"
"v_exp_f32 v5, v5\n"
"v_exp_f32 v6, v6\n"
"v_exp_f32 v7, v7\n"
"v_cvt_pk_bf16_f32 v24, v0, v1\n"
"v_cvt_pk_bf16_f32 v25, v2, v3\n"
"v_exp_f32 v8, v8\n"
"v_exp_f32 v9, v9\n"
"v_exp_f32 v10, v10\n"
"v_exp_f32 v11, v11\n"
"v_cvt_pk_bf16_f32 v26, v4, v5\n"
"v_cvt_pk_bf16_f32 v27, v6, v7\n"
"v_exp_f32 v12, v12\n"
"v_exp_f32 v13, v13\n"
"v_exp_f32 v14, v14\n"
"v_exp_f32 v15, v15\n"
"v_cvt_pk_bf16_f32 v28, v8, v9\n"
"v_cvt_pk_bf16_f32 v29, v10, v11\n"
"s_nop 0\n"
"v_cvt_pk_bf16_f32 v30, v12, v13\n"
"v_cvt_pk_bf16_f32 v31, v14, v15\n"
"s_add_u32 m0, s62, 16384\n"
"s_nop 0\n"
"buffer_load_dwordx4 v183, s[44:47], s74 offen offset:0 nt lds\n"
"buffer_load_dwordx4 v184, s[44:47], s74 offen offset:1024 nt lds\n"
"buffer_load_dwordx4 v183, s[44:47], s74 offen offset:2048 nt lds\n"
"buffer_load_dwordx4 v184, s[44:47], s74 offen offset:3072 nt lds\n"
"s_waitcnt vmcnt(8)\n"
"ds_read_b128 v[56:59], v186 offset:8192\n"
"ds_read_b128 v[60:63], v186 offset:8448\n"
"ds_read_b128 v[64:67], v186 offset:8704\n"
"ds_read_b128 v[68:71], v186 offset:8960\n"
"s_waitcnt lgkmcnt(0)\n"
"v_lshlrev_b32 v88, 16, v56\n"
"v_and_b32 v89, 0xffff0000, v56\n"
"v_pk_mul_f32 v[88:89], v[88:89], s[56:57]\n"
"v_cvt_pk_bf16_f32 v90, v88, v89\n"
"v_accvgpr_write_b32 a192, v90\n"
"v_lshlrev_b32 v92, 16, v57\n"
"v_and_b32 v93, 0xffff0000, v57\n"
"v_pk_mul_f32 v[92:93], v[92:93], s[56:57]\n"
"v_cvt_pk_bf16_f32 v94, v92, v93\n"
"v_accvgpr_write_b32 a193, v94\n"
"v_lshlrev_b32 v88, 16, v58\n"
"v_and_b32 v89, 0xffff0000, v58\n"
"v_pk_mul_f32 v[88:89], v[88:89], s[56:57]\n"
"v_cvt_pk_bf16_f32 v90, v88, v89\n"
"v_accvgpr_write_b32 a194, v90\n"
"v_lshlrev_b32 v92, 16, v59\n"
"v_and_b32 v93, 0xffff0000, v59\n"
"v_pk_mul_f32 v[92:93], v[92:93], s[56:57]\n"
"v_cvt_pk_bf16_f32 v94, v92, v93\n"
"v_accvgpr_write_b32 a195, v94\n"
"v_lshlrev_b32 v88, 16, v60\n"
"v_and_b32 v89, 0xffff0000, v60\n"
"v_pk_mul_f32 v[88:89], v[88:89], s[56:57]\n"
"v_cvt_pk_bf16_f32 v90, v88, v89\n"
"v_accvgpr_write_b32 a196, v90\n"
"v_lshlrev_b32 v92, 16, v61\n"
"v_and_b32 v93, 0xffff0000, v61\n"
"v_pk_mul_f32 v[92:93], v[92:93], s[56:57]\n"
"v_cvt_pk_bf16_f32 v94, v92, v93\n"
"v_accvgpr_write_b32 a197, v94\n"
"v_lshlrev_b32 v88, 16, v62\n"
"v_and_b32 v89, 0xffff0000, v62\n"
"v_pk_mul_f32 v[88:89], v[88:89], s[56:57]\n"
"v_cvt_pk_bf16_f32 v90, v88, v89\n"
"v_accvgpr_write_b32 a198, v90\n"
"v_lshlrev_b32 v92, 16, v63\n"
"v_and_b32 v93, 0xffff0000, v63\n"
"v_pk_mul_f32 v[92:93], v[92:93], s[56:57]\n"
"v_cvt_pk_bf16_f32 v94, v92, v93\n"
"v_accvgpr_write_b32 a199, v94\n"
"v_mfma_f32_16x16x32_bf16 v[0:3], v[192:195], a[192:195], 0\n"
"v_mfma_f32_16x16x32_bf16 v[4:7], v[208:211], a[192:195], 0\n"
"v_mfma_f32_16x16x32_bf16 v[8:11], v[224:227], a[192:195], 0\n"
"v_mfma_f32_16x16x32_bf16 v[12:15], v[240:243], a[192:195], 0\n"
"v_lshlrev_b32 v88, 16, v64\n"
"v_and_b32 v89, 0xffff0000, v64\n"
"v_pk_mul_f32 v[88:89], v[88:89], s[56:57]\n"
"v_cvt_pk_bf16_f32 v90, v88, v89\n"
"v_accvgpr_write_b32 a200, v90\n"
"v_lshlrev_b32 v92, 16, v65\n"
"v_and_b32 v93, 0xffff0000, v65\n"
"v_pk_mul_f32 v[92:93], v[92:93], s[56:57]\n"
"v_cvt_pk_bf16_f32 v94, v92, v93\n"
"v_accvgpr_write_b32 a201, v94\n"
"v_lshlrev_b32 v88, 16, v66\n"
"v_and_b32 v89, 0xffff0000, v66\n"
"v_pk_mul_f32 v[88:89], v[88:89], s[56:57]\n"
"v_cvt_pk_bf16_f32 v90, v88, v89\n"
"v_accvgpr_write_b32 a202, v90\n"
"v_lshlrev_b32 v92, 16, v67\n"
"v_and_b32 v93, 0xffff0000, v67\n"
"v_pk_mul_f32 v[92:93], v[92:93], s[56:57]\n"
"v_cvt_pk_bf16_f32 v94, v92, v93\n"
"v_accvgpr_write_b32 a203, v94\n"
"v_mfma_f32_16x16x32_bf16 v[0:3], v[196:199], a[196:199], v[0:3]\n"
"v_mfma_f32_16x16x32_bf16 v[4:7], v[212:215], a[196:199], v[4:7]\n"
"v_mfma_f32_16x16x32_bf16 v[8:11], v[228:231], a[196:199], v[8:11]\n"
"v_mfma_f32_16x16x32_bf16 v[12:15], v[244:247], a[196:199], v[12:15]\n"
"v_lshlrev_b32 v88, 16, v68\n"
"v_and_b32 v89, 0xffff0000, v68\n"
"v_pk_mul_f32 v[88:89], v[88:89], s[56:57]\n"
"v_cvt_pk_bf16_f32 v90, v88, v89\n"
"v_accvgpr_write_b32 a204, v90\n"
"v_lshlrev_b32 v92, 16, v69\n"
"v_and_b32 v93, 0xffff0000, v69\n"
"v_pk_mul_f32 v[92:93], v[92:93], s[56:57]\n"
"v_cvt_pk_bf16_f32 v94, v92, v93\n"
"v_accvgpr_write_b32 a205, v94\n"
"v_lshlrev_b32 v88, 16, v70\n"
"v_and_b32 v89, 0xffff0000, v70\n"
"v_pk_mul_f32 v[88:89], v[88:89], s[56:57]\n"
"v_cvt_pk_bf16_f32 v90, v88, v89\n"
"v_accvgpr_write_b32 a206, v90\n"
"v_lshlrev_b32 v92, 16, v71\n"
"v_and_b32 v93, 0xffff0000, v71\n"
"v_pk_mul_f32 v[92:93], v[92:93], s[56:57]\n"
"v_cvt_pk_bf16_f32 v94, v92, v93\n"
"v_accvgpr_write_b32 a207, v94\n"
"v_mfma_f32_16x16x32_bf16 v[0:3], v[200:203], a[200:203], v[0:3]\n"
"v_mfma_f32_16x16x32_bf16 v[4:7], v[216:219], a[200:203], v[4:7]\n"
"v_mfma_f32_16x16x32_bf16 v[8:11], v[232:235], a[200:203], v[8:11]\n"
"v_mfma_f32_16x16x32_bf16 v[12:15], v[248:251], a[200:203], v[12:15]\n"
"s_nop 1\n"
"v_mfma_f32_16x16x32_bf16 v[0:3], v[204:207], a[204:207], v[0:3]\n"
"v_mfma_f32_16x16x32_bf16 v[4:7], v[220:223], a[204:207], v[4:7]\n"
"v_mfma_f32_16x16x32_bf16 v[8:11], v[236:239], a[204:207], v[8:11]\n"
"v_mfma_f32_16x16x32_bf16 v[12:15], v[252:255], a[204:207], v[12:15]\n"
"s_nop 3\n"
"s_cmp_lg_u32 s66, 0\n"
"s_cbranch_scc1 L_gm2_a_%=\n"
"v_max3_f32 v144, v0, v1, v2\n"
"v_max3_f32 v145, v3, v4, v5\n"
"v_max3_f32 v144, v144, v6, v7\n"
"v_max3_f32 v145, v145, v8, v9\n"
"v_max3_f32 v144, v144, v10, v11\n"
"v_max3_f32 v145, v145, v12, v13\n"
"v_max3_f32 v144, v144, v14, v15\n"
"v_max_f32 v146, v144, v145\n"
"v_max_f32 v147, v144, v145\n"
"s_nop 1\n"
"v_permlane32_swap_b32 v146, v147\n"
"s_nop 1\n"
"v_max_f32 v144, v146, v147\n"
"v_max_f32 v145, v146, v147\n"
"s_nop 1\n"
"v_permlane16_swap_b32 v144, v145\n"
"s_nop 1\n"
"v_max_f32 v144, v144, v145\n"
"v_sub_f32 v144, 0, v144\n"
"L_gm2_a_%=:\n"
"v_mov_b32 v145, v144\n"
"v_mov_b32 v146, v144\n"
"v_mov_b32 v147, v144\n"
"v_pk_add_f32 v[0:1], v[0:1], v[144:145]\n"
"v_pk_add_f32 v[2:3], v[2:3], v[144:145]\n"
"v_pk_add_f32 v[4:5], v[4:5], v[144:145]\n"
"v_pk_add_f32 v[6:7], v[6:7], v[144:145]\n"
"v_pk_add_f32 v[8:9], v[8:9], v[144:145]\n"
"v_pk_add_f32 v[10:11], v[10:11], v[144:145]\n"
"v_pk_add_f32 v[12:13], v[12:13], v[144:145]\n"
"v_pk_add_f32 v[14:15], v[14:15], v[144:145]\n"
"v_exp_f32 v0, v0\n"
"v_exp_f32 v1, v1\n"
"v_exp_f32 v2, v2\n"
"v_exp_f32 v3, v3\n"
"v_exp_f32 v4, v4\n"
"v_exp_f32 v5, v5\n"
"v_exp_f32 v6, v6\n"
"v_exp_f32 v7, v7\n"
"v_cvt_pk_bf16_f32 v32, v0, v1\n"
"v_cvt_pk_bf16_f32 v33, v2, v3\n"
"v_exp_f32 v8, v8\n"
"v_exp_f32 v9, v9\n"
"v_exp_f32 v10, v10\n"
"v_exp_f32 v11, v11\n"
"v_cvt_pk_bf16_f32 v34, v4, v5\n"
"v_cvt_pk_bf16_f32 v35, v6, v7\n"
"v_exp_f32 v12, v12\n"
"v_exp_f32 v13, v13\n"
"v_exp_f32 v14, v14\n"
"v_exp_f32 v15, v15\n"
"v_cvt_pk_bf16_f32 v36, v8, v9\n"
"v_cvt_pk_bf16_f32 v37, v10, v11\n"
"s_nop 0\n"
"v_cvt_pk_bf16_f32 v38, v12, v13\n"
"v_cvt_pk_bf16_f32 v39, v14, v15\n"
"s_add_u32 s63, s53, 0x4000\n"
"s_add_u32 m0, s53, 0x4000\n"
"s_nop 0\n"
"buffer_load_dwordx4 v183, s[36:39], s63 offen offset:0 lds\n"
"buffer_load_dwordx4 v184, s[36:39], s63 offen offset:1024 lds\n"
"buffer_load_dwordx4 v183, s[36:39], s63 offen offset:2048 lds\n"
"buffer_load_dwordx4 v184, s[36:39], s63 offen offset:3072 lds\n"
"s_waitcnt vmcnt(8)\n"
"ds_read_b128 v[72:75], v186 offset:12288\n"
"ds_read_b128 v[76:79], v186 offset:12544\n"
"ds_read_b128 v[80:83], v186 offset:12800\n"
"ds_read_b128 v[84:87], v186 offset:13056\n"
"s_waitcnt lgkmcnt(0)\n"
"v_lshlrev_b32 v88, 16, v72\n"
"v_and_b32 v89, 0xffff0000, v72\n"
"v_pk_mul_f32 v[88:89], v[88:89], s[56:57]\n"
"v_cvt_pk_bf16_f32 v90, v88, v89\n"
"v_accvgpr_write_b32 a208, v90\n"
"v_lshlrev_b32 v92, 16, v73\n"
"v_and_b32 v93, 0xffff0000, v73\n"
"v_pk_mul_f32 v[92:93], v[92:93], s[56:57]\n"
"v_cvt_pk_bf16_f32 v94, v92, v93\n"
"v_accvgpr_write_b32 a209, v94\n"
"v_lshlrev_b32 v88, 16, v74\n"
"v_and_b32 v89, 0xffff0000, v74\n"
"v_pk_mul_f32 v[88:89], v[88:89], s[56:57]\n"
"v_cvt_pk_bf16_f32 v90, v88, v89\n"
"v_accvgpr_write_b32 a210, v90\n"
"v_lshlrev_b32 v92, 16, v75\n"
"v_and_b32 v93, 0xffff0000, v75\n"
"v_pk_mul_f32 v[92:93], v[92:93], s[56:57]\n"
"v_cvt_pk_bf16_f32 v94, v92, v93\n"
"v_accvgpr_write_b32 a211, v94\n"
"v_lshlrev_b32 v88, 16, v76\n"
"v_and_b32 v89, 0xffff0000, v76\n"
"v_pk_mul_f32 v[88:89], v[88:89], s[56:57]\n"
"v_cvt_pk_bf16_f32 v90, v88, v89\n"
"v_accvgpr_write_b32 a212, v90\n"
"v_lshlrev_b32 v92, 16, v77\n"
"v_and_b32 v93, 0xffff0000, v77\n"
"v_pk_mul_f32 v[92:93], v[92:93], s[56:57]\n"
"v_cvt_pk_bf16_f32 v94, v92, v93\n"
"v_accvgpr_write_b32 a213, v94\n"
"v_lshlrev_b32 v88, 16, v78\n"
"v_and_b32 v89, 0xffff0000, v78\n"
"v_pk_mul_f32 v[88:89], v[88:89], s[56:57]\n"
"v_cvt_pk_bf16_f32 v90, v88, v89\n"
"v_accvgpr_write_b32 a214, v90\n"
"v_lshlrev_b32 v92, 16, v79\n"
"v_and_b32 v93, 0xffff0000, v79\n"
"v_pk_mul_f32 v[92:93], v[92:93], s[56:57]\n"
"v_cvt_pk_bf16_f32 v94, v92, v93\n"
"v_accvgpr_write_b32 a215, v94\n"
"v_mfma_f32_16x16x32_bf16 v[0:3], v[192:195], a[208:211], 0\n"
"v_mfma_f32_16x16x32_bf16 v[4:7], v[208:211], a[208:211], 0\n"
"v_mfma_f32_16x16x32_bf16 v[8:11], v[224:227], a[208:211], 0\n"
"v_mfma_f32_16x16x32_bf16 v[12:15], v[240:243], a[208:211], 0\n"
"v_lshlrev_b32 v88, 16, v80\n"
"v_and_b32 v89, 0xffff0000, v80\n"
"v_pk_mul_f32 v[88:89], v[88:89], s[56:57]\n"
"v_cvt_pk_bf16_f32 v90, v88, v89\n"
"v_accvgpr_write_b32 a216, v90\n"
"v_lshlrev_b32 v92, 16, v81\n"
"v_and_b32 v93, 0xffff0000, v81\n"
"v_pk_mul_f32 v[92:93], v[92:93], s[56:57]\n"
"v_cvt_pk_bf16_f32 v94, v92, v93\n"
"v_accvgpr_write_b32 a217, v94\n"
"v_lshlrev_b32 v88, 16, v82\n"
"v_and_b32 v89, 0xffff0000, v82\n"
"v_pk_mul_f32 v[88:89], v[88:89], s[56:57]\n"
"v_cvt_pk_bf16_f32 v90, v88, v89\n"
"v_accvgpr_write_b32 a218, v90\n"
"v_lshlrev_b32 v92, 16, v83\n"
"v_and_b32 v93, 0xffff0000, v83\n"
"v_pk_mul_f32 v[92:93], v[92:93], s[56:57]\n"
"v_cvt_pk_bf16_f32 v94, v92, v93\n"
"v_accvgpr_write_b32 a219, v94\n"
"v_mfma_f32_16x16x32_bf16 v[0:3], v[196:199], a[212:215], v[0:3]\n"
"v_mfma_f32_16x16x32_bf16 v[4:7], v[212:215], a[212:215], v[4:7]\n"
"v_mfma_f32_16x16x32_bf16 v[8:11], v[228:231], a[212:215], v[8:11]\n"
"v_mfma_f32_16x16x32_bf16 v[12:15], v[244:247], a[212:215], v[12:15]\n"
"v_lshlrev_b32 v88, 16, v84\n"
"v_and_b32 v89, 0xffff0000, v84\n"
"v_pk_mul_f32 v[88:89], v[88:89], s[56:57]\n"
"v_cvt_pk_bf16_f32 v90, v88, v89\n"
"v_accvgpr_write_b32 a220, v90\n"
"v_lshlrev_b32 v92, 16, v85\n"
"v_and_b32 v93, 0xffff0000, v85\n"
"v_pk_mul_f32 v[92:93], v[92:93], s[56:57]\n"
"v_cvt_pk_bf16_f32 v94, v92, v93\n"
"v_accvgpr_write_b32 a221, v94\n"
"v_lshlrev_b32 v88, 16, v86\n"
"v_and_b32 v89, 0xffff0000, v86\n"
"v_pk_mul_f32 v[88:89], v[88:89], s[56:57]\n"
"v_cvt_pk_bf16_f32 v90, v88, v89\n"
"v_accvgpr_write_b32 a222, v90\n"
"v_lshlrev_b32 v92, 16, v87\n"
"v_and_b32 v93, 0xffff0000, v87\n"
"v_pk_mul_f32 v[92:93], v[92:93], s[56:57]\n"
"v_cvt_pk_bf16_f32 v94, v92, v93\n"
"v_accvgpr_write_b32 a223, v94\n"
"v_mfma_f32_16x16x32_bf16 v[0:3], v[200:203], a[216:219], v[0:3]\n"
"v_mfma_f32_16x16x32_bf16 v[4:7], v[216:219], a[216:219], v[4:7]\n"
"v_mfma_f32_16x16x32_bf16 v[8:11], v[232:235], a[216:219], v[8:11]\n"
"v_mfma_f32_16x16x32_bf16 v[12:15], v[248:251], a[216:219], v[12:15]\n"
"s_nop 1\n"
"v_mfma_f32_16x16x32_bf16 v[0:3], v[204:207], a[220:223], v[0:3]\n"
"v_mfma_f32_16x16x32_bf16 v[4:7], v[220:223], a[220:223], v[4:7]\n"
"v_mfma_f32_16x16x32_bf16 v[8:11], v[236:239], a[220:223], v[8:11]\n"
"v_mfma_f32_16x16x32_bf16 v[12:15], v[252:255], a[220:223], v[12:15]\n"
"s_nop 3\n"
"s_cmp_lg_u32 s66, 0\n"
"s_cbranch_scc1 L_gm3_a_%=\n"
"v_max3_f32 v148, v0, v1, v2\n"
"v_max3_f32 v149, v3, v4, v5\n"
"v_max3_f32 v148, v148, v6, v7\n"
"v_max3_f32 v149, v149, v8, v9\n"
"v_max3_f32 v148, v148, v10, v11\n"
"v_max3_f32 v149, v149, v12, v13\n"
"v_max3_f32 v148, v148, v14, v15\n"
"v_max_f32 v150, v148, v149\n"
"v_max_f32 v151, v148, v149\n"
"s_nop 1\n"
"v_permlane32_swap_b32 v150, v151\n"
"s_nop 1\n"
"v_max_f32 v148, v150, v151\n"
"v_max_f32 v149, v150, v151\n"
"s_nop 1\n"
"v_permlane16_swap_b32 v148, v149\n"
"s_nop 1\n"
"v_max_f32 v148, v148, v149\n"
"v_sub_f32 v148, 0, v148\n"
"L_gm3_a_%=:\n"
"v_mov_b32 v149, v148\n"
"v_mov_b32 v150, v148\n"
"v_mov_b32 v151, v148\n"
"v_pk_add_f32 v[0:1], v[0:1], v[148:149]\n"
"v_pk_add_f32 v[2:3], v[2:3], v[148:149]\n"
"v_pk_add_f32 v[4:5], v[4:5], v[148:149]\n"
"v_pk_add_f32 v[6:7], v[6:7], v[148:149]\n"
"v_pk_add_f32 v[8:9], v[8:9], v[148:149]\n"
"v_pk_add_f32 v[10:11], v[10:11], v[148:149]\n"
"v_pk_add_f32 v[12:13], v[12:13], v[148:149]\n"
"v_pk_add_f32 v[14:15], v[14:15], v[148:149]\n"
"v_exp_f32 v0, v0\n"
"v_exp_f32 v1, v1\n"
"v_exp_f32 v2, v2\n"
"v_exp_f32 v3, v3\n"
"v_exp_f32 v4, v4\n"
"v_exp_f32 v5, v5\n"
"v_exp_f32 v6, v6\n"
"v_exp_f32 v7, v7\n"
"v_cvt_pk_bf16_f32 v40, v0, v1\n"
"v_cvt_pk_bf16_f32 v41, v2, v3\n"
"v_exp_f32 v8, v8\n"
"v_exp_f32 v9, v9\n"
"v_exp_f32 v10, v10\n"
"v_exp_f32 v11, v11\n"
"v_cvt_pk_bf16_f32 v42, v4, v5\n"
"v_cvt_pk_bf16_f32 v43, v6, v7\n"
"v_exp_f32 v12, v12\n"
"v_exp_f32 v13, v13\n"
"v_exp_f32 v14, v14\n"
"v_exp_f32 v15, v15\n"
"v_cvt_pk_bf16_f32 v44, v8, v9\n"
"v_cvt_pk_bf16_f32 v45, v10, v11\n"
"s_nop 0\n"
"v_cvt_pk_bf16_f32 v46, v12, v13\n"
"v_cvt_pk_bf16_f32 v47, v14, v15\n"
"s_add_u32 s63, s53, 0x4000\n"
"s_add_u32 m0, s54, 0x0\n"
"s_nop 0\n"
"buffer_load_dwordx4 v183, s[40:43], s63 offen offset:0 lds\n"
"buffer_load_dwordx4 v184, s[40:43], s63 offen offset:1024 lds\n"
"buffer_load_dwordx4 v183, s[40:43], s63 offen offset:2048 lds\n"
"buffer_load_dwordx4 v184, s[40:43], s63 offen offset:3072 lds\n"
"s_waitcnt vmcnt(8)\n"
"ds_read_b128 v[56:59], v186 offset:16384\n"
"ds_read_b128 v[60:63], v186 offset:16640\n"
"ds_read_b128 v[64:67], v186 offset:16896\n"
"ds_read_b128 v[68:71], v186 offset:17152\n"
"s_waitcnt lgkmcnt(0)\n"
"s_add_u32 s44, s44, 155648\n"
"s_addc_u32 s45, s45, 0\n"
"s_add_u32 s63, s53, 0x8000\n"
"s_add_u32 m0, s53, 0x8000\n"
"s_nop 0\n"
"buffer_load_dwordx4 v183, s[36:39], s63 offen offset:0 lds\n"
"buffer_load_dwordx4 v184, s[36:39], s63 offen offset:1024 lds\n"
"buffer_load_dwordx4 v183, s[36:39], s63 offen offset:2048 lds\n"
"buffer_load_dwordx4 v184, s[36:39], s63 offen offset:3072 lds\n"
"v_lshlrev_b32 v88, 16, v56\n"
"v_and_b32 v89, 0xffff0000, v56\n"
"v_pk_mul_f32 v[88:89], v[88:89], s[56:57]\n"
"v_cvt_pk_bf16_f32 v90, v88, v89\n"
"v_accvgpr_write_b32 a224, v90\n"
"v_lshlrev_b32 v92, 16, v57\n"
"v_and_b32 v93, 0xffff0000, v57\n"
"v_pk_mul_f32 v[92:93], v[92:93], s[56:57]\n"
"v_cvt_pk_bf16_f32 v94, v92, v93\n"
"v_accvgpr_write_b32 a225, v94\n"
"v_lshlrev_b32 v88, 16, v58\n"
"v_and_b32 v89, 0xffff0000, v58\n"
"v_pk_mul_f32 v[88:89], v[88:89], s[56:57]\n"
"v_cvt_pk_bf16_f32 v90, v88, v89\n"
"v_accvgpr_write_b32 a226, v90\n"
"v_lshlrev_b32 v92, 16, v59\n"
"v_and_b32 v93, 0xffff0000, v59\n"
"v_pk_mul_f32 v[92:93], v[92:93], s[56:57]\n"
"v_cvt_pk_bf16_f32 v94, v92, v93\n"
"v_accvgpr_write_b32 a227, v94\n"
"v_lshlrev_b32 v88, 16, v60\n"
"v_and_b32 v89, 0xffff0000, v60\n"
"v_pk_mul_f32 v[88:89], v[88:89], s[56:57]\n"
"v_cvt_pk_bf16_f32 v90, v88, v89\n"
"v_accvgpr_write_b32 a228, v90\n"
"v_lshlrev_b32 v92, 16, v61\n"
"v_and_b32 v93, 0xffff0000, v61\n"
"v_pk_mul_f32 v[92:93], v[92:93], s[56:57]\n"
"v_cvt_pk_bf16_f32 v94, v92, v93\n"
"v_accvgpr_write_b32 a229, v94\n"
"v_lshlrev_b32 v88, 16, v62\n"
"v_and_b32 v89, 0xffff0000, v62\n"
"v_pk_mul_f32 v[88:89], v[88:89], s[56:57]\n"
"v_cvt_pk_bf16_f32 v90, v88, v89\n"
"v_accvgpr_write_b32 a230, v90\n"
"v_lshlrev_b32 v92, 16, v63\n"
"v_and_b32 v93, 0xffff0000, v63\n"
"v_pk_mul_f32 v[92:93], v[92:93], s[56:57]\n"
"v_cvt_pk_bf16_f32 v94, v92, v93\n"
"v_accvgpr_write_b32 a231, v94\n"
"v_mfma_f32_16x16x32_bf16 v[0:3], v[192:195], a[224:227], 0\n"
"v_mfma_f32_16x16x32_bf16 v[4:7], v[208:211], a[224:227], 0\n"
"v_mfma_f32_16x16x32_bf16 v[8:11], v[224:227], a[224:227], 0\n"
"v_mfma_f32_16x16x32_bf16 v[12:15], v[240:243], a[224:227], 0\n"
"v_lshlrev_b32 v88, 16, v64\n"
"v_and_b32 v89, 0xffff0000, v64\n"
"v_pk_mul_f32 v[88:89], v[88:89], s[56:57]\n"
"v_cvt_pk_bf16_f32 v90, v88, v89\n"
"v_accvgpr_write_b32 a232, v90\n"
"v_lshlrev_b32 v92, 16, v65\n"
"v_and_b32 v93, 0xffff0000, v65\n"
"v_pk_mul_f32 v[92:93], v[92:93], s[56:57]\n"
"v_cvt_pk_bf16_f32 v94, v92, v93\n"
"v_accvgpr_write_b32 a233, v94\n"
"v_lshlrev_b32 v88, 16, v66\n"
"v_and_b32 v89, 0xffff0000, v66\n"
"v_pk_mul_f32 v[88:89], v[88:89], s[56:57]\n"
"v_cvt_pk_bf16_f32 v90, v88, v89\n"
"v_accvgpr_write_b32 a234, v90\n"
"v_lshlrev_b32 v92, 16, v67\n"
"v_and_b32 v93, 0xffff0000, v67\n"
"v_pk_mul_f32 v[92:93], v[92:93], s[56:57]\n"
"v_cvt_pk_bf16_f32 v94, v92, v93\n"
"v_accvgpr_write_b32 a235, v94\n"
"v_mfma_f32_16x16x32_bf16 v[0:3], v[196:199], a[228:231], v[0:3]\n"
"v_mfma_f32_16x16x32_bf16 v[4:7], v[212:215], a[228:231], v[4:7]\n"
"v_mfma_f32_16x16x32_bf16 v[8:11], v[228:231], a[228:231], v[8:11]\n"
"v_mfma_f32_16x16x32_bf16 v[12:15], v[244:247], a[228:231], v[12:15]\n"
"v_lshlrev_b32 v88, 16, v68\n"
"v_and_b32 v89, 0xffff0000, v68\n"
"v_pk_mul_f32 v[88:89], v[88:89], s[56:57]\n"
"v_cvt_pk_bf16_f32 v90, v88, v89\n"
"v_accvgpr_write_b32 a236, v90\n"
"v_lshlrev_b32 v92, 16, v69\n"
"v_and_b32 v93, 0xffff0000, v69\n"
"v_pk_mul_f32 v[92:93], v[92:93], s[56:57]\n"
"v_cvt_pk_bf16_f32 v94, v92, v93\n"
"v_accvgpr_write_b32 a237, v94\n"
"v_lshlrev_b32 v88, 16, v70\n"
"v_and_b32 v89, 0xffff0000, v70\n"
"v_pk_mul_f32 v[88:89], v[88:89], s[56:57]\n"
"v_cvt_pk_bf16_f32 v90, v88, v89\n"
"v_accvgpr_write_b32 a238, v90\n"
"v_lshlrev_b32 v92, 16, v71\n"
"v_and_b32 v93, 0xffff0000, v71\n"
"v_pk_mul_f32 v[92:93], v[92:93], s[56:57]\n"
"v_cvt_pk_bf16_f32 v94, v92, v93\n"
"v_accvgpr_write_b32 a239, v94\n"
"v_mfma_f32_16x16x32_bf16 v[0:3], v[200:203], a[232:235], v[0:3]\n"
"v_mfma_f32_16x16x32_bf16 v[4:7], v[216:219], a[232:235], v[4:7]\n"
"v_mfma_f32_16x16x32_bf16 v[8:11], v[232:235], a[232:235], v[8:11]\n"
"v_mfma_f32_16x16x32_bf16 v[12:15], v[248:251], a[232:235], v[12:15]\n"
"s_add_u32 s63, s53, 0x8000\n"
"s_add_u32 m0, s54, 0x4000\n"
"s_nop 0\n"
"buffer_load_dwordx4 v183, s[40:43], s63 offen offset:0 lds\n"
"buffer_load_dwordx4 v184, s[40:43], s63 offen offset:1024 lds\n"
"buffer_load_dwordx4 v183, s[40:43], s63 offen offset:2048 lds\n"
"buffer_load_dwordx4 v184, s[40:43], s63 offen offset:3072 lds\n"
"v_mfma_f32_16x16x32_bf16 v[0:3], v[204:207], a[236:239], v[0:3]\n"
"v_mfma_f32_16x16x32_bf16 v[4:7], v[220:223], a[236:239], v[4:7]\n"
"v_mfma_f32_16x16x32_bf16 v[8:11], v[236:239], a[236:239], v[8:11]\n"
"v_mfma_f32_16x16x32_bf16 v[12:15], v[252:255], a[236:239], v[12:15]\n"
"s_nop 3\n"
"s_cmp_lg_u32 s66, 0\n"
"s_cbranch_scc1 L_gm4_a_%=\n"
"v_max3_f32 v152, v0, v1, v2\n"
"v_max3_f32 v153, v3, v4, v5\n"
"v_max3_f32 v152, v152, v6, v7\n"
"v_max3_f32 v153, v153, v8, v9\n"
"v_max3_f32 v152, v152, v10, v11\n"
"v_max3_f32 v153, v153, v12, v13\n"
"v_max3_f32 v152, v152, v14, v15\n"
"v_max_f32 v154, v152, v153\n"
"v_max_f32 v155, v152, v153\n"
"s_nop 1\n"
"v_permlane32_swap_b32 v154, v155\n"
"s_nop 1\n"
"v_max_f32 v152, v154, v155\n"
"v_max_f32 v153, v154, v155\n"
"s_nop 1\n"
"v_permlane16_swap_b32 v152, v153\n"
"s_nop 1\n"
"v_max_f32 v152, v152, v153\n"
"v_sub_f32 v152, 0, v152\n"
"L_gm4_a_%=:\n"
"v_mov_b32 v153, v152\n"
"v_mov_b32 v154, v152\n"
"v_mov_b32 v155, v152\n"
"v_pk_add_f32 v[0:1], v[0:1], v[152:153]\n"
"v_pk_add_f32 v[2:3], v[2:3], v[152:153]\n"
"v_pk_add_f32 v[4:5], v[4:5], v[152:153]\n"
"v_pk_add_f32 v[6:7], v[6:7], v[152:153]\n"
"v_pk_add_f32 v[8:9], v[8:9], v[152:153]\n"
"v_pk_add_f32 v[10:11], v[10:11], v[152:153]\n"
"v_pk_add_f32 v[12:13], v[12:13], v[152:153]\n"
"v_pk_add_f32 v[14:15], v[14:15], v[152:153]\n"
"v_exp_f32 v0, v0\n"
"v_exp_f32 v1, v1\n"
"v_exp_f32 v2, v2\n"
"v_exp_f32 v3, v3\n"
"v_exp_f32 v4, v4\n"
"v_exp_f32 v5, v5\n"
"v_exp_f32 v6, v6\n"
"v_exp_f32 v7, v7\n"
"v_cvt_pk_bf16_f32 v48, v0, v1\n"
"v_cvt_pk_bf16_f32 v49, v2, v3\n"
"v_exp_f32 v8, v8\n"
"v_exp_f32 v9, v9\n"
"v_exp_f32 v10, v10\n"
"v_exp_f32 v11, v11\n"
"v_cvt_pk_bf16_f32 v50, v4, v5\n"
"v_cvt_pk_bf16_f32 v51, v6, v7\n"
"s_nop 0\n"
"v_cvt_pk_bf16_f32 v52, v8, v9\n"
"v_cvt_pk_bf16_f32 v53, v10, v11\n"
"s_waitcnt vmcnt(8) lgkmcnt(0)\n"
"s_barrier\n"
"ds_read_b128 v[96:99], v180 offset:16384\n"
"ds_read_b128 v[100:103], v180 offset:16640\n"
"ds_read_b64_tr_b16 v[120:121], v181 offset:0\n"
"ds_read_b64_tr_b16 v[122:123], v181 offset:4096\n"
"ds_read_b64_tr_b16 v[124:125], v181 offset:8192\n"
"ds_read_b64_tr_b16 v[126:127], v181 offset:12288\n"
"ds_read_b128 v[112:115], v180 offset:16896\n"
"ds_read_b128 v[116:119], v180 offset:17152\n"
"s_add_u32 s63, s53, 0xc000\n"
"s_add_u32 m0, s53, 0xc000\n"
"s_nop 0\n"
"buffer_load_dwordx4 v183, s[36:39], s63 offen offset:0 lds\n"
"buffer_load_dwordx4 v184, s[36:39], s63 offen offset:1024 lds\n"
"buffer_load_dwordx4 v183, s[36:39], s63 offen offset:2048 lds\n"
"buffer_load_dwordx4 v184, s[36:39], s63 offen offset:3072 lds\n"
"s_add_u32 s63, s53, 0xc000\n"
"s_add_u32 m0, s54, 0x8000\n"
"s_nop 0\n"
"buffer_load_dwordx4 v183, s[40:43], s63 offen offset:0 lds\n"
"buffer_load_dwordx4 v184, s[40:43], s63 offen offset:1024 lds\n"
"buffer_load_dwordx4 v183, s[40:43], s63 offen offset:2048 lds\n"
"buffer_load_dwordx4 v184, s[40:43], s63 offen offset:3072 lds\n"
"s_add_u32 m0, s54, 0xc000\n"
"s_nop 0\n"
"buffer_load_dwordx4 v183, s[40:43], s52 offen offset:0 lds\n"
"s_waitcnt lgkmcnt(2)\n"
"v_mfma_f32_16x16x32_bf16 v[0:3], v[96:99], a[160:163], v[136:139]\n"
"v_exp_f32 v12, v12\n"
"v_mfma_f32_16x16x32_bf16 v[0:3], v[100:103], a[164:167], v[0:3]\n"
"s_waitcnt lgkmcnt(0)\n"
"v_mfma_f32_16x16x32_bf16 v[0:3], v[112:115], a[168:171], v[0:3]\n"
"buffer_load_dwordx4 v184, s[40:43], s52 offen offset:1024 lds\n"
"v_mfma_f32_16x16x32_bf16 v[0:3], v[116:119], a[172:175], v[0:3]\n"
"v_exp_f32 v13, v13\n"
"v_mfma_f32_16x16x32_bf16 a[0:3], v[120:123], v[16:19], 0\n"
"ds_read_b128 v[104:107], v180 offset:20480\n"
"v_cvt_pk_bf16_f32 v54, v12, v13\n"
"v_mfma_f32_16x16x32_bf16 a[0:3], v[124:127], v[20:23], a[0:3]\n"
"ds_read_b64_tr_b16 v[128:129], v182 offset:0\n"
"v_mfma_f32_16x16x32_bf16 a[32:35], v[120:123], v[24:27], 0\n"
"v_exp_f32 v14, v14\n"
"v_mfma_f32_16x16x32_bf16 a[32:35], v[124:127], v[28:31], a[32:35]\n"
"ds_read_b64_tr_b16 v[130:131], v182 offset:4096\n"
"v_mfma_f32_16x16x32_bf16 v[4:7], v[96:99], a[176:179], v[140:143]\n"
"v_exp_f32 v15, v15\n"
"v_mfma_f32_16x16x32_bf16 v[4:7], v[100:103], a[180:183], v[4:7]\n"
"ds_read_b64_tr_b16 v[132:133], v182 offset:8192\n"
"v_cvt_pk_bf16_f32 v55, v14, v15\n"
"v_mfma_f32_16x16x32_bf16 v[4:7], v[112:115], a[184:187], v[4:7]\n"
"v_exp_f32 v0, v0\n"
"v_mfma_f32_16x16x32_bf16 v[4:7], v[116:119], a[188:191], v[4:7]\n"
"ds_read_b64_tr_b16 v[134:135], v182 offset:12288\n"
"v_mfma_f32_16x16x32_bf16 a[64:67], v[120:123], v[32:35], 0\n"
"v_exp_f32 v1, v1\n"
"v_mfma_f32_16x16x32_bf16 a[64:67], v[124:127], v[36:39], a[64:67]\n"
"v_exp_f32 v2, v2\n"
"v_mfma_f32_16x16x32_bf16 a[96:99], v[120:123], v[40:43], 0\n"
"ds_read_b128 v[108:111], v180 offset:20736\n"
"v_cvt_pk_bf16_f32 v56, v0, v1\n"
"v_mfma_f32_16x16x32_bf16 a[96:99], v[124:127], v[44:47], a[96:99]\n"
"v_exp_f32 v3, v3\n"
"v_mfma_f32_16x16x32_bf16 v[8:11], v[96:99], a[192:195], v[144:147]\n"
"v_exp_f32 v4, v4\n"
"v_mfma_f32_16x16x32_bf16 v[8:11], v[100:103], a[196:199], v[8:11]\n"
"v_exp_f32 v5, v5\n"
"v_mfma_f32_16x16x32_bf16 v[8:11], v[112:115], a[200:203], v[8:11]\n"
"buffer_load_dwordx4 v183, s[40:43], s52 offen offset:2048 lds\n"
"v_cvt_pk_bf16_f32 v57, v2, v3\n"
"v_mfma_f32_16x16x32_bf16 v[8:11], v[116:119], a[204:207], v[8:11]\n"
"v_exp_f32 v6, v6\n"
"v_mfma_f32_16x16x32_bf16 a[128:131], v[120:123], v[48:51], 0\n"
"v_exp_f32 v7, v7\n"
"v_mfma_f32_16x16x32_bf16 a[128:131], v[124:127], v[52:55], a[128:131]\n"
"v_cvt_pk_bf16_f32 v64, v4, v5\n"
"s_waitcnt lgkmcnt(1)\n"
"v_mfma_f32_16x16x32_bf16 a[4:7], v[128:131], v[16:19], 0\n"
"ds_read_b64_tr_b16 v[120:121], v181 offset:256\n"
"v_cvt_pk_bf16_f32 v65, v6, v7\n"
"v_mfma_f32_16x16x32_bf16 a[4:7], v[132:135], v[20:23], a[4:7]\n"
"v_mfma_f32_16x16x32_bf16 v[12:15], v[96:99], a[208:211], v[148:151]\n"
"v_exp_f32 v8, v8\n"
"v_mfma_f32_16x16x32_bf16 v[12:15], v[100:103], a[212:215], v[12:15]\n"
"ds_read_b64_tr_b16 v[122:123], v181 offset:4352\n"
"v_mfma_f32_16x16x32_bf16 v[12:15], v[112:115], a[216:219], v[12:15]\n"
"v_exp_f32 v9, v9\n"
"v_mfma_f32_16x16x32_bf16 v[12:15], v[116:119], a[220:223], v[12:15]\n"
"v_exp_f32 v10, v10\n"
"v_mfma_f32_16x16x32_bf16 a[36:39], v[128:131], v[24:27], 0\n"
"ds_read_b64_tr_b16 v[124:125], v181 offset:8448\n"
"v_cvt_pk_bf16_f32 v72, v8, v9\n"
"v_mfma_f32_16x16x32_bf16 a[36:39], v[132:135], v[28:31], a[36:39]\n"
"v_exp_f32 v11, v11\n"
"v_mfma_f32_16x16x32_bf16 a[68:71], v[128:131], v[32:35], 0\n"
"v_cvt_pk_bf16_f32 v73, v10, v11\n"
"v_mfma_f32_16x16x32_bf16 a[68:71], v[132:135], v[36:39], a[68:71]\n"
"ds_read_b64_tr_b16 v[126:127], v181 offset:12544\n"
"v_mfma_f32_16x16x32_bf16 v[0:3], v[112:115], a[232:235], v[152:155]\n"
"ds_read_b128 v[112:115], v180 offset:20992\n"
"v_mfma_f32_16x16x32_bf16 v[0:3], v[116:119], a[236:239], v[0:3]\n"
"ds_read_b128 v[116:119], v180 offset:21248\n"
"v_mfma_f32_16x16x32_bf16 v[0:3], v[96:99], a[224:227], v[0:3]\n"
"buffer_load_dwordx4 v184, s[40:43], s52 offen offset:3072 lds\n"
"v_mfma_f32_16x16x32_bf16 v[0:3], v[100:103], a[228:231], v[0:3]\n"
"v_exp_f32 v12, v12\n"
"v_mfma_f32_16x16x32_bf16 a[100:103], v[128:131], v[40:43], 0\n"
"v_exp_f32 v13, v13\n"
"v_mfma_f32_16x16x32_bf16 a[100:103], v[132:135], v[44:47], a[100:103]\n"
"v_exp_f32 v14, v14\n"
"v_mfma_f32_16x16x32_bf16 a[132:135], v[128:131], v[48:51], 0\n"
"v_exp_f32 v15, v15\n"
"v_mfma_f32_16x16x32_bf16 a[132:135], v[132:135], v[52:55], a[132:135]\n"
"v_cvt_pk_bf16_f32 v80, v12, v13\n"
"v_cvt_pk_bf16_f32 v81, v14, v15\n"
"v_mfma_f32_16x16x32_bf16 v[156:159], v[176:179], v[16:19], 0\n"
"ds_read_b64_tr_b16 v[128:129], v182 offset:256\n"
"v_mfma_f32_16x16x32_bf16 v[156:159], v[176:179], v[20:23], v[156:159]\n"
"s_waitcnt lgkmcnt(7)\n"
"v_mfma_f32_16x16x32_bf16 v[4:7], v[104:107], a[160:163], v[136:139]\n"
"v_exp_f32 v0, v0\n"
"v_mfma_f32_16x16x32_bf16 v[4:7], v[108:111], a[164:167], v[4:7]\n"
"s_waitcnt lgkmcnt(1)\n"
"v_mfma_f32_16x16x32_bf16 v[4:7], v[112:115], a[168:171], v[4:7]\n"
"ds_read_b64_tr_b16 v[130:131], v182 offset:4352\n"
"v_mfma_f32_16x16x32_bf16 v[4:7], v[116:119], a[172:175], v[4:7]\n"
"v_exp_f32 v1, v1\n"
"v_mfma_f32_16x16x32_bf16 a[8:11], v[120:123], v[16:19], 0\n"
"ds_read_b128 v[96:99], v180 offset:24576\n"
"v_cvt_pk_bf16_f32 v88, v0, v1\n"
"v_mfma_f32_16x16x32_bf16 a[8:11], v[124:127], v[20:23], a[8:11]\n"
"ds_read_b64_tr_b16 v[132:133], v182 offset:8448\n"
"v_mfma_f32_16x16x32_bf16 a[40:43], v[120:123], v[24:27], 0\n"
"v_exp_f32 v2, v2\n"
"v_mfma_f32_16x16x32_bf16 a[40:43], v[124:127], v[28:31], a[40:43]\n"
"ds_read_b64_tr_b16 v[134:135], v182 offset:12544\n"
"v_mfma_f32_16x16x32_bf16 v[8:11], v[104:107], a[176:179], v[140:143]\n"
"s_add_u32 m0, s53, 0x0\n"
"v_mfma_f32_16x16x32_bf16 v[8:11], v[108:111], a[180:183], v[8:11]\n"
"v_exp_f32 v3, v3\n"
"v_mfma_f32_16x16x32_bf16 v[8:11], v[112:115], a[184:187], v[8:11]\n"
"v_exp_f32 v4, v4\n"
"v_mfma_f32_16x16x32_bf16 v[8:11], v[116:119], a[188:191], v[8:11]\n"
"v_exp_f32 v5, v5\n"
"v_mfma_f32_16x16x32_bf16 a[72:75], v[120:123], v[32:35], 0\n"
"v_cvt_pk_bf16_f32 v89, v2, v3\n"
"v_cvt_pk_bf16_f32 v58, v4, v5\n"
"v_mfma_f32_16x16x32_bf16 a[72:75], v[124:127], v[36:39], a[72:75]\n"
"v_exp_f32 v6, v6\n"
"v_mfma_f32_16x16x32_bf16 a[104:107], v[120:123], v[40:43], 0\n"
"ds_read_b128 v[100:103], v180 offset:24832\n"
"v_mfma_f32_16x16x32_bf16 a[104:107], v[124:127], v[44:47], a[104:107]\n"
"v_exp_f32 v7, v7\n"
"v_mfma_f32_16x16x32_bf16 v[12:15], v[104:107], a[192:195], v[144:147]\n"
"v_exp_f32 v8, v8\n"
"v_mfma_f32_16x16x32_bf16 v[12:15], v[108:111], a[196:199], v[12:15]\n"
"v_exp_f32 v9, v9\n"
"v_mfma_f32_16x16x32_bf16 v[12:15], v[112:115], a[200:203], v[12:15]\n"
"v_cvt_pk_bf16_f32 v59, v6, v7\n"
"v_cvt_pk_bf16_f32 v66, v8, v9\n"
"v_mfma_f32_16x16x32_bf16 v[12:15], v[116:119], a[204:207], v[12:15]\n"
"v_exp_f32 v10, v10\n"
"v_mfma_f32_16x16x32_bf16 a[136:139], v[120:123], v[48:51], 0\n"
"v_exp_f32 v11, v11\n"
"v_mfma_f32_16x16x32_bf16 a[136:139], v[124:127], v[52:55], a[136:139]\n"
"v_cvt_pk_bf16_f32 v67, v10, v11\n"
"s_waitcnt lgkmcnt(1)\n"
"v_mfma_f32_16x16x32_bf16 a[12:15], v[128:131], v[16:19], 0\n"
"ds_read_b64_tr_b16 v[120:121], v181 offset:512\n"
"v_mfma_f32_16x16x32_bf16 a[12:15], v[132:135], v[20:23], a[12:15]\n"
"v_mfma_f32_16x16x32_bf16 v[0:3], v[104:107], a[208:211], v[148:151]\n"
"buffer_load_dwordx4 v183, s[36:39], s52 offen offset:0 lds\n"
"v_mfma_f32_16x16x32_bf16 v[0:3], v[108:111], a[212:215], v[0:3]\n"
"ds_read_b64_tr_b16 v[122:123], v181 offset:4608\n"
"v_mfma_f32_16x16x32_bf16 v[0:3], v[112:115], a[216:219], v[0:3]\n"
"v_exp_f32 v12, v12\n"
"v_mfma_f32_16x16x32_bf16 v[0:3], v[116:119], a[220:223], v[0:3]\n"
"v_exp_f32 v13, v13\n"
"v_mfma_f32_16x16x32_bf16 a[44:47], v[128:131], v[24:27], 0\n"
"ds_read_b64_tr_b16 v[124:125], v181 offset:8704\n"
"v_cvt_pk_bf16_f32 v74, v12, v13\n"
"v_mfma_f32_16x16x32_bf16 a[44:47], v[132:135], v[28:31], a[44:47]\n"
"v_exp_f32 v14, v14\n"
"v_mfma_f32_16x16x32_bf16 a[76:79], v[128:131], v[32:35], 0\n"
"v_exp_f32 v15, v15\n"
"v_mfma_f32_16x16x32_bf16 a[76:79], v[132:135], v[36:39], a[76:79]\n"
"ds_read_b64_tr_b16 v[126:127], v181 offset:12800\n"
"v_cvt_pk_bf16_f32 v75, v14, v15\n"
"v_mfma_f32_16x16x32_bf16 v[4:7], v[112:115], a[232:235], v[152:155]\n"
"ds_read_b128 v[112:115], v180 offset:25088\n"
"v_mfma_f32_16x16x32_bf16 v[4:7], v[116:119], a[236:239], v[4:7]\n"
"ds_read_b128 v[116:119], v180 offset:25344\n"
"v_mfma_f32_16x16x32_bf16 v[4:7], v[104:107], a[224:227], v[4:7]\n"
"v_exp_f32 v0, v0\n"
"v_mfma_f32_16x16x32_bf16 v[4:7], v[108:111], a[228:231], v[4:7]\n"
"v_exp_f32 v1, v1\n"
"v_mfma_f32_16x16x32_bf16 a[108:111], v[128:131], v[40:43], 0\n"
"v_exp_f32 v2, v2\n"
"v_mfma_f32_16x16x32_bf16 a[108:111], v[132:135], v[44:47], a[108:111]\n"
"v_exp_f32 v3, v3\n"
"v_mfma_f32_16x16x32_bf16 a[140:143], v[128:131], v[48:51], 0\n"
"v_cvt_pk_bf16_f32 v82, v0, v1\n"
"v_cvt_pk_bf16_f32 v83, v2, v3\n"
"v_mfma_f32_16x16x32_bf16 a[140:143], v[132:135], v[52:55], a[140:143]\n"
"v_mfma_f32_16x16x32_bf16 v[160:163], v[176:179], v[24:27], 0\n"
"ds_read_b64_tr_b16 v[128:129], v182 offset:512\n"
"v_mfma_f32_16x16x32_bf16 v[160:163], v[176:179], v[28:31], v[160:163]\n"
"s_waitcnt lgkmcnt(7)\n"
"v_mfma_f32_16x16x32_bf16 v[8:11], v[96:99], a[160:163], v[136:139]\n"
"buffer_load_dwordx4 v184, s[36:39], s52 offen offset:1024 lds\n"
"v_mfma_f32_16x16x32_bf16 v[8:11], v[100:103], a[164:167], v[8:11]\n"
"s_waitcnt lgkmcnt(1)\n"
"v_mfma_f32_16x16x32_bf16 v[8:11], v[112:115], a[168:171], v[8:11]\n"
"ds_read_b64_tr_b16 v[130:131], v182 offset:4608\n"
"v_mfma_f32_16x16x32_bf16 v[8:11], v[116:119], a[172:175], v[8:11]\n"
"v_exp_f32 v4, v4\n"
"v_mfma_f32_16x16x32_bf16 a[16:19], v[120:123], v[16:19], 0\n"
"ds_read_b128 v[104:107], v180 offset:28672\n"
"v_mfma_f32_16x16x32_bf16 a[16:19], v[124:127], v[20:23], a[16:19]\n"
"ds_read_b64_tr_b16 v[132:133], v182 offset:8704\n"
"v_mfma_f32_16x16x32_bf16 a[48:51], v[120:123], v[24:27], 0\n"
"v_exp_f32 v5, v5\n"
"v_mfma_f32_16x16x32_bf16 a[48:51], v[124:127], v[28:31], a[48:51]\n"
"ds_read_b64_tr_b16 v[134:135], v182 offset:12800\n"
"v_cvt_pk_bf16_f32 v90, v4, v5\n"
"v_mfma_f32_16x16x32_bf16 v[12:15], v[96:99], a[176:179], v[140:143]\n"
"v_exp_f32 v6, v6\n"
"v_mfma_f32_16x16x32_bf16 v[12:15], v[100:103], a[180:183], v[12:15]\n"
"v_exp_f32 v7, v7\n"
"v_mfma_f32_16x16x32_bf16 v[12:15], v[112:115], a[184:187], v[12:15]\n"
"v_exp_f32 v8, v8\n"
"v_mfma_f32_16x16x32_bf16 v[12:15], v[116:119], a[188:191], v[12:15]\n"
"v_exp_f32 v9, v9\n"
"v_mfma_f32_16x16x32_bf16 a[80:83], v[120:123], v[32:35], 0\n"
"v_cvt_pk_bf16_f32 v91, v6, v7\n"
"v_cvt_pk_bf16_f32 v60, v8, v9\n"
"v_mfma_f32_16x16x32_bf16 a[80:83], v[124:127], v[36:39], a[80:83]\n"
"v_exp_f32 v10, v10\n"
"v_mfma_f32_16x16x32_bf16 a[112:115], v[120:123], v[40:43], 0\n"
"ds_read_b128 v[108:111], v180 offset:28928\n"
"v_mfma_f32_16x16x32_bf16 a[112:115], v[124:127], v[44:47], a[112:115]\n"
"buffer_load_dwordx4 v183, s[36:39], s52 offen offset:2048 lds\n"
"v_mfma_f32_16x16x32_bf16 v[0:3], v[96:99], a[192:195], v[144:147]\n"
"v_exp_f32 v11, v11\n"
"v_mfma_f32_16x16x32_bf16 v[0:3], v[100:103], a[196:199], v[0:3]\n"
"v_exp_f32 v12, v12\n"
"v_mfma_f32_16x16x32_bf16 v[0:3], v[112:115], a[200:203], v[0:3]\n"
"v_exp_f32 v13, v13\n"
"v_mfma_f32_16x16x32_bf16 v[0:3], v[116:119], a[204:207], v[0:3]\n"
"v_cvt_pk_bf16_f32 v61, v10, v11\n"
"v_cvt_pk_bf16_f32 v68, v12, v13\n"
"v_mfma_f32_16x16x32_bf16 a[144:147], v[120:123], v[48:51], 0\n"
"v_exp_f32 v14, v14\n"
"v_mfma_f32_16x16x32_bf16 a[144:147], v[124:127], v[52:55], a[144:147]\n"
"s_waitcnt lgkmcnt(1)\n"
"v_mfma_f32_16x16x32_bf16 a[20:23], v[128:131], v[16:19], 0\n"
"ds_read_b64_tr_b16 v[120:121], v181 offset:768\n"
"v_mfma_f32_16x16x32_bf16 a[20:23], v[132:135], v[20:23], a[20:23]\n"
"v_exp_f32 v15, v15\n"
"v_mfma_f32_16x16x32_bf16 v[172:175], v[176:179], v[48:51], 0\n"
"v_exp_f32 v0, v0\n"
"v_mfma_f32_16x16x32_bf16 v[172:175], v[176:179], v[52:55], v[172:175]\n"
"ds_read_b64_tr_b16 v[122:123], v181 offset:4864\n"
"v_cvt_pk_bf16_f32 v69, v14, v15\n"
"v_mfma_f32_16x16x32_bf16 v[4:7], v[96:99], a[208:211], v[148:151]\n"
"v_exp_f32 v1, v1\n"
"v_mfma_f32_16x16x32_bf16 v[4:7], v[100:103], a[212:215], v[4:7]\n"
"v_exp_f32 v2, v2\n"
"v_mfma_f32_16x16x32_bf16 v[4:7], v[112:115], a[216:219], v[4:7]\n"
"ds_read_b64_tr_b16 v[124:125], v181 offset:8960\n"
"v_cvt_pk_bf16_f32 v76, v0, v1\n"
"v_mfma_f32_16x16x32_bf16 v[4:7], v[116:119], a[220:223], v[4:7]\n"
"v_exp_f32 v3, v3\n"
"v_mfma_f32_16x16x32_bf16 a[52:55], v[128:131], v[24:27], 0\n"
"buffer_load_dwordx4 v184, s[36:39], s52 offen offset:3072 lds\n"
"v_cvt_pk_bf16_f32 v77, v2, v3\n"
"v_mfma_f32_16x16x32_bf16 a[52:55], v[132:135], v[28:31], a[52:55]\n"
"ds_read_b64_tr_b16 v[126:127], v181 offset:13056\n"
"v_mfma_f32_16x16x32_bf16 a[84:87], v[128:131], v[32:35], 0\n"
"v_mfma_f32_16x16x32_bf16 a[84:87], v[132:135], v[36:39], a[84:87]\n"
"v_mfma_f32_16x16x32_bf16 v[8:11], v[112:115], a[232:235], v[152:155]\n"
"ds_read_b128 v[112:115], v180 offset:29184\n"
"v_mfma_f32_16x16x32_bf16 v[8:11], v[116:119], a[236:239], v[8:11]\n"
"ds_read_b128 v[116:119], v180 offset:29440\n"
"v_mfma_f32_16x16x32_bf16 v[8:11], v[96:99], a[224:227], v[8:11]\n"
"v_exp_f32 v4, v4\n"
"v_mfma_f32_16x16x32_bf16 v[8:11], v[100:103], a[228:231], v[8:11]\n"
"v_exp_f32 v5, v5\n"
"v_mfma_f32_16x16x32_bf16 a[116:119], v[128:131], v[40:43], 0\n"
"v_exp_f32 v6, v6\n"
"v_mfma_f32_16x16x32_bf16 a[116:119], v[132:135], v[44:47], a[116:119]\n"
"v_exp_f32 v7, v7\n"
"v_mfma_f32_16x16x32_bf16 a[148:151], v[128:131], v[48:51], 0\n"
"v_cvt_pk_bf16_f32 v84, v4, v5\n"
"v_cvt_pk_bf16_f32 v85, v6, v7\n"
"v_mfma_f32_16x16x32_bf16 a[148:151], v[132:135], v[52:55], a[148:151]\n"
"v_mfma_f32_16x16x32_bf16 v[164:167], v[176:179], v[32:35], 0\n"
"ds_read_b64_tr_b16 v[128:129], v182 offset:768\n"
"v_mfma_f32_16x16x32_bf16 v[164:167], v[176:179], v[36:39], v[164:167]\n"
"s_waitcnt lgkmcnt(7)\n"
"v_mfma_f32_16x16x32_bf16 v[12:15], v[104:107], a[160:163], v[136:139]\n"
"v_exp_f32 v8, v8\n"
"v_mfma_f32_16x16x32_bf16 v[12:15], v[108:111], a[164:167], v[12:15]\n"
"s_waitcnt lgkmcnt(1)\n"
"v_mfma_f32_16x16x32_bf16 v[12:15], v[112:115], a[168:171], v[12:15]\n"
"ds_read_b64_tr_b16 v[130:131], v182 offset:4864\n"
"v_mfma_f32_16x16x32_bf16 v[12:15], v[116:119], a[172:175], v[12:15]\n"
"s_add_u32 s52, s52, 0x4000\n"
"v_mfma_f32_16x16x32_bf16 a[24:27], v[120:123], v[16:19], 0\n"
"ds_read_b64_tr_b16 v[132:133], v182 offset:8960\n"
"v_mfma_f32_16x16x32_bf16 a[24:27], v[124:127], v[20:23], a[24:27]\n"
"v_exp_f32 v9, v9\n"
"v_mfma_f32_16x16x32_bf16 a[56:59], v[120:123], v[24:27], 0\n"
"v_exp_f32 v10, v10\n"
"v_mfma_f32_16x16x32_bf16 a[56:59], v[124:127], v[28:31], a[56:59]\n"
"ds_read_b64_tr_b16 v[134:135], v182 offset:13056\n"
"v_cvt_pk_bf16_f32 v92, v8, v9\n"
"v_mfma_f32_16x16x32_bf16 v[0:3], v[104:107], a[176:179], v[140:143]\n"
"v_exp_f32 v11, v11\n"
"v_mfma_f32_16x16x32_bf16 v[0:3], v[108:111], a[180:183], v[0:3]\n"
"v_exp_f32 v12, v12\n"
"v_mfma_f32_16x16x32_bf16 v[0:3], v[112:115], a[184:187], v[0:3]\n"
"v_exp_f32 v13, v13\n"
"v_mfma_f32_16x16x32_bf16 v[0:3], v[116:119], a[188:191], v[0:3]\n"
"v_cvt_pk_bf16_f32 v93, v10, v11\n"
"v_cvt_pk_bf16_f32 v62, v12, v13\n"
"v_mfma_f32_16x16x32_bf16 a[88:91], v[120:123], v[32:35], 0\n"
"v_exp_f32 v14, v14\n"
"v_mfma_f32_16x16x32_bf16 a[88:91], v[124:127], v[36:39], a[88:91]\n"
"v_exp_f32 v15, v15\n"
"v_mfma_f32_16x16x32_bf16 a[120:123], v[120:123], v[40:43], 0\n"
"v_cvt_pk_bf16_f32 v63, v14, v15\n"
"v_mfma_f32_16x16x32_bf16 a[120:123], v[124:127], v[44:47], a[120:123]\n"
"v_mfma_f32_16x16x32_bf16 v[4:7], v[104:107], a[192:195], v[144:147]\n"
"v_exp_f32 v0, v0\n"
"v_mfma_f32_16x16x32_bf16 v[4:7], v[108:111], a[196:199], v[4:7]\n"
"v_exp_f32 v1, v1\n"
"v_mfma_f32_16x16x32_bf16 v[4:7], v[112:115], a[200:203], v[4:7]\n"
"v_exp_f32 v2, v2\n"
"v_mfma_f32_16x16x32_bf16 v[4:7], v[116:119], a[204:207], v[4:7]\n"
"v_exp_f32 v3, v3\n"
"v_mfma_f32_16x16x32_bf16 a[152:155], v[120:123], v[48:51], 0\n"
"v_cvt_pk_bf16_f32 v70, v0, v1\n"
"v_cvt_pk_bf16_f32 v71, v2, v3\n"
"v_mfma_f32_16x16x32_bf16 a[152:155], v[124:127], v[52:55], a[152:155]\n"
"s_waitcnt vmcnt(16) lgkmcnt(0)\n"
"v_xor_b32 v180, 0x8000, v180\n"
"s_barrier\n"
"v_mfma_f32_16x16x32_bf16 a[28:31], v[128:131], v[16:19], 0\n"
"ds_read_b128 v[96:99], v180 offset:0\n"
"v_mfma_f32_16x16x32_bf16 a[28:31], v[132:135], v[20:23], a[28:31]\n"
"ds_read_b128 v[100:103], v180 offset:256\n"
"v_mfma_f32_16x16x32_bf16 v[8:11], v[104:107], a[208:211], v[148:151]\n"
"ds_read_b64_tr_b16 v[120:121], v181 offset:16384\n"
"v_mfma_f32_16x16x32_bf16 v[8:11], v[108:111], a[212:215], v[8:11]\n"
"ds_read_b64_tr_b16 v[122:123], v181 offset:20480\n"
"v_mfma_f32_16x16x32_bf16 v[8:11], v[112:115], a[216:219], v[8:11]\n"
"ds_read_b64_tr_b16 v[124:125], v181 offset:24576\n"
"v_mfma_f32_16x16x32_bf16 v[8:11], v[116:119], a[220:223], v[8:11]\n"
"ds_read_b64_tr_b16 v[126:127], v181 offset:28672\n"
"v_mfma_f32_16x16x32_bf16 a[60:63], v[128:131], v[24:27], 0\n"
"s_add_u32 m0, s54, 0x0\n"
"v_mfma_f32_16x16x32_bf16 a[60:63], v[132:135], v[28:31], a[60:63]\n"
"v_exp_f32 v4, v4\n"
"v_mfma_f32_16x16x32_bf16 a[92:95], v[128:131], v[32:35], 0\n"
"v_exp_f32 v5, v5\n"
"v_mfma_f32_16x16x32_bf16 a[92:95], v[132:135], v[36:39], a[92:95]\n"
"buffer_load_dwordx4 v183, s[40:43], s52 offen offset:0 lds\n"
"v_cvt_pk_bf16_f32 v78, v4, v5\n"
"v_mfma_f32_16x16x32_bf16 v[12:15], v[112:115], a[232:235], v[152:155]\n"
"ds_read_b128 v[112:115], v180 offset:512\n"
"v_mfma_f32_16x16x32_bf16 v[12:15], v[116:119], a[236:239], v[12:15]\n"
"ds_read_b128 v[116:119], v180 offset:768\n"
"v_mfma_f32_16x16x32_bf16 v[12:15], v[104:107], a[224:227], v[12:15]\n"
"v_exp_f32 v6, v6\n"
"v_mfma_f32_16x16x32_bf16 v[12:15], v[108:111], a[228:231], v[12:15]\n"
"v_exp_f32 v7, v7\n"
"v_mfma_f32_16x16x32_bf16 a[124:127], v[128:131], v[40:43], 0\n"
"v_exp_f32 v8, v8\n"
"v_mfma_f32_16x16x32_bf16 a[124:127], v[132:135], v[44:47], a[124:127]\n"
"v_exp_f32 v9, v9\n"
"v_mfma_f32_16x16x32_bf16 a[156:159], v[128:131], v[48:51], 0\n"
"v_cvt_pk_bf16_f32 v79, v6, v7\n"
"v_cvt_pk_bf16_f32 v86, v8, v9\n"
"v_mfma_f32_16x16x32_bf16 a[156:159], v[132:135], v[52:55], a[156:159]\n"
"v_exp_f32 v10, v10\n"
"v_mfma_f32_16x16x32_bf16 v[168:171], v[176:179], v[40:43], 0\n"
"v_exp_f32 v11, v11\n"
"v_mfma_f32_16x16x32_bf16 v[168:171], v[176:179], v[44:47], v[168:171]\n"
"v_cvt_pk_bf16_f32 v87, v10, v11\n"
"s_waitcnt lgkmcnt(2)\n"
"s_mov_b32 s55, 34\n"
"s_mov_b32 s59, s53\n"
"s_xor_b32 s58, s53, 0x8000\n"
".p2align 6\n"
"L_loop_a_%=:\n"
"v_mfma_f32_16x16x32_bf16 v[0:3], v[96:99], a[160:163], v[136:139]\n"
"v_exp_f32 v12, v12\n"
"v_mfma_f32_16x16x32_bf16 v[0:3], v[100:103], a[164:167], v[0:3]\n"
"s_waitcnt lgkmcnt(0)\n"
"v_mfma_f32_16x16x32_bf16 v[0:3], v[112:115], a[168:171], v[0:3]\n"
"buffer_load_dwordx4 v184, s[40:43], s52 offen offset:1024 lds\n"
"v_mfma_f32_16x16x32_bf16 v[0:3], v[116:119], a[172:175], v[0:3]\n"
"v_exp_f32 v13, v13\n"
"v_mfma_f32_16x16x32_bf16 a[0:3], v[120:123], v[56:59], a[0:3]\n"
"ds_read_b128 v[104:107], v180 offset:4096\n"
"v_cvt_pk_bf16_f32 v94, v12, v13\n"
"v_mfma_f32_16x16x32_bf16 a[0:3], v[124:127], v[60:63], a[0:3]\n"
"ds_read_b64_tr_b16 v[128:129], v182 offset:16384\n"
"v_mfma_f32_16x16x32_bf16 a[32:35], v[120:123], v[64:67], a[32:35]\n"
"v_exp_f32 v14, v14\n"
"v_mfma_f32_16x16x32_bf16 a[32:35], v[124:127], v[68:71], a[32:35]\n"
"ds_read_b64_tr_b16 v[130:131], v182 offset:20480\n"
"v_mfma_f32_16x16x32_bf16 v[4:7], v[96:99], a[176:179], v[140:143]\n"
"v_exp_f32 v15, v15\n"
"v_mfma_f32_16x16x32_bf16 v[4:7], v[100:103], a[180:183], v[4:7]\n"
"ds_read_b64_tr_b16 v[132:133], v182 offset:24576\n"
"v_cvt_pk_bf16_f32 v95, v14, v15\n"
"v_mfma_f32_16x16x32_bf16 v[4:7], v[112:115], a[184:187], v[4:7]\n"
"v_exp_f32 v0, v0\n"
"v_mfma_f32_16x16x32_bf16 v[4:7], v[116:119], a[188:191], v[4:7]\n"
"ds_read_b64_tr_b16 v[134:135], v182 offset:28672\n"
"v_mfma_f32_16x16x32_bf16 a[64:67], v[120:123], v[72:75], a[64:67]\n"
"v_exp_f32 v1, v1\n"
"v_mfma_f32_16x16x32_bf16 a[64:67], v[124:127], v[76:79], a[64:67]\n"
"v_exp_f32 v2, v2\n"
"v_mfma_f32_16x16x32_bf16 a[96:99], v[120:123], v[80:83], a[96:99]\n"
"ds_read_b128 v[108:111], v180 offset:4352\n"
"v_cvt_pk_bf16_f32 v16, v0, v1\n"
"v_mfma_f32_16x16x32_bf16 a[96:99], v[124:127], v[84:87], a[96:99]\n"
"v_exp_f32 v3, v3\n"
"v_mfma_f32_16x16x32_bf16 v[8:11], v[96:99], a[192:195], v[144:147]\n"
"v_exp_f32 v4, v4\n"
"v_mfma_f32_16x16x32_bf16 v[8:11], v[100:103], a[196:199], v[8:11]\n"
"v_exp_f32 v5, v5\n"
"v_mfma_f32_16x16x32_bf16 v[8:11], v[112:115], a[200:203], v[8:11]\n"
"buffer_load_dwordx4 v183, s[40:43], s52 offen offset:2048 lds\n"
"v_cvt_pk_bf16_f32 v17, v2, v3\n"
"v_mfma_f32_16x16x32_bf16 v[8:11], v[116:119], a[204:207], v[8:11]\n"
"v_exp_f32 v6, v6\n"
"v_mfma_f32_16x16x32_bf16 a[128:131], v[120:123], v[88:91], a[128:131]\n"
"v_exp_f32 v7, v7\n"
"v_mfma_f32_16x16x32_bf16 a[128:131], v[124:127], v[92:95], a[128:131]\n"
"v_cvt_pk_bf16_f32 v24, v4, v5\n"
"s_waitcnt lgkmcnt(1)\n"
"v_mfma_f32_16x16x32_bf16 a[4:7], v[128:131], v[56:59], a[4:7]\n"
"ds_read_b64_tr_b16 v[120:121], v181 offset:16640\n"
"v_cvt_pk_bf16_f32 v25, v6, v7\n"
"v_mfma_f32_16x16x32_bf16 a[4:7], v[132:135], v[60:63], a[4:7]\n"
"v_mfma_f32_16x16x32_bf16 v[12:15], v[96:99], a[208:211], v[148:151]\n"
"v_exp_f32 v8, v8\n"
"v_mfma_f32_16x16x32_bf16 v[12:15], v[100:103], a[212:215], v[12:15]\n"
"ds_read_b64_tr_b16 v[122:123], v181 offset:20736\n"
"v_mfma_f32_16x16x32_bf16 v[12:15], v[112:115], a[216:219], v[12:15]\n"
"v_exp_f32 v9, v9\n"
"v_mfma_f32_16x16x32_bf16 v[12:15], v[116:119], a[220:223], v[12:15]\n"
"v_exp_f32 v10, v10\n"
"v_mfma_f32_16x16x32_bf16 a[36:39], v[128:131], v[64:67], a[36:39]\n"
"ds_read_b64_tr_b16 v[124:125], v181 offset:24832\n"
"v_cvt_pk_bf16_f32 v32, v8, v9\n"
"v_mfma_f32_16x16x32_bf16 a[36:39], v[132:135], v[68:71], a[36:39]\n"
"v_exp_f32 v11, v11\n"
"v_mfma_f32_16x16x32_bf16 a[68:71], v[128:131], v[72:75], a[68:71]\n"
"v_cvt_pk_bf16_f32 v33, v10, v11\n"
"v_mfma_f32_16x16x32_bf16 a[68:71], v[132:135], v[76:79], a[68:71]\n"
"ds_read_b64_tr_b16 v[126:127], v181 offset:28928\n"
"v_mfma_f32_16x16x32_bf16 v[0:3], v[112:115], a[232:235], v[152:155]\n"
"ds_read_b128 v[112:115], v180 offset:4608\n"
"v_mfma_f32_16x16x32_bf16 v[0:3], v[116:119], a[236:239], v[0:3]\n"
"ds_read_b128 v[116:119], v180 offset:4864\n"
"v_mfma_f32_16x16x32_bf16 v[0:3], v[96:99], a[224:227], v[0:3]\n"
"buffer_load_dwordx4 v184, s[40:43], s52 offen offset:3072 lds\n"
"v_mfma_f32_16x16x32_bf16 v[0:3], v[100:103], a[228:231], v[0:3]\n"
"v_exp_f32 v12, v12\n"
"v_mfma_f32_16x16x32_bf16 a[100:103], v[128:131], v[80:83], a[100:103]\n"
"v_exp_f32 v13, v13\n"
"v_mfma_f32_16x16x32_bf16 a[100:103], v[132:135], v[84:87], a[100:103]\n"
"v_exp_f32 v14, v14\n"
"v_mfma_f32_16x16x32_bf16 a[132:135], v[128:131], v[88:91], a[132:135]\n"
"v_exp_f32 v15, v15\n"
"v_mfma_f32_16x16x32_bf16 a[132:135], v[132:135], v[92:95], a[132:135]\n"
"v_cvt_pk_bf16_f32 v40, v12, v13\n"
"v_cvt_pk_bf16_f32 v41, v14, v15\n"
"v_mfma_f32_16x16x32_bf16 v[156:159], v[176:179], v[56:59], v[156:159]\n"
"ds_read_b64_tr_b16 v[128:129], v182 offset:16640\n"
"v_mfma_f32_16x16x32_bf16 v[156:159], v[176:179], v[60:63], v[156:159]\n"
"s_waitcnt lgkmcnt(7)\n"
"v_mfma_f32_16x16x32_bf16 v[4:7], v[104:107], a[160:163], v[136:139]\n"
"v_exp_f32 v0, v0\n"
"v_mfma_f32_16x16x32_bf16 v[4:7], v[108:111], a[164:167], v[4:7]\n"
"s_waitcnt lgkmcnt(1)\n"
"v_mfma_f32_16x16x32_bf16 v[4:7], v[112:115], a[168:171], v[4:7]\n"
"ds_read_b64_tr_b16 v[130:131], v182 offset:20736\n"
"v_mfma_f32_16x16x32_bf16 v[4:7], v[116:119], a[172:175], v[4:7]\n"
"v_exp_f32 v1, v1\n"
"v_mfma_f32_16x16x32_bf16 a[8:11], v[120:123], v[56:59], a[8:11]\n"
"ds_read_b128 v[96:99], v180 offset:8192\n"
"v_cvt_pk_bf16_f32 v48, v0, v1\n"
"v_mfma_f32_16x16x32_bf16 a[8:11], v[124:127], v[60:63], a[8:11]\n"
"ds_read_b64_tr_b16 v[132:133], v182 offset:24832\n"
"v_mfma_f32_16x16x32_bf16 a[40:43], v[120:123], v[64:67], a[40:43]\n"
"v_exp_f32 v2, v2\n"
"v_mfma_f32_16x16x32_bf16 a[40:43], v[124:127], v[68:71], a[40:43]\n"
"ds_read_b64_tr_b16 v[134:135], v182 offset:28928\n"
"v_mfma_f32_16x16x32_bf16 v[8:11], v[104:107], a[176:179], v[140:143]\n"
"s_add_u32 m0, s59, 0x4000\n"
"v_mfma_f32_16x16x32_bf16 v[8:11], v[108:111], a[180:183], v[8:11]\n"
"v_exp_f32 v3, v3\n"
"v_mfma_f32_16x16x32_bf16 v[8:11], v[112:115], a[184:187], v[8:11]\n"
"v_exp_f32 v4, v4\n"
"v_mfma_f32_16x16x32_bf16 v[8:11], v[116:119], a[188:191], v[8:11]\n"
"v_exp_f32 v5, v5\n"
"v_mfma_f32_16x16x32_bf16 a[72:75], v[120:123], v[72:75], a[72:75]\n"
"v_cvt_pk_bf16_f32 v49, v2, v3\n"
"v_cvt_pk_bf16_f32 v18, v4, v5\n"
"v_mfma_f32_16x16x32_bf16 a[72:75], v[124:127], v[76:79], a[72:75]\n"
"v_exp_f32 v6, v6\n"
"v_mfma_f32_16x16x32_bf16 a[104:107], v[120:123], v[80:83], a[104:107]\n"
"ds_read_b128 v[100:103], v180 offset:8448\n"
"v_mfma_f32_16x16x32_bf16 a[104:107], v[124:127], v[84:87], a[104:107]\n"
"v_exp_f32 v7, v7\n"
"v_mfma_f32_16x16x32_bf16 v[12:15], v[104:107], a[192:195], v[144:147]\n"
"v_exp_f32 v8, v8\n"
"v_mfma_f32_16x16x32_bf16 v[12:15], v[108:111], a[196:199], v[12:15]\n"
"v_exp_f32 v9, v9\n"
"v_mfma_f32_16x16x32_bf16 v[12:15], v[112:115], a[200:203], v[12:15]\n"
"v_cvt_pk_bf16_f32 v19, v6, v7\n"
"v_cvt_pk_bf16_f32 v26, v8, v9\n"
"v_mfma_f32_16x16x32_bf16 v[12:15], v[116:119], a[204:207], v[12:15]\n"
"v_exp_f32 v10, v10\n"
"v_mfma_f32_16x16x32_bf16 a[136:139], v[120:123], v[88:91], a[136:139]\n"
"v_exp_f32 v11, v11\n"
"v_mfma_f32_16x16x32_bf16 a[136:139], v[124:127], v[92:95], a[136:139]\n"
"v_cvt_pk_bf16_f32 v27, v10, v11\n"
"s_waitcnt lgkmcnt(1)\n"
"v_mfma_f32_16x16x32_bf16 a[12:15], v[128:131], v[56:59], a[12:15]\n"
"ds_read_b64_tr_b16 v[120:121], v181 offset:16896\n"
"v_mfma_f32_16x16x32_bf16 a[12:15], v[132:135], v[60:63], a[12:15]\n"
"v_mfma_f32_16x16x32_bf16 v[0:3], v[104:107], a[208:211], v[148:151]\n"
"buffer_load_dwordx4 v183, s[36:39], s52 offen offset:0 lds\n"
"v_mfma_f32_16x16x32_bf16 v[0:3], v[108:111], a[212:215], v[0:3]\n"
"ds_read_b64_tr_b16 v[122:123], v181 offset:20992\n"
"v_mfma_f32_16x16x32_bf16 v[0:3], v[112:115], a[216:219], v[0:3]\n"
"v_exp_f32 v12, v12\n"
"v_mfma_f32_16x16x32_bf16 v[0:3], v[116:119], a[220:223], v[0:3]\n"
"v_exp_f32 v13, v13\n"
"v_mfma_f32_16x16x32_bf16 a[44:47], v[128:131], v[64:67], a[44:47]\n"
"ds_read_b64_tr_b16 v[124:125], v181 offset:25088\n"
"v_cvt_pk_bf16_f32 v34, v12, v13\n"
"v_mfma_f32_16x16x32_bf16 a[44:47], v[132:135], v[68:71], a[44:47]\n"
"v_exp_f32 v14, v14\n"
"v_mfma_f32_16x16x32_bf16 a[76:79], v[128:131], v[72:75], a[76:79]\n"
"v_exp_f32 v15, v15\n"
"v_mfma_f32_16x16x32_bf16 a[76:79], v[132:135], v[76:79], a[76:79]\n"
"ds_read_b64_tr_b16 v[126:127], v181 offset:29184\n"
"v_cvt_pk_bf16_f32 v35, v14, v15\n"
"v_mfma_f32_16x16x32_bf16 v[4:7], v[112:115], a[232:235], v[152:155]\n"
"ds_read_b128 v[112:115], v180 offset:8704\n"
"v_mfma_f32_16x16x32_bf16 v[4:7], v[116:119], a[236:239], v[4:7]\n"
"ds_read_b128 v[116:119], v180 offset:8960\n"
"v_mfma_f32_16x16x32_bf16 v[4:7], v[104:107], a[224:227], v[4:7]\n"
"v_exp_f32 v0, v0\n"
"v_mfma_f32_16x16x32_bf16 v[4:7], v[108:111], a[228:231], v[4:7]\n"
"v_exp_f32 v1, v1\n"
"v_mfma_f32_16x16x32_bf16 a[108:111], v[128:131], v[80:83], a[108:111]\n"
"v_exp_f32 v2, v2\n"
"v_mfma_f32_16x16x32_bf16 a[108:111], v[132:135], v[84:87], a[108:111]\n"
"v_exp_f32 v3, v3\n"
"v_mfma_f32_16x16x32_bf16 a[140:143], v[128:131], v[88:91], a[140:143]\n"
"v_cvt_pk_bf16_f32 v42, v0, v1\n"
"v_cvt_pk_bf16_f32 v43, v2, v3\n"
"v_mfma_f32_16x16x32_bf16 a[140:143], v[132:135], v[92:95], a[140:143]\n"
"v_mfma_f32_16x16x32_bf16 v[160:163], v[176:179], v[64:67], v[160:163]\n"
"ds_read_b64_tr_b16 v[128:129], v182 offset:16896\n"
"v_mfma_f32_16x16x32_bf16 v[160:163], v[176:179], v[68:71], v[160:163]\n"
"s_waitcnt lgkmcnt(7)\n"
"v_mfma_f32_16x16x32_bf16 v[8:11], v[96:99], a[160:163], v[136:139]\n"
"buffer_load_dwordx4 v184, s[36:39], s52 offen offset:1024 lds\n"
"v_mfma_f32_16x16x32_bf16 v[8:11], v[100:103], a[164:167], v[8:11]\n"
"s_waitcnt lgkmcnt(1)\n"
"v_mfma_f32_16x16x32_bf16 v[8:11], v[112:115], a[168:171], v[8:11]\n"
"ds_read_b64_tr_b16 v[130:131], v182 offset:20992\n"
"v_mfma_f32_16x16x32_bf16 v[8:11], v[116:119], a[172:175], v[8:11]\n"
"v_exp_f32 v4, v4\n"
"v_mfma_f32_16x16x32_bf16 a[16:19], v[120:123], v[56:59], a[16:19]\n"
"ds_read_b128 v[104:107], v180 offset:12288\n"
"v_mfma_f32_16x16x32_bf16 a[16:19], v[124:127], v[60:63], a[16:19]\n"
"ds_read_b64_tr_b16 v[132:133], v182 offset:25088\n"
"v_mfma_f32_16x16x32_bf16 a[48:51], v[120:123], v[64:67], a[48:51]\n"
"v_exp_f32 v5, v5\n"
"v_mfma_f32_16x16x32_bf16 a[48:51], v[124:127], v[68:71], a[48:51]\n"
"ds_read_b64_tr_b16 v[134:135], v182 offset:29184\n"
"v_cvt_pk_bf16_f32 v50, v4, v5\n"
"v_mfma_f32_16x16x32_bf16 v[12:15], v[96:99], a[176:179], v[140:143]\n"
"v_exp_f32 v6, v6\n"
"v_mfma_f32_16x16x32_bf16 v[12:15], v[100:103], a[180:183], v[12:15]\n"
"v_exp_f32 v7, v7\n"
"v_mfma_f32_16x16x32_bf16 v[12:15], v[112:115], a[184:187], v[12:15]\n"
"v_exp_f32 v8, v8\n"
"v_mfma_f32_16x16x32_bf16 v[12:15], v[116:119], a[188:191], v[12:15]\n"
"v_exp_f32 v9, v9\n"
"v_mfma_f32_16x16x32_bf16 a[80:83], v[120:123], v[72:75], a[80:83]\n"
"v_cvt_pk_bf16_f32 v51, v6, v7\n"
"v_cvt_pk_bf16_f32 v20, v8, v9\n"
"v_mfma_f32_16x16x32_bf16 a[80:83], v[124:127], v[76:79], a[80:83]\n"
"v_exp_f32 v10, v10\n"
"v_mfma_f32_16x16x32_bf16 a[112:115], v[120:123], v[80:83], a[112:115]\n"
"ds_read_b128 v[108:111], v180 offset:12544\n"
"v_mfma_f32_16x16x32_bf16 a[112:115], v[124:127], v[84:87], a[112:115]\n"
"buffer_load_dwordx4 v183, s[36:39], s52 offen offset:2048 lds\n"
"v_mfma_f32_16x16x32_bf16 v[0:3], v[96:99], a[192:195], v[144:147]\n"
"v_exp_f32 v11, v11\n"
"v_mfma_f32_16x16x32_bf16 v[0:3], v[100:103], a[196:199], v[0:3]\n"
"v_exp_f32 v12, v12\n"
"v_mfma_f32_16x16x32_bf16 v[0:3], v[112:115], a[200:203], v[0:3]\n"
"v_exp_f32 v13, v13\n"
"v_mfma_f32_16x16x32_bf16 v[0:3], v[116:119], a[204:207], v[0:3]\n"
"v_cvt_pk_bf16_f32 v21, v10, v11\n"
"v_cvt_pk_bf16_f32 v28, v12, v13\n"
"v_mfma_f32_16x16x32_bf16 a[144:147], v[120:123], v[88:91], a[144:147]\n"
"v_exp_f32 v14, v14\n"
"v_mfma_f32_16x16x32_bf16 a[144:147], v[124:127], v[92:95], a[144:147]\n"
"s_waitcnt lgkmcnt(1)\n"
"v_mfma_f32_16x16x32_bf16 a[20:23], v[128:131], v[56:59], a[20:23]\n"
"ds_read_b64_tr_b16 v[120:121], v181 offset:17152\n"
"v_mfma_f32_16x16x32_bf16 a[20:23], v[132:135], v[60:63], a[20:23]\n"
"v_exp_f32 v15, v15\n"
"v_mfma_f32_16x16x32_bf16 v[172:175], v[176:179], v[88:91], v[172:175]\n"
"v_exp_f32 v0, v0\n"
"v_mfma_f32_16x16x32_bf16 v[172:175], v[176:179], v[92:95], v[172:175]\n"
"ds_read_b64_tr_b16 v[122:123], v181 offset:21248\n"
"v_cvt_pk_bf16_f32 v29, v14, v15\n"
"v_mfma_f32_16x16x32_bf16 v[4:7], v[96:99], a[208:211], v[148:151]\n"
"v_exp_f32 v1, v1\n"
"v_mfma_f32_16x16x32_bf16 v[4:7], v[100:103], a[212:215], v[4:7]\n"
"v_exp_f32 v2, v2\n"
"v_mfma_f32_16x16x32_bf16 v[4:7], v[112:115], a[216:219], v[4:7]\n"
"ds_read_b64_tr_b16 v[124:125], v181 offset:25344\n"
"v_cvt_pk_bf16_f32 v36, v0, v1\n"
"v_mfma_f32_16x16x32_bf16 v[4:7], v[116:119], a[220:223], v[4:7]\n"
"v_exp_f32 v3, v3\n"
"v_mfma_f32_16x16x32_bf16 a[52:55], v[128:131], v[64:67], a[52:55]\n"
"buffer_load_dwordx4 v184, s[36:39], s52 offen offset:3072 lds\n"
"v_cvt_pk_bf16_f32 v37, v2, v3\n"
"v_mfma_f32_16x16x32_bf16 a[52:55], v[132:135], v[68:71], a[52:55]\n"
"ds_read_b64_tr_b16 v[126:127], v181 offset:29440\n"
"v_mfma_f32_16x16x32_bf16 a[84:87], v[128:131], v[72:75], a[84:87]\n"
"v_mfma_f32_16x16x32_bf16 a[84:87], v[132:135], v[76:79], a[84:87]\n"
"v_mfma_f32_16x16x32_bf16 v[8:11], v[112:115], a[232:235], v[152:155]\n"
"ds_read_b128 v[112:115], v180 offset:12800\n"
"v_mfma_f32_16x16x32_bf16 v[8:11], v[116:119], a[236:239], v[8:11]\n"
"ds_read_b128 v[116:119], v180 offset:13056\n"
"v_mfma_f32_16x16x32_bf16 v[8:11], v[96:99], a[224:227], v[8:11]\n"
"v_exp_f32 v4, v4\n"
"v_mfma_f32_16x16x32_bf16 v[8:11], v[100:103], a[228:231], v[8:11]\n"
"v_exp_f32 v5, v5\n"
"v_mfma_f32_16x16x32_bf16 a[116:119], v[128:131], v[80:83], a[116:119]\n"
"v_exp_f32 v6, v6\n"
"v_mfma_f32_16x16x32_bf16 a[116:119], v[132:135], v[84:87], a[116:119]\n"
"v_exp_f32 v7, v7\n"
"v_mfma_f32_16x16x32_bf16 a[148:151], v[128:131], v[88:91], a[148:151]\n"
"v_cvt_pk_bf16_f32 v44, v4, v5\n"
"v_cvt_pk_bf16_f32 v45, v6, v7\n"
"v_mfma_f32_16x16x32_bf16 a[148:151], v[132:135], v[92:95], a[148:151]\n"
"v_mfma_f32_16x16x32_bf16 v[164:167], v[176:179], v[72:75], v[164:167]\n"
"ds_read_b64_tr_b16 v[128:129], v182 offset:17152\n"
"v_mfma_f32_16x16x32_bf16 v[164:167], v[176:179], v[76:79], v[164:167]\n"
"s_waitcnt lgkmcnt(7)\n"
"v_mfma_f32_16x16x32_bf16 v[12:15], v[104:107], a[160:163], v[136:139]\n"
"v_exp_f32 v8, v8\n"
"v_mfma_f32_16x16x32_bf16 v[12:15], v[108:111], a[164:167], v[12:15]\n"
"s_waitcnt lgkmcnt(1)\n"
"v_mfma_f32_16x16x32_bf16 v[12:15], v[112:115], a[168:171], v[12:15]\n"
"ds_read_b64_tr_b16 v[130:131], v182 offset:21248\n"
"v_mfma_f32_16x16x32_bf16 v[12:15], v[116:119], a[172:175], v[12:15]\n"
"s_add_u32 s52, s52, 0x4000\n"
"v_mfma_f32_16x16x32_bf16 a[24:27], v[120:123], v[56:59], a[24:27]\n"
"ds_read_b64_tr_b16 v[132:133], v182 offset:25344\n"
"v_mfma_f32_16x16x32_bf16 a[24:27], v[124:127], v[60:63], a[24:27]\n"
"v_exp_f32 v9, v9\n"
"v_mfma_f32_16x16x32_bf16 a[56:59], v[120:123], v[64:67], a[56:59]\n"
"v_exp_f32 v10, v10\n"
"v_mfma_f32_16x16x32_bf16 a[56:59], v[124:127], v[68:71], a[56:59]\n"
"ds_read_b64_tr_b16 v[134:135], v182 offset:29440\n"
"v_cvt_pk_bf16_f32 v52, v8, v9\n"
"v_mfma_f32_16x16x32_bf16 v[0:3], v[104:107], a[176:179], v[140:143]\n"
"v_exp_f32 v11, v11\n"
"v_mfma_f32_16x16x32_bf16 v[0:3], v[108:111], a[180:183], v[0:3]\n"
"v_exp_f32 v12, v12\n"
"v_mfma_f32_16x16x32_bf16 v[0:3], v[112:115], a[184:187], v[0:3]\n"
"v_exp_f32 v13, v13\n"
"v_mfma_f32_16x16x32_bf16 v[0:3], v[116:119], a[188:191], v[0:3]\n"
"v_cvt_pk_bf16_f32 v53, v10, v11\n"
"v_cvt_pk_bf16_f32 v22, v12, v13\n"
"v_mfma_f32_16x16x32_bf16 a[88:91], v[120:123], v[72:75], a[88:91]\n"
"v_exp_f32 v14, v14\n"
"v_mfma_f32_16x16x32_bf16 a[88:91], v[124:127], v[76:79], a[88:91]\n"
"v_exp_f32 v15, v15\n"
"v_mfma_f32_16x16x32_bf16 a[120:123], v[120:123], v[80:83], a[120:123]\n"
"v_cvt_pk_bf16_f32 v23, v14, v15\n"
"v_mfma_f32_16x16x32_bf16 a[120:123], v[124:127], v[84:87], a[120:123]\n"
"v_mfma_f32_16x16x32_bf16 v[4:7], v[104:107], a[192:195], v[144:147]\n"
"v_exp_f32 v0, v0\n"
"v_mfma_f32_16x16x32_bf16 v[4:7], v[108:111], a[196:199], v[4:7]\n"
"v_exp_f32 v1, v1\n"
"v_mfma_f32_16x16x32_bf16 v[4:7], v[112:115], a[200:203], v[4:7]\n"
"v_exp_f32 v2, v2\n"
"v_mfma_f32_16x16x32_bf16 v[4:7], v[116:119], a[204:207], v[4:7]\n"
"v_exp_f32 v3, v3\n"
"v_mfma_f32_16x16x32_bf16 a[152:155], v[120:123], v[88:91], a[152:155]\n"
"v_cvt_pk_bf16_f32 v30, v0, v1\n"
"v_cvt_pk_bf16_f32 v31, v2, v3\n"
"v_mfma_f32_16x16x32_bf16 a[152:155], v[124:127], v[92:95], a[152:155]\n"
"s_waitcnt vmcnt(16) lgkmcnt(0)\n"
"v_xor_b32 v181, 0x8000, v181\n"
"v_xor_b32 v182, 0x8000, v182\n"
"s_barrier\n"
"v_mfma_f32_16x16x32_bf16 a[28:31], v[128:131], v[56:59], a[28:31]\n"
"ds_read_b128 v[96:99], v180 offset:16384\n"
"v_mfma_f32_16x16x32_bf16 a[28:31], v[132:135], v[60:63], a[28:31]\n"
"ds_read_b128 v[100:103], v180 offset:16640\n"
"v_mfma_f32_16x16x32_bf16 v[8:11], v[104:107], a[208:211], v[148:151]\n"
"ds_read_b64_tr_b16 v[120:121], v181 offset:0\n"
"v_mfma_f32_16x16x32_bf16 v[8:11], v[108:111], a[212:215], v[8:11]\n"
"ds_read_b64_tr_b16 v[122:123], v181 offset:4096\n"
"v_mfma_f32_16x16x32_bf16 v[8:11], v[112:115], a[216:219], v[8:11]\n"
"ds_read_b64_tr_b16 v[124:125], v181 offset:8192\n"
"v_mfma_f32_16x16x32_bf16 v[8:11], v[116:119], a[220:223], v[8:11]\n"
"ds_read_b64_tr_b16 v[126:127], v181 offset:12288\n"
"v_mfma_f32_16x16x32_bf16 a[60:63], v[128:131], v[64:67], a[60:63]\n"
"s_add_u32 m0, s59, 0x14000\n"
"v_mfma_f32_16x16x32_bf16 a[60:63], v[132:135], v[68:71], a[60:63]\n"
"v_exp_f32 v4, v4\n"
"v_mfma_f32_16x16x32_bf16 a[92:95], v[128:131], v[72:75], a[92:95]\n"
"v_exp_f32 v5, v5\n"
"v_mfma_f32_16x16x32_bf16 a[92:95], v[132:135], v[76:79], a[92:95]\n"
"buffer_load_dwordx4 v183, s[40:43], s52 offen offset:0 lds\n"
"v_cvt_pk_bf16_f32 v38, v4, v5\n"
"v_mfma_f32_16x16x32_bf16 v[12:15], v[112:115], a[232:235], v[152:155]\n"
"ds_read_b128 v[112:115], v180 offset:16896\n"
"v_mfma_f32_16x16x32_bf16 v[12:15], v[116:119], a[236:239], v[12:15]\n"
"ds_read_b128 v[116:119], v180 offset:17152\n"
"v_mfma_f32_16x16x32_bf16 v[12:15], v[104:107], a[224:227], v[12:15]\n"
"v_exp_f32 v6, v6\n"
"v_mfma_f32_16x16x32_bf16 v[12:15], v[108:111], a[228:231], v[12:15]\n"
"v_exp_f32 v7, v7\n"
"v_mfma_f32_16x16x32_bf16 a[124:127], v[128:131], v[80:83], a[124:127]\n"
"v_exp_f32 v8, v8\n"
"v_mfma_f32_16x16x32_bf16 a[124:127], v[132:135], v[84:87], a[124:127]\n"
"v_exp_f32 v9, v9\n"
"v_mfma_f32_16x16x32_bf16 a[156:159], v[128:131], v[88:91], a[156:159]\n"
"v_cvt_pk_bf16_f32 v39, v6, v7\n"
"v_cvt_pk_bf16_f32 v46, v8, v9\n"
"v_mfma_f32_16x16x32_bf16 a[156:159], v[132:135], v[92:95], a[156:159]\n"
"v_exp_f32 v10, v10\n"
"v_mfma_f32_16x16x32_bf16 v[168:171], v[176:179], v[80:83], v[168:171]\n"
"v_exp_f32 v11, v11\n"
"v_mfma_f32_16x16x32_bf16 v[168:171], v[176:179], v[84:87], v[168:171]\n"
"v_cvt_pk_bf16_f32 v47, v10, v11\n"
"s_waitcnt lgkmcnt(2)\n"
"v_mfma_f32_16x16x32_bf16 v[0:3], v[96:99], a[160:163], v[136:139]\n"
"v_exp_f32 v12, v12\n"
"v_mfma_f32_16x16x32_bf16 v[0:3], v[100:103], a[164:167], v[0:3]\n"
"s_waitcnt lgkmcnt(0)\n"
"v_mfma_f32_16x16x32_bf16 v[0:3], v[112:115], a[168:171], v[0:3]\n"
"buffer_load_dwordx4 v184, s[40:43], s52 offen offset:1024 lds\n"
"v_mfma_f32_16x16x32_bf16 v[0:3], v[116:119], a[172:175], v[0:3]\n"
"v_exp_f32 v13, v13\n"
"v_mfma_f32_16x16x32_bf16 a[0:3], v[120:123], v[16:19], a[0:3]\n"
"ds_read_b128 v[104:107], v180 offset:20480\n"
"v_cvt_pk_bf16_f32 v54, v12, v13\n"
"v_mfma_f32_16x16x32_bf16 a[0:3], v[124:127], v[20:23], a[0:3]\n"
"ds_read_b64_tr_b16 v[128:129], v182 offset:0\n"
"v_mfma_f32_16x16x32_bf16 a[32:35], v[120:123], v[24:27], a[32:35]\n"
"v_exp_f32 v14, v14\n"
"v_mfma_f32_16x16x32_bf16 a[32:35], v[124:127], v[28:31], a[32:35]\n"
"ds_read_b64_tr_b16 v[130:131], v182 offset:4096\n"
"v_mfma_f32_16x16x32_bf16 v[4:7], v[96:99], a[176:179], v[140:143]\n"
"v_exp_f32 v15, v15\n"
"v_mfma_f32_16x16x32_bf16 v[4:7], v[100:103], a[180:183], v[4:7]\n"
"ds_read_b64_tr_b16 v[132:133], v182 offset:8192\n"
"v_cvt_pk_bf16_f32 v55, v14, v15\n"
"v_mfma_f32_16x16x32_bf16 v[4:7], v[112:115], a[184:187], v[4:7]\n"
"v_exp_f32 v0, v0\n"
"v_mfma_f32_16x16x32_bf16 v[4:7], v[116:119], a[188:191], v[4:7]\n"
"ds_read_b64_tr_b16 v[134:135], v182 offset:12288\n"
"v_mfma_f32_16x16x32_bf16 a[64:67], v[120:123], v[32:35], a[64:67]\n"
"v_exp_f32 v1, v1\n"
"v_mfma_f32_16x16x32_bf16 a[64:67], v[124:127], v[36:39], a[64:67]\n"
"v_exp_f32 v2, v2\n"
"v_mfma_f32_16x16x32_bf16 a[96:99], v[120:123], v[40:43], a[96:99]\n"
"ds_read_b128 v[108:111], v180 offset:20736\n"
"v_cvt_pk_bf16_f32 v56, v0, v1\n"
"v_mfma_f32_16x16x32_bf16 a[96:99], v[124:127], v[44:47], a[96:99]\n"
"v_exp_f32 v3, v3\n"
"v_mfma_f32_16x16x32_bf16 v[8:11], v[96:99], a[192:195], v[144:147]\n"
"v_exp_f32 v4, v4\n"
"v_mfma_f32_16x16x32_bf16 v[8:11], v[100:103], a[196:199], v[8:11]\n"
"v_exp_f32 v5, v5\n"
"v_mfma_f32_16x16x32_bf16 v[8:11], v[112:115], a[200:203], v[8:11]\n"
"buffer_load_dwordx4 v183, s[40:43], s52 offen offset:2048 lds\n"
"v_cvt_pk_bf16_f32 v57, v2, v3\n"
"v_mfma_f32_16x16x32_bf16 v[8:11], v[116:119], a[204:207], v[8:11]\n"
"v_exp_f32 v6, v6\n"
"v_mfma_f32_16x16x32_bf16 a[128:131], v[120:123], v[48:51], a[128:131]\n"
"v_exp_f32 v7, v7\n"
"v_mfma_f32_16x16x32_bf16 a[128:131], v[124:127], v[52:55], a[128:131]\n"
"v_cvt_pk_bf16_f32 v64, v4, v5\n"
"s_waitcnt lgkmcnt(1)\n"
"v_mfma_f32_16x16x32_bf16 a[4:7], v[128:131], v[16:19], a[4:7]\n"
"ds_read_b64_tr_b16 v[120:121], v181 offset:256\n"
"v_cvt_pk_bf16_f32 v65, v6, v7\n"
"v_mfma_f32_16x16x32_bf16 a[4:7], v[132:135], v[20:23], a[4:7]\n"
"v_mfma_f32_16x16x32_bf16 v[12:15], v[96:99], a[208:211], v[148:151]\n"
"v_exp_f32 v8, v8\n"
"v_mfma_f32_16x16x32_bf16 v[12:15], v[100:103], a[212:215], v[12:15]\n"
"ds_read_b64_tr_b16 v[122:123], v181 offset:4352\n"
"v_mfma_f32_16x16x32_bf16 v[12:15], v[112:115], a[216:219], v[12:15]\n"
"v_exp_f32 v9, v9\n"
"v_mfma_f32_16x16x32_bf16 v[12:15], v[116:119], a[220:223], v[12:15]\n"
"v_exp_f32 v10, v10\n"
"v_mfma_f32_16x16x32_bf16 a[36:39], v[128:131], v[24:27], a[36:39]\n"
"ds_read_b64_tr_b16 v[124:125], v181 offset:8448\n"
"v_cvt_pk_bf16_f32 v72, v8, v9\n"
"v_mfma_f32_16x16x32_bf16 a[36:39], v[132:135], v[28:31], a[36:39]\n"
"v_exp_f32 v11, v11\n"
"v_mfma_f32_16x16x32_bf16 a[68:71], v[128:131], v[32:35], a[68:71]\n"
"v_cvt_pk_bf16_f32 v73, v10, v11\n"
"v_mfma_f32_16x16x32_bf16 a[68:71], v[132:135], v[36:39], a[68:71]\n"
"ds_read_b64_tr_b16 v[126:127], v181 offset:12544\n"
"v_mfma_f32_16x16x32_bf16 v[0:3], v[112:115], a[232:235], v[152:155]\n"
"ds_read_b128 v[112:115], v180 offset:20992\n"
"v_mfma_f32_16x16x32_bf16 v[0:3], v[116:119], a[236:239], v[0:3]\n"
"ds_read_b128 v[116:119], v180 offset:21248\n"
"v_mfma_f32_16x16x32_bf16 v[0:3], v[96:99], a[224:227], v[0:3]\n"
"buffer_load_dwordx4 v184, s[40:43], s52 offen offset:3072 lds\n"
"v_mfma_f32_16x16x32_bf16 v[0:3], v[100:103], a[228:231], v[0:3]\n"
"v_exp_f32 v12, v12\n"
"v_mfma_f32_16x16x32_bf16 a[100:103], v[128:131], v[40:43], a[100:103]\n"
"v_exp_f32 v13, v13\n"
"v_mfma_f32_16x16x32_bf16 a[100:103], v[132:135], v[44:47], a[100:103]\n"
"v_exp_f32 v14, v14\n"
"v_mfma_f32_16x16x32_bf16 a[132:135], v[128:131], v[48:51], a[132:135]\n"
"v_exp_f32 v15, v15\n"
"v_mfma_f32_16x16x32_bf16 a[132:135], v[132:135], v[52:55], a[132:135]\n"
"v_cvt_pk_bf16_f32 v80, v12, v13\n"
"v_cvt_pk_bf16_f32 v81, v14, v15\n"
"v_mfma_f32_16x16x32_bf16 v[156:159], v[176:179], v[16:19], v[156:159]\n"
"ds_read_b64_tr_b16 v[128:129], v182 offset:256\n"
"v_mfma_f32_16x16x32_bf16 v[156:159], v[176:179], v[20:23], v[156:159]\n"
"s_waitcnt lgkmcnt(7)\n"
"v_mfma_f32_16x16x32_bf16 v[4:7], v[104:107], a[160:163], v[136:139]\n"
"v_exp_f32 v0, v0\n"
"v_mfma_f32_16x16x32_bf16 v[4:7], v[108:111], a[164:167], v[4:7]\n"
"s_waitcnt lgkmcnt(1)\n"
"v_mfma_f32_16x16x32_bf16 v[4:7], v[112:115], a[168:171], v[4:7]\n"
"ds_read_b64_tr_b16 v[130:131], v182 offset:4352\n"
"v_mfma_f32_16x16x32_bf16 v[4:7], v[116:119], a[172:175], v[4:7]\n"
"v_exp_f32 v1, v1\n"
"v_mfma_f32_16x16x32_bf16 a[8:11], v[120:123], v[16:19], a[8:11]\n"
"ds_read_b128 v[96:99], v180 offset:24576\n"
"v_cvt_pk_bf16_f32 v88, v0, v1\n"
"v_mfma_f32_16x16x32_bf16 a[8:11], v[124:127], v[20:23], a[8:11]\n"
"ds_read_b64_tr_b16 v[132:133], v182 offset:8448\n"
"v_mfma_f32_16x16x32_bf16 a[40:43], v[120:123], v[24:27], a[40:43]\n"
"v_exp_f32 v2, v2\n"
"v_mfma_f32_16x16x32_bf16 a[40:43], v[124:127], v[28:31], a[40:43]\n"
"ds_read_b64_tr_b16 v[134:135], v182 offset:12544\n"
"v_mfma_f32_16x16x32_bf16 v[8:11], v[104:107], a[176:179], v[140:143]\n"
"s_add_u32 m0, s58, 0x0\n"
"v_mfma_f32_16x16x32_bf16 v[8:11], v[108:111], a[180:183], v[8:11]\n"
"v_exp_f32 v3, v3\n"
"v_mfma_f32_16x16x32_bf16 v[8:11], v[112:115], a[184:187], v[8:11]\n"
"v_exp_f32 v4, v4\n"
"v_mfma_f32_16x16x32_bf16 v[8:11], v[116:119], a[188:191], v[8:11]\n"
"v_exp_f32 v5, v5\n"
"v_mfma_f32_16x16x32_bf16 a[72:75], v[120:123], v[32:35], a[72:75]\n"
"v_cvt_pk_bf16_f32 v89, v2, v3\n"
"v_cvt_pk_bf16_f32 v58, v4, v5\n"
"v_mfma_f32_16x16x32_bf16 a[72:75], v[124:127], v[36:39], a[72:75]\n"
"v_exp_f32 v6, v6\n"
"v_mfma_f32_16x16x32_bf16 a[104:107], v[120:123], v[40:43], a[104:107]\n"
"ds_read_b128 v[100:103], v180 offset:24832\n"
"v_mfma_f32_16x16x32_bf16 a[104:107], v[124:127], v[44:47], a[104:107]\n"
"v_exp_f32 v7, v7\n"
"v_mfma_f32_16x16x32_bf16 v[12:15], v[104:107], a[192:195], v[144:147]\n"
"v_exp_f32 v8, v8\n"
"v_mfma_f32_16x16x32_bf16 v[12:15], v[108:111], a[196:199], v[12:15]\n"
"v_exp_f32 v9, v9\n"
"v_mfma_f32_16x16x32_bf16 v[12:15], v[112:115], a[200:203], v[12:15]\n"
"v_cvt_pk_bf16_f32 v59, v6, v7\n"
"v_cvt_pk_bf16_f32 v66, v8, v9\n"
"v_mfma_f32_16x16x32_bf16 v[12:15], v[116:119], a[204:207], v[12:15]\n"
"v_exp_f32 v10, v10\n"
"v_mfma_f32_16x16x32_bf16 a[136:139], v[120:123], v[48:51], a[136:139]\n"
"v_exp_f32 v11, v11\n"
"v_mfma_f32_16x16x32_bf16 a[136:139], v[124:127], v[52:55], a[136:139]\n"
"v_cvt_pk_bf16_f32 v67, v10, v11\n"
"s_waitcnt lgkmcnt(1)\n"
"v_mfma_f32_16x16x32_bf16 a[12:15], v[128:131], v[16:19], a[12:15]\n"
"ds_read_b64_tr_b16 v[120:121], v181 offset:512\n"
"v_mfma_f32_16x16x32_bf16 a[12:15], v[132:135], v[20:23], a[12:15]\n"
"v_mfma_f32_16x16x32_bf16 v[0:3], v[104:107], a[208:211], v[148:151]\n"
"buffer_load_dwordx4 v183, s[36:39], s52 offen offset:0 lds\n"
"v_mfma_f32_16x16x32_bf16 v[0:3], v[108:111], a[212:215], v[0:3]\n"
"ds_read_b64_tr_b16 v[122:123], v181 offset:4608\n"
"v_mfma_f32_16x16x32_bf16 v[0:3], v[112:115], a[216:219], v[0:3]\n"
"v_exp_f32 v12, v12\n"
"v_mfma_f32_16x16x32_bf16 v[0:3], v[116:119], a[220:223], v[0:3]\n"
"v_exp_f32 v13, v13\n"
"v_mfma_f32_16x16x32_bf16 a[44:47], v[128:131], v[24:27], a[44:47]\n"
"ds_read_b64_tr_b16 v[124:125], v181 offset:8704\n"
"v_cvt_pk_bf16_f32 v74, v12, v13\n"
"v_mfma_f32_16x16x32_bf16 a[44:47], v[132:135], v[28:31], a[44:47]\n"
"v_exp_f32 v14, v14\n"
"v_mfma_f32_16x16x32_bf16 a[76:79], v[128:131], v[32:35], a[76:79]\n"
"v_exp_f32 v15, v15\n"
"v_mfma_f32_16x16x32_bf16 a[76:79], v[132:135], v[36:39], a[76:79]\n"
"ds_read_b64_tr_b16 v[126:127], v181 offset:12800\n"
"v_cvt_pk_bf16_f32 v75, v14, v15\n"
"v_mfma_f32_16x16x32_bf16 v[4:7], v[112:115], a[232:235], v[152:155]\n"
"ds_read_b128 v[112:115], v180 offset:25088\n"
"v_mfma_f32_16x16x32_bf16 v[4:7], v[116:119], a[236:239], v[4:7]\n"
"ds_read_b128 v[116:119], v180 offset:25344\n"
"v_mfma_f32_16x16x32_bf16 v[4:7], v[104:107], a[224:227], v[4:7]\n"
"v_exp_f32 v0, v0\n"
"v_mfma_f32_16x16x32_bf16 v[4:7], v[108:111], a[228:231], v[4:7]\n"
"v_exp_f32 v1, v1\n"
"v_mfma_f32_16x16x32_bf16 a[108:111], v[128:131], v[40:43], a[108:111]\n"
"v_exp_f32 v2, v2\n"
"v_mfma_f32_16x16x32_bf16 a[108:111], v[132:135], v[44:47], a[108:111]\n"
"v_exp_f32 v3, v3\n"
"v_mfma_f32_16x16x32_bf16 a[140:143], v[128:131], v[48:51], a[140:143]\n"
"v_cvt_pk_bf16_f32 v82, v0, v1\n"
"v_cvt_pk_bf16_f32 v83, v2, v3\n"
"v_mfma_f32_16x16x32_bf16 a[140:143], v[132:135], v[52:55], a[140:143]\n"
"v_mfma_f32_16x16x32_bf16 v[160:163], v[176:179], v[24:27], v[160:163]\n"
"ds_read_b64_tr_b16 v[128:129], v182 offset:512\n"
"v_mfma_f32_16x16x32_bf16 v[160:163], v[176:179], v[28:31], v[160:163]\n"
"s_waitcnt lgkmcnt(7)\n"
"v_mfma_f32_16x16x32_bf16 v[8:11], v[96:99], a[160:163], v[136:139]\n"
"buffer_load_dwordx4 v184, s[36:39], s52 offen offset:1024 lds\n"
"v_mfma_f32_16x16x32_bf16 v[8:11], v[100:103], a[164:167], v[8:11]\n"
"s_waitcnt lgkmcnt(1)\n"
"v_mfma_f32_16x16x32_bf16 v[8:11], v[112:115], a[168:171], v[8:11]\n"
"ds_read_b64_tr_b16 v[130:131], v182 offset:4608\n"
"v_mfma_f32_16x16x32_bf16 v[8:11], v[116:119], a[172:175], v[8:11]\n"
"v_exp_f32 v4, v4\n"
"v_mfma_f32_16x16x32_bf16 a[16:19], v[120:123], v[16:19], a[16:19]\n"
"ds_read_b128 v[104:107], v180 offset:28672\n"
"v_mfma_f32_16x16x32_bf16 a[16:19], v[124:127], v[20:23], a[16:19]\n"
"ds_read_b64_tr_b16 v[132:133], v182 offset:8704\n"
"v_mfma_f32_16x16x32_bf16 a[48:51], v[120:123], v[24:27], a[48:51]\n"
"v_exp_f32 v5, v5\n"
"v_mfma_f32_16x16x32_bf16 a[48:51], v[124:127], v[28:31], a[48:51]\n"
"ds_read_b64_tr_b16 v[134:135], v182 offset:12800\n"
"v_cvt_pk_bf16_f32 v90, v4, v5\n"
"v_mfma_f32_16x16x32_bf16 v[12:15], v[96:99], a[176:179], v[140:143]\n"
"v_exp_f32 v6, v6\n"
"v_mfma_f32_16x16x32_bf16 v[12:15], v[100:103], a[180:183], v[12:15]\n"
"v_exp_f32 v7, v7\n"
"v_mfma_f32_16x16x32_bf16 v[12:15], v[112:115], a[184:187], v[12:15]\n"
"v_exp_f32 v8, v8\n"
"v_mfma_f32_16x16x32_bf16 v[12:15], v[116:119], a[188:191], v[12:15]\n"
"v_exp_f32 v9, v9\n"
"v_mfma_f32_16x16x32_bf16 a[80:83], v[120:123], v[32:35], a[80:83]\n"
"v_cvt_pk_bf16_f32 v91, v6, v7\n"
"v_cvt_pk_bf16_f32 v60, v8, v9\n"
"v_mfma_f32_16x16x32_bf16 a[80:83], v[124:127], v[36:39], a[80:83]\n"
"v_exp_f32 v10, v10\n"
"v_mfma_f32_16x16x32_bf16 a[112:115], v[120:123], v[40:43], a[112:115]\n"
"ds_read_b128 v[108:111], v180 offset:28928\n"
"v_mfma_f32_16x16x32_bf16 a[112:115], v[124:127], v[44:47], a[112:115]\n"
"buffer_load_dwordx4 v183, s[36:39], s52 offen offset:2048 lds\n"
"v_mfma_f32_16x16x32_bf16 v[0:3], v[96:99], a[192:195], v[144:147]\n"
"v_exp_f32 v11, v11\n"
"v_mfma_f32_16x16x32_bf16 v[0:3], v[100:103], a[196:199], v[0:3]\n"
"v_exp_f32 v12, v12\n"
"v_mfma_f32_16x16x32_bf16 v[0:3], v[112:115], a[200:203], v[0:3]\n"
"v_exp_f32 v13, v13\n"
"v_mfma_f32_16x16x32_bf16 v[0:3], v[116:119], a[204:207], v[0:3]\n"
"v_cvt_pk_bf16_f32 v61, v10, v11\n"
"v_cvt_pk_bf16_f32 v68, v12, v13\n"
"v_mfma_f32_16x16x32_bf16 a[144:147], v[120:123], v[48:51], a[144:147]\n"
"v_exp_f32 v14, v14\n"
"v_mfma_f32_16x16x32_bf16 a[144:147], v[124:127], v[52:55], a[144:147]\n"
"s_waitcnt lgkmcnt(1)\n"
"v_mfma_f32_16x16x32_bf16 a[20:23], v[128:131], v[16:19], a[20:23]\n"
"ds_read_b64_tr_b16 v[120:121], v181 offset:768\n"
"v_mfma_f32_16x16x32_bf16 a[20:23], v[132:135], v[20:23], a[20:23]\n"
"v_exp_f32 v15, v15\n"
"v_mfma_f32_16x16x32_bf16 v[172:175], v[176:179], v[48:51], v[172:175]\n"
"v_exp_f32 v0, v0\n"
"v_mfma_f32_16x16x32_bf16 v[172:175], v[176:179], v[52:55], v[172:175]\n"
"ds_read_b64_tr_b16 v[122:123], v181 offset:4864\n"
"v_cvt_pk_bf16_f32 v69, v14, v15\n"
"v_mfma_f32_16x16x32_bf16 v[4:7], v[96:99], a[208:211], v[148:151]\n"
"v_exp_f32 v1, v1\n"
"v_mfma_f32_16x16x32_bf16 v[4:7], v[100:103], a[212:215], v[4:7]\n"
"v_exp_f32 v2, v2\n"
"v_mfma_f32_16x16x32_bf16 v[4:7], v[112:115], a[216:219], v[4:7]\n"
"ds_read_b64_tr_b16 v[124:125], v181 offset:8960\n"
"v_cvt_pk_bf16_f32 v76, v0, v1\n"
"v_mfma_f32_16x16x32_bf16 v[4:7], v[116:119], a[220:223], v[4:7]\n"
"v_exp_f32 v3, v3\n"
"v_mfma_f32_16x16x32_bf16 a[52:55], v[128:131], v[24:27], a[52:55]\n"
"buffer_load_dwordx4 v184, s[36:39], s52 offen offset:3072 lds\n"
"v_cvt_pk_bf16_f32 v77, v2, v3\n"
"v_mfma_f32_16x16x32_bf16 a[52:55], v[132:135], v[28:31], a[52:55]\n"
"ds_read_b64_tr_b16 v[126:127], v181 offset:13056\n"
"v_mfma_f32_16x16x32_bf16 a[84:87], v[128:131], v[32:35], a[84:87]\n"
"v_mfma_f32_16x16x32_bf16 a[84:87], v[132:135], v[36:39], a[84:87]\n"
"v_mfma_f32_16x16x32_bf16 v[8:11], v[112:115], a[232:235], v[152:155]\n"
"ds_read_b128 v[112:115], v180 offset:29184\n"
"v_mfma_f32_16x16x32_bf16 v[8:11], v[116:119], a[236:239], v[8:11]\n"
"ds_read_b128 v[116:119], v180 offset:29440\n"
"v_mfma_f32_16x16x32_bf16 v[8:11], v[96:99], a[224:227], v[8:11]\n"
"v_exp_f32 v4, v4\n"
"v_mfma_f32_16x16x32_bf16 v[8:11], v[100:103], a[228:231], v[8:11]\n"
"v_exp_f32 v5, v5\n"
"v_mfma_f32_16x16x32_bf16 a[116:119], v[128:131], v[40:43], a[116:119]\n"
"v_exp_f32 v6, v6\n"
"v_mfma_f32_16x16x32_bf16 a[116:119], v[132:135], v[44:47], a[116:119]\n"
"v_exp_f32 v7, v7\n"
"v_mfma_f32_16x16x32_bf16 a[148:151], v[128:131], v[48:51], a[148:151]\n"
"v_cvt_pk_bf16_f32 v84, v4, v5\n"
"v_cvt_pk_bf16_f32 v85, v6, v7\n"
"v_mfma_f32_16x16x32_bf16 a[148:151], v[132:135], v[52:55], a[148:151]\n"
"v_mfma_f32_16x16x32_bf16 v[164:167], v[176:179], v[32:35], v[164:167]\n"
"ds_read_b64_tr_b16 v[128:129], v182 offset:768\n"
"v_mfma_f32_16x16x32_bf16 v[164:167], v[176:179], v[36:39], v[164:167]\n"
"s_waitcnt lgkmcnt(7)\n"
"v_mfma_f32_16x16x32_bf16 v[12:15], v[104:107], a[160:163], v[136:139]\n"
"v_exp_f32 v8, v8\n"
"v_mfma_f32_16x16x32_bf16 v[12:15], v[108:111], a[164:167], v[12:15]\n"
"s_waitcnt lgkmcnt(1)\n"
"v_mfma_f32_16x16x32_bf16 v[12:15], v[112:115], a[168:171], v[12:15]\n"
"ds_read_b64_tr_b16 v[130:131], v182 offset:4864\n"
"v_mfma_f32_16x16x32_bf16 v[12:15], v[116:119], a[172:175], v[12:15]\n"
"s_add_u32 s52, s52, 0x4000\n"
"v_mfma_f32_16x16x32_bf16 a[24:27], v[120:123], v[16:19], a[24:27]\n"
"ds_read_b64_tr_b16 v[132:133], v182 offset:8960\n"
"v_mfma_f32_16x16x32_bf16 a[24:27], v[124:127], v[20:23], a[24:27]\n"
"v_exp_f32 v9, v9\n"
"v_mfma_f32_16x16x32_bf16 a[56:59], v[120:123], v[24:27], a[56:59]\n"
"v_exp_f32 v10, v10\n"
"v_mfma_f32_16x16x32_bf16 a[56:59], v[124:127], v[28:31], a[56:59]\n"
"ds_read_b64_tr_b16 v[134:135], v182 offset:13056\n"
"v_cvt_pk_bf16_f32 v92, v8, v9\n"
"v_mfma_f32_16x16x32_bf16 v[0:3], v[104:107], a[176:179], v[140:143]\n"
"v_exp_f32 v11, v11\n"
"v_mfma_f32_16x16x32_bf16 v[0:3], v[108:111], a[180:183], v[0:3]\n"
"v_exp_f32 v12, v12\n"
"v_mfma_f32_16x16x32_bf16 v[0:3], v[112:115], a[184:187], v[0:3]\n"
"v_exp_f32 v13, v13\n"
"v_mfma_f32_16x16x32_bf16 v[0:3], v[116:119], a[188:191], v[0:3]\n"
"v_cvt_pk_bf16_f32 v93, v10, v11\n"
"v_cvt_pk_bf16_f32 v62, v12, v13\n"
"v_mfma_f32_16x16x32_bf16 a[88:91], v[120:123], v[32:35], a[88:91]\n"
"v_exp_f32 v14, v14\n"
"v_mfma_f32_16x16x32_bf16 a[88:91], v[124:127], v[36:39], a[88:91]\n"
"v_exp_f32 v15, v15\n"
"v_mfma_f32_16x16x32_bf16 a[120:123], v[120:123], v[40:43], a[120:123]\n"
"v_cvt_pk_bf16_f32 v63, v14, v15\n"
"v_mfma_f32_16x16x32_bf16 a[120:123], v[124:127], v[44:47], a[120:123]\n"
"v_mfma_f32_16x16x32_bf16 v[4:7], v[104:107], a[192:195], v[144:147]\n"
"v_exp_f32 v0, v0\n"
"v_mfma_f32_16x16x32_bf16 v[4:7], v[108:111], a[196:199], v[4:7]\n"
"v_exp_f32 v1, v1\n"
"v_mfma_f32_16x16x32_bf16 v[4:7], v[112:115], a[200:203], v[4:7]\n"
"v_exp_f32 v2, v2\n"
"v_mfma_f32_16x16x32_bf16 v[4:7], v[116:119], a[204:207], v[4:7]\n"
"v_exp_f32 v3, v3\n"
"v_mfma_f32_16x16x32_bf16 a[152:155], v[120:123], v[48:51], a[152:155]\n"
"v_cvt_pk_bf16_f32 v70, v0, v1\n"
"v_cvt_pk_bf16_f32 v71, v2, v3\n"
"v_mfma_f32_16x16x32_bf16 a[152:155], v[124:127], v[52:55], a[152:155]\n"
"s_waitcnt vmcnt(16) lgkmcnt(0)\n"
"v_xor_b32 v180, 0x8000, v180\n"
"s_barrier\n"
"v_mfma_f32_16x16x32_bf16 a[28:31], v[128:131], v[16:19], a[28:31]\n"
"ds_read_b128 v[96:99], v180 offset:0\n"
"v_mfma_f32_16x16x32_bf16 a[28:31], v[132:135], v[20:23], a[28:31]\n"
"ds_read_b128 v[100:103], v180 offset:256\n"
"v_mfma_f32_16x16x32_bf16 v[8:11], v[104:107], a[208:211], v[148:151]\n"
"ds_read_b64_tr_b16 v[120:121], v181 offset:16384\n"
"v_mfma_f32_16x16x32_bf16 v[8:11], v[108:111], a[212:215], v[8:11]\n"
"ds_read_b64_tr_b16 v[122:123], v181 offset:20480\n"
"v_mfma_f32_16x16x32_bf16 v[8:11], v[112:115], a[216:219], v[8:11]\n"
"ds_read_b64_tr_b16 v[124:125], v181 offset:24576\n"
"v_mfma_f32_16x16x32_bf16 v[8:11], v[116:119], a[220:223], v[8:11]\n"
"ds_read_b64_tr_b16 v[126:127], v181 offset:28672\n"
"v_mfma_f32_16x16x32_bf16 a[60:63], v[128:131], v[24:27], a[60:63]\n"
"s_add_u32 m0, s58, 0x10000\n"
"v_mfma_f32_16x16x32_bf16 a[60:63], v[132:135], v[28:31], a[60:63]\n"
"v_exp_f32 v4, v4\n"
"v_mfma_f32_16x16x32_bf16 a[92:95], v[128:131], v[32:35], a[92:95]\n"
"v_exp_f32 v5, v5\n"
"v_mfma_f32_16x16x32_bf16 a[92:95], v[132:135], v[36:39], a[92:95]\n"
"buffer_load_dwordx4 v183, s[40:43], s52 offen offset:0 lds\n"
"v_cvt_pk_bf16_f32 v78, v4, v5\n"
"v_mfma_f32_16x16x32_bf16 v[12:15], v[112:115], a[232:235], v[152:155]\n"
"ds_read_b128 v[112:115], v180 offset:512\n"
"v_mfma_f32_16x16x32_bf16 v[12:15], v[116:119], a[236:239], v[12:15]\n"
"ds_read_b128 v[116:119], v180 offset:768\n"
"v_mfma_f32_16x16x32_bf16 v[12:15], v[104:107], a[224:227], v[12:15]\n"
"v_exp_f32 v6, v6\n"
"v_mfma_f32_16x16x32_bf16 v[12:15], v[108:111], a[228:231], v[12:15]\n"
"v_exp_f32 v7, v7\n"
"v_mfma_f32_16x16x32_bf16 a[124:127], v[128:131], v[40:43], a[124:127]\n"
"v_exp_f32 v8, v8\n"
"v_mfma_f32_16x16x32_bf16 a[124:127], v[132:135], v[44:47], a[124:127]\n"
"v_exp_f32 v9, v9\n"
"v_mfma_f32_16x16x32_bf16 a[156:159], v[128:131], v[48:51], a[156:159]\n"
"v_cvt_pk_bf16_f32 v79, v6, v7\n"
"v_cvt_pk_bf16_f32 v86, v8, v9\n"
"v_mfma_f32_16x16x32_bf16 a[156:159], v[132:135], v[52:55], a[156:159]\n"
"v_exp_f32 v10, v10\n"
"v_mfma_f32_16x16x32_bf16 v[168:171], v[176:179], v[40:43], v[168:171]\n"
"v_exp_f32 v11, v11\n"
"v_mfma_f32_16x16x32_bf16 v[168:171], v[176:179], v[44:47], v[168:171]\n"
"v_cvt_pk_bf16_f32 v87, v10, v11\n"
"s_waitcnt lgkmcnt(2)\n"
"s_xor_b32 s58, s58, 0x8000\n"
"s_xor_b32 s59, s59, 0x8000\n"
"s_add_u32 s55, s55, -1\n"
"s_and_b32 s62, s55, 15\n"
"s_cmp_lg_u32 s62, 0\n"
"s_cbranch_scc1 L_loop_a_%=\n"
"s_cmp_lg_u32 s55, 32\n"
"s_cbranch_scc1 L_seam_a_%=\n"
"v_mfma_f32_16x16x32_bf16 v[0:3], v[96:99], a[160:163], v[136:139]\n"
"v_exp_f32 v12, v12\n"
"v_mfma_f32_16x16x32_bf16 v[0:3], v[100:103], a[164:167], v[0:3]\n"
"s_waitcnt lgkmcnt(0)\n"
"v_mfma_f32_16x16x32_bf16 v[0:3], v[112:115], a[168:171], v[0:3]\n"
"buffer_load_dwordx4 v184, s[40:43], s52 offen offset:1024 lds\n"
"v_mfma_f32_16x16x32_bf16 v[0:3], v[116:119], a[172:175], v[0:3]\n"
"v_exp_f32 v13, v13\n"
"v_mfma_f32_16x16x32_bf16 a[0:3], v[120:123], v[56:59], a[0:3]\n"
"ds_read_b128 v[104:107], v180 offset:4096\n"
"v_cvt_pk_bf16_f32 v94, v12, v13\n"
"v_mfma_f32_16x16x32_bf16 a[0:3], v[124:127], v[60:63], a[0:3]\n"
"ds_read_b64_tr_b16 v[128:129], v182 offset:16384\n"
"v_mfma_f32_16x16x32_bf16 a[32:35], v[120:123], v[64:67], a[32:35]\n"
"v_exp_f32 v14, v14\n"
"v_mfma_f32_16x16x32_bf16 a[32:35], v[124:127], v[68:71], a[32:35]\n"
"ds_read_b64_tr_b16 v[130:131], v182 offset:20480\n"
"v_mfma_f32_16x16x32_bf16 v[4:7], v[96:99], a[176:179], v[140:143]\n"
"v_exp_f32 v15, v15\n"
"v_mfma_f32_16x16x32_bf16 v[4:7], v[100:103], a[180:183], v[4:7]\n"
"ds_read_b64_tr_b16 v[132:133], v182 offset:24576\n"
"v_cvt_pk_bf16_f32 v95, v14, v15\n"
"v_mfma_f32_16x16x32_bf16 v[4:7], v[112:115], a[184:187], v[4:7]\n"
"v_exp_f32 v0, v0\n"
"v_mfma_f32_16x16x32_bf16 v[4:7], v[116:119], a[188:191], v[4:7]\n"
"ds_read_b64_tr_b16 v[134:135], v182 offset:28672\n"
"v_mfma_f32_16x16x32_bf16 a[64:67], v[120:123], v[72:75], a[64:67]\n"
"buffer_load_dwordx4 v183, s[40:43], s52 offen offset:2048 lds\n"
"v_mfma_f32_16x16x32_bf16 a[64:67], v[124:127], v[76:79], a[64:67]\n"
"v_exp_f32 v1, v1\n"
"v_mfma_f32_16x16x32_bf16 a[96:99], v[120:123], v[80:83], a[96:99]\n"
"ds_read_b128 v[108:111], v180 offset:4352\n"
"v_cvt_pk_bf16_f32 v16, v0, v1\n"
"v_mfma_f32_16x16x32_bf16 a[96:99], v[124:127], v[84:87], a[96:99]\n"
"v_exp_f32 v2, v2\n"
"v_mfma_f32_16x16x32_bf16 v[8:11], v[96:99], a[192:195], v[144:147]\n"
"v_exp_f32 v3, v3\n"
"v_mfma_f32_16x16x32_bf16 v[8:11], v[100:103], a[196:199], v[8:11]\n"
"v_exp_f32 v4, v4\n"
"v_mfma_f32_16x16x32_bf16 v[8:11], v[112:115], a[200:203], v[8:11]\n"
"v_exp_f32 v5, v5\n"
"v_mfma_f32_16x16x32_bf16 v[8:11], v[116:119], a[204:207], v[8:11]\n"
"v_cvt_pk_bf16_f32 v17, v2, v3\n"
"v_cvt_pk_bf16_f32 v24, v4, v5\n"
"v_mfma_f32_16x16x32_bf16 a[128:131], v[120:123], v[88:91], a[128:131]\n"
"buffer_load_dwordx4 v[192:195], v185, s[44:47], s70 offen offset:0 nt\n"
"v_mfma_f32_16x16x32_bf16 a[128:131], v[124:127], v[92:95], a[128:131]\n"
"s_waitcnt lgkmcnt(1)\n"
"v_mfma_f32_16x16x32_bf16 a[4:7], v[128:131], v[56:59], a[4:7]\n"
"ds_read_b64_tr_b16 v[120:121], v181 offset:16640\n"
"v_mfma_f32_16x16x32_bf16 a[4:7], v[132:135], v[60:63], a[4:7]\n"
"v_exp_f32 v6, v6\n"
"v_mfma_f32_16x16x32_bf16 v[12:15], v[96:99], a[208:211], v[148:151]\n"
"v_exp_f32 v7, v7\n"
"v_mfma_f32_16x16x32_bf16 v[12:15], v[100:103], a[212:215], v[12:15]\n"
"ds_read_b64_tr_b16 v[122:123], v181 offset:20736\n"
"v_cvt_pk_bf16_f32 v25, v6, v7\n"
"v_mfma_f32_16x16x32_bf16 v[12:15], v[112:115], a[216:219], v[12:15]\n"
"v_exp_f32 v8, v8\n"
"v_mfma_f32_16x16x32_bf16 v[12:15], v[116:119], a[220:223], v[12:15]\n"
"v_exp_f32 v9, v9\n"
"v_mfma_f32_16x16x32_bf16 a[36:39], v[128:131], v[64:67], a[36:39]\n"
"ds_read_b64_tr_b16 v[124:125], v181 offset:24832\n"
"v_cvt_pk_bf16_f32 v32, v8, v9\n"
"v_mfma_f32_16x16x32_bf16 a[36:39], v[132:135], v[68:71], a[36:39]\n"
"buffer_load_dwordx4 v184, s[40:43], s52 offen offset:3072 lds\n"
"v_mfma_f32_16x16x32_bf16 a[68:71], v[128:131], v[72:75], a[68:71]\n"
"v_exp_f32 v10, v10\n"
"v_mfma_f32_16x16x32_bf16 a[68:71], v[132:135], v[76:79], a[68:71]\n"
"ds_read_b64_tr_b16 v[126:127], v181 offset:28928\n"
"v_mfma_f32_16x16x32_bf16 v[0:3], v[112:115], a[232:235], v[152:155]\n"
"ds_read_b128 v[112:115], v180 offset:4608\n"
"v_mfma_f32_16x16x32_bf16 v[0:3], v[116:119], a[236:239], v[0:3]\n"
"ds_read_b128 v[116:119], v180 offset:4864\n"
"v_mfma_f32_16x16x32_bf16 v[0:3], v[96:99], a[224:227], v[0:3]\n"
"v_exp_f32 v11, v11\n"
"v_mfma_f32_16x16x32_bf16 v[0:3], v[100:103], a[228:231], v[0:3]\n"
"v_exp_f32 v12, v12\n"
"v_mfma_f32_16x16x32_bf16 a[100:103], v[128:131], v[80:83], a[100:103]\n"
"v_exp_f32 v13, v13\n"
"v_mfma_f32_16x16x32_bf16 a[100:103], v[132:135], v[84:87], a[100:103]\n"
"v_cvt_pk_bf16_f32 v33, v10, v11\n"
"v_cvt_pk_bf16_f32 v40, v12, v13\n"
"v_mfma_f32_16x16x32_bf16 a[132:135], v[128:131], v[88:91], a[132:135]\n"
"buffer_load_dwordx4 v[196:199], v185, s[44:47], s70 offen offset:64 nt\n"
"v_mfma_f32_16x16x32_bf16 a[132:135], v[132:135], v[92:95], a[132:135]\n"
"v_exp_f32 v14, v14\n"
"v_mfma_f32_16x16x32_bf16 v[156:159], v[176:179], v[56:59], v[156:159]\n"
"ds_read_b64_tr_b16 v[128:129], v182 offset:16640\n"
"v_mfma_f32_16x16x32_bf16 v[156:159], v[176:179], v[60:63], v[156:159]\n"
"s_waitcnt lgkmcnt(7)\n"
"v_mfma_f32_16x16x32_bf16 v[4:7], v[104:107], a[160:163], v[136:139]\n"
"v_exp_f32 v15, v15\n"
"v_mfma_f32_16x16x32_bf16 v[4:7], v[108:111], a[164:167], v[4:7]\n"
"v_cvt_pk_bf16_f32 v41, v14, v15\n"
"s_waitcnt lgkmcnt(1)\n"
"v_mfma_f32_16x16x32_bf16 v[4:7], v[112:115], a[168:171], v[4:7]\n"
"ds_read_b64_tr_b16 v[130:131], v182 offset:20736\n"
"v_mfma_f32_16x16x32_bf16 v[4:7], v[116:119], a[172:175], v[4:7]\n"
"v_exp_f32 v0, v0\n"
"v_mfma_f32_16x16x32_bf16 a[8:11], v[120:123], v[56:59], a[8:11]\n"
"ds_read_b128 v[96:99], v180 offset:8192\n"
"v_mfma_f32_16x16x32_bf16 a[8:11], v[124:127], v[60:63], a[8:11]\n"
"ds_read_b64_tr_b16 v[132:133], v182 offset:24832\n"
"v_mfma_f32_16x16x32_bf16 a[40:43], v[120:123], v[64:67], a[40:43]\n"
"s_add_u32 m0, s53, 0x4000\n"
"v_mfma_f32_16x16x32_bf16 a[40:43], v[124:127], v[68:71], a[40:43]\n"
"ds_read_b64_tr_b16 v[134:135], v182 offset:28928\n"
"v_mfma_f32_16x16x32_bf16 v[8:11], v[104:107], a[176:179], v[140:143]\n"
"v_exp_f32 v1, v1\n"
"v_mfma_f32_16x16x32_bf16 v[8:11], v[108:111], a[180:183], v[8:11]\n"
"v_exp_f32 v2, v2\n"
"v_mfma_f32_16x16x32_bf16 v[8:11], v[112:115], a[184:187], v[8:11]\n"
"v_exp_f32 v3, v3\n"
"v_mfma_f32_16x16x32_bf16 v[8:11], v[116:119], a[188:191], v[8:11]\n"
"v_cvt_pk_bf16_f32 v48, v0, v1\n"
"v_cvt_pk_bf16_f32 v49, v2, v3\n"
"v_mfma_f32_16x16x32_bf16 a[72:75], v[120:123], v[72:75], a[72:75]\n"
"v_exp_f32 v4, v4\n"
"v_mfma_f32_16x16x32_bf16 a[72:75], v[124:127], v[76:79], a[72:75]\n"
"v_exp_f32 v5, v5\n"
"v_mfma_f32_16x16x32_bf16 a[104:107], v[120:123], v[80:83], a[104:107]\n"
"ds_read_b128 v[100:103], v180 offset:8448\n"
"v_cvt_pk_bf16_f32 v18, v4, v5\n"
"v_mfma_f32_16x16x32_bf16 a[104:107], v[124:127], v[84:87], a[104:107]\n"
"buffer_load_dwordx4 v183, s[36:39], s52 offen offset:0 lds\n"
"v_mfma_f32_16x16x32_bf16 v[12:15], v[104:107], a[192:195], v[144:147]\n"
"v_exp_f32 v6, v6\n"
"v_mfma_f32_16x16x32_bf16 v[12:15], v[108:111], a[196:199], v[12:15]\n"
"v_exp_f32 v7, v7\n"
"v_mfma_f32_16x16x32_bf16 v[12:15], v[112:115], a[200:203], v[12:15]\n"
"v_exp_f32 v8, v8\n"
"v_mfma_f32_16x16x32_bf16 v[12:15], v[116:119], a[204:207], v[12:15]\n"
"v_exp_f32 v9, v9\n"
"v_mfma_f32_16x16x32_bf16 a[136:139], v[120:123], v[88:91], a[136:139]\n"
"v_cvt_pk_bf16_f32 v19, v6, v7\n"
"v_cvt_pk_bf16_f32 v26, v8, v9\n"
"v_mfma_f32_16x16x32_bf16 a[136:139], v[124:127], v[92:95], a[136:139]\n"
"s_waitcnt lgkmcnt(1)\n"
"v_mfma_f32_16x16x32_bf16 a[12:15], v[128:131], v[56:59], a[12:15]\n"
"ds_read_b64_tr_b16 v[120:121], v181 offset:16896\n"
"v_mfma_f32_16x16x32_bf16 a[12:15], v[132:135], v[60:63], a[12:15]\n"
"buffer_load_dwordx4 v[200:203], v185, s[44:47], s70 offen offset:128 nt\n"
"v_mfma_f32_16x16x32_bf16 v[0:3], v[104:107], a[208:211], v[148:151]\n"
"v_exp_f32 v10, v10\n"
"v_mfma_f32_16x16x32_bf16 v[0:3], v[108:111], a[212:215], v[0:3]\n"
"ds_read_b64_tr_b16 v[122:123], v181 offset:20992\n"
"v_mfma_f32_16x16x32_bf16 v[0:3], v[112:115], a[216:219], v[0:3]\n"
"v_exp_f32 v11, v11\n"
"v_mfma_f32_16x16x32_bf16 v[0:3], v[116:119], a[220:223], v[0:3]\n"
"v_exp_f32 v12, v12\n"
"v_mfma_f32_16x16x32_bf16 a[44:47], v[128:131], v[64:67], a[44:47]\n"
"ds_read_b64_tr_b16 v[124:125], v181 offset:25088\n"
"v_cvt_pk_bf16_f32 v27, v10, v11\n"
"v_mfma_f32_16x16x32_bf16 a[44:47], v[132:135], v[68:71], a[44:47]\n"
"v_exp_f32 v13, v13\n"
"v_mfma_f32_16x16x32_bf16 a[76:79], v[128:131], v[72:75], a[76:79]\n"
"v_exp_f32 v14, v14\n"
"v_mfma_f32_16x16x32_bf16 a[76:79], v[132:135], v[76:79], a[76:79]\n"
"ds_read_b64_tr_b16 v[126:127], v181 offset:29184\n"
"v_cvt_pk_bf16_f32 v34, v12, v13\n"
"v_mfma_f32_16x16x32_bf16 v[4:7], v[112:115], a[232:235], v[152:155]\n"
"ds_read_b128 v[112:115], v180 offset:8704\n"
"v_mfma_f32_16x16x32_bf16 v[4:7], v[116:119], a[236:239], v[4:7]\n"
"ds_read_b128 v[116:119], v180 offset:8960\n"
"v_mfma_f32_16x16x32_bf16 v[4:7], v[104:107], a[224:227], v[4:7]\n"
"buffer_load_dwordx4 v184, s[36:39], s52 offen offset:1024 lds\n"
"v_mfma_f32_16x16x32_bf16 v[4:7], v[108:111], a[228:231], v[4:7]\n"
"v_exp_f32 v15, v15\n"
"v_mfma_f32_16x16x32_bf16 a[108:111], v[128:131], v[80:83], a[108:111]\n"
"v_exp_f32 v0, v0\n"
"v_mfma_f32_16x16x32_bf16 a[108:111], v[132:135], v[84:87], a[108:111]\n"
"v_exp_f32 v1, v1\n"
"v_mfma_f32_16x16x32_bf16 a[140:143], v[128:131], v[88:91], a[140:143]\n"
"v_cvt_pk_bf16_f32 v35, v14, v15\n"
"v_cvt_pk_bf16_f32 v42, v0, v1\n"
"v_mfma_f32_16x16x32_bf16 a[140:143], v[132:135], v[92:95], a[140:143]\n"
"v_exp_f32 v2, v2\n"
"v_mfma_f32_16x16x32_bf16 v[160:163], v[176:179], v[64:67], v[160:163]\n"
"ds_read_b64_tr_b16 v[128:129], v182 offset:16896\n"
"v_mfma_f32_16x16x32_bf16 v[160:163], v[176:179], v[68:71], v[160:163]\n"
"s_waitcnt lgkmcnt(7)\n"
"v_mfma_f32_16x16x32_bf16 v[8:11], v[96:99], a[160:163], v[136:139]\n"
"buffer_load_dwordx4 v[204:207], v185, s[44:47], s70 offen offset:192 nt\n"
"v_mfma_f32_16x16x32_bf16 v[8:11], v[100:103], a[164:167], v[8:11]\n"
"s_waitcnt lgkmcnt(1)\n"
"v_mfma_f32_16x16x32_bf16 v[8:11], v[112:115], a[168:171], v[8:11]\n"
"ds_read_b64_tr_b16 v[130:131], v182 offset:20992\n"
"v_mfma_f32_16x16x32_bf16 v[8:11], v[116:119], a[172:175], v[8:11]\n"
"v_exp_f32 v3, v3\n"
"v_mfma_f32_16x16x32_bf16 a[16:19], v[120:123], v[56:59], a[16:19]\n"
"ds_read_b128 v[104:107], v180 offset:12288\n"
"v_cvt_pk_bf16_f32 v43, v2, v3\n"
"v_mfma_f32_16x16x32_bf16 a[16:19], v[124:127], v[60:63], a[16:19]\n"
"ds_read_b64_tr_b16 v[132:133], v182 offset:25088\n"
"v_mfma_f32_16x16x32_bf16 a[48:51], v[120:123], v[64:67], a[48:51]\n"
"v_exp_f32 v4, v4\n"
"v_mfma_f32_16x16x32_bf16 a[48:51], v[124:127], v[68:71], a[48:51]\n"
"ds_read_b64_tr_b16 v[134:135], v182 offset:29184\n"
"v_mfma_f32_16x16x32_bf16 v[12:15], v[96:99], a[176:179], v[140:143]\n"
"buffer_load_dwordx4 v183, s[36:39], s52 offen offset:2048 lds\n"
"v_mfma_f32_16x16x32_bf16 v[12:15], v[100:103], a[180:183], v[12:15]\n"
"v_exp_f32 v5, v5\n"
"v_mfma_f32_16x16x32_bf16 v[12:15], v[112:115], a[184:187], v[12:15]\n"
"v_exp_f32 v6, v6\n"
"v_mfma_f32_16x16x32_bf16 v[12:15], v[116:119], a[188:191], v[12:15]\n"
"v_exp_f32 v7, v7\n"
"v_mfma_f32_16x16x32_bf16 a[80:83], v[120:123], v[72:75], a[80:83]\n"
"v_cvt_pk_bf16_f32 v50, v4, v5\n"
"v_cvt_pk_bf16_f32 v51, v6, v7\n"
"v_mfma_f32_16x16x32_bf16 a[80:83], v[124:127], v[76:79], a[80:83]\n"
"v_exp_f32 v8, v8\n"
"v_mfma_f32_16x16x32_bf16 a[112:115], v[120:123], v[80:83], a[112:115]\n"
"ds_read_b128 v[108:111], v180 offset:12544\n"
"v_mfma_f32_16x16x32_bf16 a[112:115], v[124:127], v[84:87], a[112:115]\n"
"v_exp_f32 v9, v9\n"
"v_mfma_f32_16x16x32_bf16 v[0:3], v[96:99], a[192:195], v[144:147]\n"
"v_exp_f32 v10, v10\n"
"v_mfma_f32_16x16x32_bf16 v[0:3], v[100:103], a[196:199], v[0:3]\n"
"buffer_load_dwordx4 v[208:211], v185, s[44:47], s71 offen offset:0 nt\n"
"v_cvt_pk_bf16_f32 v20, v8, v9\n"
"v_mfma_f32_16x16x32_bf16 v[0:3], v[112:115], a[200:203], v[0:3]\n"
"v_exp_f32 v11, v11\n"
"v_mfma_f32_16x16x32_bf16 v[0:3], v[116:119], a[204:207], v[0:3]\n"
"v_exp_f32 v12, v12\n"
"v_mfma_f32_16x16x32_bf16 a[144:147], v[120:123], v[88:91], a[144:147]\n"
"v_exp_f32 v13, v13\n"
"v_mfma_f32_16x16x32_bf16 a[144:147], v[124:127], v[92:95], a[144:147]\n"
"v_cvt_pk_bf16_f32 v21, v10, v11\n"
"s_waitcnt lgkmcnt(1)\n"
"v_mfma_f32_16x16x32_bf16 a[20:23], v[128:131], v[56:59], a[20:23]\n"
"ds_read_b64_tr_b16 v[120:121], v181 offset:17152\n"
"v_cvt_pk_bf16_f32 v28, v12, v13\n"
"v_mfma_f32_16x16x32_bf16 a[20:23], v[132:135], v[60:63], a[20:23]\n"
"v_exp_f32 v14, v14\n"
"v_mfma_f32_16x16x32_bf16 v[172:175], v[176:179], v[88:91], v[172:175]\n"
"v_exp_f32 v15, v15\n"
"v_mfma_f32_16x16x32_bf16 v[172:175], v[176:179], v[92:95], v[172:175]\n"
"ds_read_b64_tr_b16 v[122:123], v181 offset:21248\n"
"v_cvt_pk_bf16_f32 v29, v14, v15\n"
"v_mfma_f32_16x16x32_bf16 v[4:7], v[96:99], a[208:211], v[148:151]\n"
"buffer_load_dwordx4 v184, s[36:39], s52 offen offset:3072 lds\n"
"v_mfma_f32_16x16x32_bf16 v[4:7], v[100:103], a[212:215], v[4:7]\n"
"v_exp_f32 v0, v0\n"
"v_mfma_f32_16x16x32_bf16 v[4:7], v[112:115], a[216:219], v[4:7]\n"
"ds_read_b64_tr_b16 v[124:125], v181 offset:25344\n"
"v_mfma_f32_16x16x32_bf16 v[4:7], v[116:119], a[220:223], v[4:7]\n"
"v_exp_f32 v1, v1\n"
"v_mfma_f32_16x16x32_bf16 a[52:55], v[128:131], v[64:67], a[52:55]\n"
"v_exp_f32 v2, v2\n"
"v_mfma_f32_16x16x32_bf16 a[52:55], v[132:135], v[68:71], a[52:55]\n"
"ds_read_b64_tr_b16 v[126:127], v181 offset:29440\n"
"v_cvt_pk_bf16_f32 v36, v0, v1\n"
"v_mfma_f32_16x16x32_bf16 a[84:87], v[128:131], v[72:75], a[84:87]\n"
"v_exp_f32 v3, v3\n"
"v_mfma_f32_16x16x32_bf16 a[84:87], v[132:135], v[76:79], a[84:87]\n"
"v_cvt_pk_bf16_f32 v37, v2, v3\n"
"v_mfma_f32_16x16x32_bf16 v[8:11], v[112:115], a[232:235], v[152:155]\n"
"ds_read_b128 v[112:115], v180 offset:12800\n"
"v_mfma_f32_16x16x32_bf16 v[8:11], v[116:119], a[236:239], v[8:11]\n"
"ds_read_b128 v[116:119], v180 offset:13056\n"
"v_mfma_f32_16x16x32_bf16 v[8:11], v[96:99], a[224:227], v[8:11]\n"
"buffer_load_dwordx4 v[212:215], v185, s[44:47], s71 offen offset:64 nt\n"
"v_mfma_f32_16x16x32_bf16 v[8:11], v[100:103], a[228:231], v[8:11]\n"
"v_exp_f32 v4, v4\n"
"v_mfma_f32_16x16x32_bf16 a[116:119], v[128:131], v[80:83], a[116:119]\n"
"v_exp_f32 v5, v5\n"
"v_mfma_f32_16x16x32_bf16 a[116:119], v[132:135], v[84:87], a[116:119]\n"
"v_exp_f32 v6, v6\n"
"v_mfma_f32_16x16x32_bf16 a[148:151], v[128:131], v[88:91], a[148:151]\n"
"v_exp_f32 v7, v7\n"
"v_mfma_f32_16x16x32_bf16 a[148:151], v[132:135], v[92:95], a[148:151]\n"
"v_cvt_pk_bf16_f32 v44, v4, v5\n"
"v_cvt_pk_bf16_f32 v45, v6, v7\n"
"v_mfma_f32_16x16x32_bf16 v[164:167], v[176:179], v[72:75], v[164:167]\n"
"ds_read_b64_tr_b16 v[128:129], v182 offset:17152\n"
"v_mfma_f32_16x16x32_bf16 v[164:167], v[176:179], v[76:79], v[164:167]\n"
"s_waitcnt lgkmcnt(7)\n"
"v_mfma_f32_16x16x32_bf16 v[12:15], v[104:107], a[160:163], v[136:139]\n"
"s_add_u32 s52, s52, 0x4000\n"
"v_mfma_f32_16x16x32_bf16 v[12:15], v[108:111], a[164:167], v[12:15]\n"
"s_waitcnt lgkmcnt(1)\n"
"v_mfma_f32_16x16x32_bf16 v[12:15], v[112:115], a[168:171], v[12:15]\n"
"ds_read_b64_tr_b16 v[130:131], v182 offset:21248\n"
"v_mfma_f32_16x16x32_bf16 v[12:15], v[116:119], a[172:175], v[12:15]\n"
"v_exp_f32 v8, v8\n"
"v_mfma_f32_16x16x32_bf16 a[24:27], v[120:123], v[56:59], a[24:27]\n"
"ds_read_b64_tr_b16 v[132:133], v182 offset:25344\n"
"v_mfma_f32_16x16x32_bf16 a[24:27], v[124:127], v[60:63], a[24:27]\n"
"v_exp_f32 v9, v9\n"
"v_mfma_f32_16x16x32_bf16 a[56:59], v[120:123], v[64:67], a[56:59]\n"
"v_exp_f32 v10, v10\n"
"v_mfma_f32_16x16x32_bf16 a[56:59], v[124:127], v[68:71], a[56:59]\n"
"ds_read_b64_tr_b16 v[134:135], v182 offset:29440\n"
"v_cvt_pk_bf16_f32 v52, v8, v9\n"
"v_mfma_f32_16x16x32_bf16 v[0:3], v[104:107], a[176:179], v[140:143]\n"
"v_exp_f32 v11, v11\n"
"v_mfma_f32_16x16x32_bf16 v[0:3], v[108:111], a[180:183], v[0:3]\n"
"buffer_load_dwordx4 v[216:219], v185, s[44:47], s71 offen offset:128 nt\n"
"v_cvt_pk_bf16_f32 v53, v10, v11\n"
"v_mfma_f32_16x16x32_bf16 v[0:3], v[112:115], a[184:187], v[0:3]\n"
"v_exp_f32 v12, v12\n"
"v_mfma_f32_16x16x32_bf16 v[0:3], v[116:119], a[188:191], v[0:3]\n"
"v_exp_f32 v13, v13\n"
"v_mfma_f32_16x16x32_bf16 a[88:91], v[120:123], v[72:75], a[88:91]\n"
"v_exp_f32 v14, v14\n"
"v_mfma_f32_16x16x32_bf16 a[88:91], v[124:127], v[76:79], a[88:91]\n"
"v_exp_f32 v15, v15\n"
"v_mfma_f32_16x16x32_bf16 a[120:123], v[120:123], v[80:83], a[120:123]\n"
"v_cvt_pk_bf16_f32 v22, v12, v13\n"
"v_cvt_pk_bf16_f32 v23, v14, v15\n"
"v_mfma_f32_16x16x32_bf16 a[120:123], v[124:127], v[84:87], a[120:123]\n"
"v_mfma_f32_16x16x32_bf16 v[4:7], v[104:107], a[192:195], v[144:147]\n"
"v_exp_f32 v0, v0\n"
"v_mfma_f32_16x16x32_bf16 v[4:7], v[108:111], a[196:199], v[4:7]\n"
"v_exp_f32 v1, v1\n"
"v_mfma_f32_16x16x32_bf16 v[4:7], v[112:115], a[200:203], v[4:7]\n"
"v_exp_f32 v2, v2\n"
"v_mfma_f32_16x16x32_bf16 v[4:7], v[116:119], a[204:207], v[4:7]\n"
"v_exp_f32 v3, v3\n"
"v_mfma_f32_16x16x32_bf16 a[152:155], v[120:123], v[88:91], a[152:155]\n"
"v_cvt_pk_bf16_f32 v30, v0, v1\n"
"v_cvt_pk_bf16_f32 v31, v2, v3\n"
"v_mfma_f32_16x16x32_bf16 a[152:155], v[124:127], v[92:95], a[152:155]\n"
"s_waitcnt vmcnt(23) lgkmcnt(0)\n"
"v_xor_b32 v181, 0x8000, v181\n"
"v_xor_b32 v182, 0x8000, v182\n"
"s_barrier\n"
"v_mfma_f32_16x16x32_bf16 a[28:31], v[128:131], v[56:59], a[28:31]\n"
"ds_read_b128 v[96:99], v180 offset:16384\n"
"v_mfma_f32_16x16x32_bf16 a[28:31], v[132:135], v[60:63], a[28:31]\n"
"ds_read_b128 v[100:103], v180 offset:16640\n"
"v_mfma_f32_16x16x32_bf16 v[8:11], v[104:107], a[208:211], v[148:151]\n"
"ds_read_b64_tr_b16 v[120:121], v181 offset:0\n"
"v_mfma_f32_16x16x32_bf16 v[8:11], v[108:111], a[212:215], v[8:11]\n"
"ds_read_b64_tr_b16 v[122:123], v181 offset:4096\n"
"v_mfma_f32_16x16x32_bf16 v[8:11], v[112:115], a[216:219], v[8:11]\n"
"ds_read_b64_tr_b16 v[124:125], v181 offset:8192\n"
"v_mfma_f32_16x16x32_bf16 v[8:11], v[116:119], a[220:223], v[8:11]\n"
"ds_read_b64_tr_b16 v[126:127], v181 offset:12288\n"
"v_mfma_f32_16x16x32_bf16 a[60:63], v[128:131], v[64:67], a[60:63]\n"
"s_add_u32 m0, s54, 0x4000\n"
"v_mfma_f32_16x16x32_bf16 a[60:63], v[132:135], v[68:71], a[60:63]\n"
"v_exp_f32 v4, v4\n"
"v_mfma_f32_16x16x32_bf16 a[92:95], v[128:131], v[72:75], a[92:95]\n"
"v_exp_f32 v5, v5\n"
"v_mfma_f32_16x16x32_bf16 a[92:95], v[132:135], v[76:79], a[92:95]\n"
"buffer_load_dwordx4 v183, s[40:43], s52 offen offset:0 lds\n"
"v_cvt_pk_bf16_f32 v38, v4, v5\n"
"v_mfma_f32_16x16x32_bf16 v[12:15], v[112:115], a[232:235], v[152:155]\n"
"ds_read_b128 v[112:115], v180 offset:16896\n"
"v_mfma_f32_16x16x32_bf16 v[12:15], v[116:119], a[236:239], v[12:15]\n"
"ds_read_b128 v[116:119], v180 offset:17152\n"
"v_mfma_f32_16x16x32_bf16 v[12:15], v[104:107], a[224:227], v[12:15]\n"
"v_exp_f32 v6, v6\n"
"v_mfma_f32_16x16x32_bf16 v[12:15], v[108:111], a[228:231], v[12:15]\n"
"v_exp_f32 v7, v7\n"
"v_mfma_f32_16x16x32_bf16 a[124:127], v[128:131], v[80:83], a[124:127]\n"
"v_exp_f32 v8, v8\n"
"v_mfma_f32_16x16x32_bf16 a[124:127], v[132:135], v[84:87], a[124:127]\n"
"v_exp_f32 v9, v9\n"
"v_mfma_f32_16x16x32_bf16 a[156:159], v[128:131], v[88:91], a[156:159]\n"
"v_cvt_pk_bf16_f32 v39, v6, v7\n"
"v_cvt_pk_bf16_f32 v46, v8, v9\n"
"v_mfma_f32_16x16x32_bf16 a[156:159], v[132:135], v[92:95], a[156:159]\n"
"v_exp_f32 v10, v10\n"
"v_mfma_f32_16x16x32_bf16 v[168:171], v[176:179], v[80:83], v[168:171]\n"
"v_exp_f32 v11, v11\n"
"v_mfma_f32_16x16x32_bf16 v[168:171], v[176:179], v[84:87], v[168:171]\n"
"v_cvt_pk_bf16_f32 v47, v10, v11\n"
"s_waitcnt lgkmcnt(2)\n"
"v_mfma_f32_16x16x32_bf16 v[0:3], v[96:99], a[160:163], v[136:139]\n"
"v_exp_f32 v12, v12\n"
"v_mfma_f32_16x16x32_bf16 v[0:3], v[100:103], a[164:167], v[0:3]\n"
"s_waitcnt lgkmcnt(0)\n"
"v_mfma_f32_16x16x32_bf16 v[0:3], v[112:115], a[168:171], v[0:3]\n"
"buffer_load_dwordx4 v184, s[40:43], s52 offen offset:1024 lds\n"
"v_mfma_f32_16x16x32_bf16 v[0:3], v[116:119], a[172:175], v[0:3]\n"
"v_exp_f32 v13, v13\n"
"v_mfma_f32_16x16x32_bf16 a[0:3], v[120:123], v[16:19], a[0:3]\n"
"ds_read_b128 v[104:107], v180 offset:20480\n"
"v_cvt_pk_bf16_f32 v54, v12, v13\n"
"v_mfma_f32_16x16x32_bf16 a[0:3], v[124:127], v[20:23], a[0:3]\n"
"ds_read_b64_tr_b16 v[128:129], v182 offset:0\n"
"v_mfma_f32_16x16x32_bf16 a[32:35], v[120:123], v[24:27], a[32:35]\n"
"v_exp_f32 v14, v14\n"
"v_mfma_f32_16x16x32_bf16 a[32:35], v[124:127], v[28:31], a[32:35]\n"
"ds_read_b64_tr_b16 v[130:131], v182 offset:4096\n"
"v_mfma_f32_16x16x32_bf16 v[4:7], v[96:99], a[176:179], v[140:143]\n"
"v_exp_f32 v15, v15\n"
"v_mfma_f32_16x16x32_bf16 v[4:7], v[100:103], a[180:183], v[4:7]\n"
"ds_read_b64_tr_b16 v[132:133], v182 offset:8192\n"
"v_cvt_pk_bf16_f32 v55, v14, v15\n"
"v_mfma_f32_16x16x32_bf16 v[4:7], v[112:115], a[184:187], v[4:7]\n"
"v_exp_f32 v0, v0\n"
"v_mfma_f32_16x16x32_bf16 v[4:7], v[116:119], a[188:191], v[4:7]\n"
"ds_read_b64_tr_b16 v[134:135], v182 offset:12288\n"
"v_mfma_f32_16x16x32_bf16 a[64:67], v[120:123], v[32:35], a[64:67]\n"
"buffer_load_dwordx4 v183, s[40:43], s52 offen offset:2048 lds\n"
"v_mfma_f32_16x16x32_bf16 a[64:67], v[124:127], v[36:39], a[64:67]\n"
"v_exp_f32 v1, v1\n"
"v_mfma_f32_16x16x32_bf16 a[96:99], v[120:123], v[40:43], a[96:99]\n"
"ds_read_b128 v[108:111], v180 offset:20736\n"
"v_cvt_pk_bf16_f32 v56, v0, v1\n"
"v_mfma_f32_16x16x32_bf16 a[96:99], v[124:127], v[44:47], a[96:99]\n"
"v_exp_f32 v2, v2\n"
"v_mfma_f32_16x16x32_bf16 v[8:11], v[96:99], a[192:195], v[144:147]\n"
"v_exp_f32 v3, v3\n"
"v_mfma_f32_16x16x32_bf16 v[8:11], v[100:103], a[196:199], v[8:11]\n"
"v_exp_f32 v4, v4\n"
"v_mfma_f32_16x16x32_bf16 v[8:11], v[112:115], a[200:203], v[8:11]\n"
"v_exp_f32 v5, v5\n"
"v_mfma_f32_16x16x32_bf16 v[8:11], v[116:119], a[204:207], v[8:11]\n"
"v_cvt_pk_bf16_f32 v57, v2, v3\n"
"v_cvt_pk_bf16_f32 v64, v4, v5\n"
"v_mfma_f32_16x16x32_bf16 a[128:131], v[120:123], v[48:51], a[128:131]\n"
"buffer_load_dwordx4 v[220:223], v185, s[44:47], s71 offen offset:192 nt\n"
"v_mfma_f32_16x16x32_bf16 a[128:131], v[124:127], v[52:55], a[128:131]\n"
"s_waitcnt lgkmcnt(1)\n"
"v_mfma_f32_16x16x32_bf16 a[4:7], v[128:131], v[16:19], a[4:7]\n"
"ds_read_b64_tr_b16 v[120:121], v181 offset:256\n"
"v_mfma_f32_16x16x32_bf16 a[4:7], v[132:135], v[20:23], a[4:7]\n"
"v_exp_f32 v6, v6\n"
"v_mfma_f32_16x16x32_bf16 v[12:15], v[96:99], a[208:211], v[148:151]\n"
"v_exp_f32 v7, v7\n"
"v_mfma_f32_16x16x32_bf16 v[12:15], v[100:103], a[212:215], v[12:15]\n"
"ds_read_b64_tr_b16 v[122:123], v181 offset:4352\n"
"v_cvt_pk_bf16_f32 v65, v6, v7\n"
"v_mfma_f32_16x16x32_bf16 v[12:15], v[112:115], a[216:219], v[12:15]\n"
"v_exp_f32 v8, v8\n"
"v_mfma_f32_16x16x32_bf16 v[12:15], v[116:119], a[220:223], v[12:15]\n"
"v_exp_f32 v9, v9\n"
"v_mfma_f32_16x16x32_bf16 a[36:39], v[128:131], v[24:27], a[36:39]\n"
"ds_read_b64_tr_b16 v[124:125], v181 offset:8448\n"
"v_cvt_pk_bf16_f32 v72, v8, v9\n"
"v_mfma_f32_16x16x32_bf16 a[36:39], v[132:135], v[28:31], a[36:39]\n"
"buffer_load_dwordx4 v184, s[40:43], s52 offen offset:3072 lds\n"
"v_mfma_f32_16x16x32_bf16 a[68:71], v[128:131], v[32:35], a[68:71]\n"
"v_exp_f32 v10, v10\n"
"v_mfma_f32_16x16x32_bf16 a[68:71], v[132:135], v[36:39], a[68:71]\n"
"ds_read_b64_tr_b16 v[126:127], v181 offset:12544\n"
"v_mfma_f32_16x16x32_bf16 v[0:3], v[112:115], a[232:235], v[152:155]\n"
"ds_read_b128 v[112:115], v180 offset:20992\n"
"v_mfma_f32_16x16x32_bf16 v[0:3], v[116:119], a[236:239], v[0:3]\n"
"ds_read_b128 v[116:119], v180 offset:21248\n"
"v_mfma_f32_16x16x32_bf16 v[0:3], v[96:99], a[224:227], v[0:3]\n"
"v_exp_f32 v11, v11\n"
"v_mfma_f32_16x16x32_bf16 v[0:3], v[100:103], a[228:231], v[0:3]\n"
"v_exp_f32 v12, v12\n"
"v_mfma_f32_16x16x32_bf16 a[100:103], v[128:131], v[40:43], a[100:103]\n"
"v_exp_f32 v13, v13\n"
"v_mfma_f32_16x16x32_bf16 a[100:103], v[132:135], v[44:47], a[100:103]\n"
"v_cvt_pk_bf16_f32 v73, v10, v11\n"
"v_cvt_pk_bf16_f32 v80, v12, v13\n"
"v_mfma_f32_16x16x32_bf16 a[132:135], v[128:131], v[48:51], a[132:135]\n"
"buffer_load_dwordx4 v[224:227], v185, s[44:47], s72 offen offset:0 nt\n"
"v_mfma_f32_16x16x32_bf16 a[132:135], v[132:135], v[52:55], a[132:135]\n"
"v_exp_f32 v14, v14\n"
"v_mfma_f32_16x16x32_bf16 v[156:159], v[176:179], v[16:19], v[156:159]\n"
"ds_read_b64_tr_b16 v[128:129], v182 offset:256\n"
"v_mfma_f32_16x16x32_bf16 v[156:159], v[176:179], v[20:23], v[156:159]\n"
"s_waitcnt lgkmcnt(7)\n"
"v_mfma_f32_16x16x32_bf16 v[4:7], v[104:107], a[160:163], v[136:139]\n"
"v_exp_f32 v15, v15\n"
"v_mfma_f32_16x16x32_bf16 v[4:7], v[108:111], a[164:167], v[4:7]\n"
"v_cvt_pk_bf16_f32 v81, v14, v15\n"
"s_waitcnt lgkmcnt(1)\n"
"v_mfma_f32_16x16x32_bf16 v[4:7], v[112:115], a[168:171], v[4:7]\n"
"ds_read_b64_tr_b16 v[130:131], v182 offset:4352\n"
"v_mfma_f32_16x16x32_bf16 v[4:7], v[116:119], a[172:175], v[4:7]\n"
"v_exp_f32 v0, v0\n"
"v_mfma_f32_16x16x32_bf16 a[8:11], v[120:123], v[16:19], a[8:11]\n"
"ds_read_b128 v[96:99], v180 offset:24576\n"
"v_mfma_f32_16x16x32_bf16 a[8:11], v[124:127], v[20:23], a[8:11]\n"
"ds_read_b64_tr_b16 v[132:133], v182 offset:8448\n"
"v_mfma_f32_16x16x32_bf16 a[40:43], v[120:123], v[24:27], a[40:43]\n"
"s_add_u32 m0, s53, 0x8000\n"
"v_mfma_f32_16x16x32_bf16 a[40:43], v[124:127], v[28:31], a[40:43]\n"
"ds_read_b64_tr_b16 v[134:135], v182 offset:12544\n"
"v_mfma_f32_16x16x32_bf16 v[8:11], v[104:107], a[176:179], v[140:143]\n"
"v_exp_f32 v1, v1\n"
"v_mfma_f32_16x16x32_bf16 v[8:11], v[108:111], a[180:183], v[8:11]\n"
"v_exp_f32 v2, v2\n"
"v_mfma_f32_16x16x32_bf16 v[8:11], v[112:115], a[184:187], v[8:11]\n"
"v_exp_f32 v3, v3\n"
"v_mfma_f32_16x16x32_bf16 v[8:11], v[116:119], a[188:191], v[8:11]\n"
"v_cvt_pk_bf16_f32 v88, v0, v1\n"
"v_cvt_pk_bf16_f32 v89, v2, v3\n"
"v_mfma_f32_16x16x32_bf16 a[72:75], v[120:123], v[32:35], a[72:75]\n"
"v_exp_f32 v4, v4\n"
"v_mfma_f32_16x16x32_bf16 a[72:75], v[124:127], v[36:39], a[72:75]\n"
"v_exp_f32 v5, v5\n"
"v_mfma_f32_16x16x32_bf16 a[104:107], v[120:123], v[40:43], a[104:107]\n"
"ds_read_b128 v[100:103], v180 offset:24832\n"
"v_cvt_pk_bf16_f32 v58, v4, v5\n"
"v_mfma_f32_16x16x32_bf16 a[104:107], v[124:127], v[44:47], a[104:107]\n"
"buffer_load_dwordx4 v183, s[36:39], s52 offen offset:0 lds\n"
"v_mfma_f32_16x16x32_bf16 v[12:15], v[104:107], a[192:195], v[144:147]\n"
"v_exp_f32 v6, v6\n"
"v_mfma_f32_16x16x32_bf16 v[12:15], v[108:111], a[196:199], v[12:15]\n"
"v_exp_f32 v7, v7\n"
"v_mfma_f32_16x16x32_bf16 v[12:15], v[112:115], a[200:203], v[12:15]\n"
"v_exp_f32 v8, v8\n"
"v_mfma_f32_16x16x32_bf16 v[12:15], v[116:119], a[204:207], v[12:15]\n"
"v_exp_f32 v9, v9\n"
"v_mfma_f32_16x16x32_bf16 a[136:139], v[120:123], v[48:51], a[136:139]\n"
"v_cvt_pk_bf16_f32 v59, v6, v7\n"
"v_cvt_pk_bf16_f32 v66, v8, v9\n"
"v_mfma_f32_16x16x32_bf16 a[136:139], v[124:127], v[52:55], a[136:139]\n"
"s_waitcnt lgkmcnt(1)\n"
"v_mfma_f32_16x16x32_bf16 a[12:15], v[128:131], v[16:19], a[12:15]\n"
"ds_read_b64_tr_b16 v[120:121], v181 offset:512\n"
"v_mfma_f32_16x16x32_bf16 a[12:15], v[132:135], v[20:23], a[12:15]\n"
"buffer_load_dwordx4 v[228:231], v185, s[44:47], s72 offen offset:64 nt\n"
"v_mfma_f32_16x16x32_bf16 v[0:3], v[104:107], a[208:211], v[148:151]\n"
"v_exp_f32 v10, v10\n"
"v_mfma_f32_16x16x32_bf16 v[0:3], v[108:111], a[212:215], v[0:3]\n"
"ds_read_b64_tr_b16 v[122:123], v181 offset:4608\n"
"v_mfma_f32_16x16x32_bf16 v[0:3], v[112:115], a[216:219], v[0:3]\n"
"v_exp_f32 v11, v11\n"
"v_mfma_f32_16x16x32_bf16 v[0:3], v[116:119], a[220:223], v[0:3]\n"
"v_exp_f32 v12, v12\n"
"v_mfma_f32_16x16x32_bf16 a[44:47], v[128:131], v[24:27], a[44:47]\n"
"ds_read_b64_tr_b16 v[124:125], v181 offset:8704\n"
"v_cvt_pk_bf16_f32 v67, v10, v11\n"
"v_mfma_f32_16x16x32_bf16 a[44:47], v[132:135], v[28:31], a[44:47]\n"
"v_exp_f32 v13, v13\n"
"v_mfma_f32_16x16x32_bf16 a[76:79], v[128:131], v[32:35], a[76:79]\n"
"v_exp_f32 v14, v14\n"
"v_mfma_f32_16x16x32_bf16 a[76:79], v[132:135], v[36:39], a[76:79]\n"
"ds_read_b64_tr_b16 v[126:127], v181 offset:12800\n"
"v_cvt_pk_bf16_f32 v74, v12, v13\n"
"v_mfma_f32_16x16x32_bf16 v[4:7], v[112:115], a[232:235], v[152:155]\n"
"ds_read_b128 v[112:115], v180 offset:25088\n"
"v_mfma_f32_16x16x32_bf16 v[4:7], v[116:119], a[236:239], v[4:7]\n"
"ds_read_b128 v[116:119], v180 offset:25344\n"
"v_mfma_f32_16x16x32_bf16 v[4:7], v[104:107], a[224:227], v[4:7]\n"
"buffer_load_dwordx4 v184, s[36:39], s52 offen offset:1024 lds\n"
"v_mfma_f32_16x16x32_bf16 v[4:7], v[108:111], a[228:231], v[4:7]\n"
"v_exp_f32 v15, v15\n"
"v_mfma_f32_16x16x32_bf16 a[108:111], v[128:131], v[40:43], a[108:111]\n"
"v_exp_f32 v0, v0\n"
"v_mfma_f32_16x16x32_bf16 a[108:111], v[132:135], v[44:47], a[108:111]\n"
"v_exp_f32 v1, v1\n"
"v_mfma_f32_16x16x32_bf16 a[140:143], v[128:131], v[48:51], a[140:143]\n"
"v_cvt_pk_bf16_f32 v75, v14, v15\n"
"v_cvt_pk_bf16_f32 v82, v0, v1\n"
"v_mfma_f32_16x16x32_bf16 a[140:143], v[132:135], v[52:55], a[140:143]\n"
"v_exp_f32 v2, v2\n"
"v_mfma_f32_16x16x32_bf16 v[160:163], v[176:179], v[24:27], v[160:163]\n"
"ds_read_b64_tr_b16 v[128:129], v182 offset:512\n"
"v_mfma_f32_16x16x32_bf16 v[160:163], v[176:179], v[28:31], v[160:163]\n"
"s_waitcnt lgkmcnt(7)\n"
"v_mfma_f32_16x16x32_bf16 v[8:11], v[96:99], a[160:163], v[136:139]\n"
"buffer_load_dwordx4 v[232:235], v185, s[44:47], s72 offen offset:128 nt\n"
"v_mfma_f32_16x16x32_bf16 v[8:11], v[100:103], a[164:167], v[8:11]\n"
"s_waitcnt lgkmcnt(1)\n"
"v_mfma_f32_16x16x32_bf16 v[8:11], v[112:115], a[168:171], v[8:11]\n"
"ds_read_b64_tr_b16 v[130:131], v182 offset:4608\n"
"v_mfma_f32_16x16x32_bf16 v[8:11], v[116:119], a[172:175], v[8:11]\n"
"v_exp_f32 v3, v3\n"
"v_mfma_f32_16x16x32_bf16 a[16:19], v[120:123], v[16:19], a[16:19]\n"
"ds_read_b128 v[104:107], v180 offset:28672\n"
"v_cvt_pk_bf16_f32 v83, v2, v3\n"
"v_mfma_f32_16x16x32_bf16 a[16:19], v[124:127], v[20:23], a[16:19]\n"
"ds_read_b64_tr_b16 v[132:133], v182 offset:8704\n"
"v_mfma_f32_16x16x32_bf16 a[48:51], v[120:123], v[24:27], a[48:51]\n"
"v_exp_f32 v4, v4\n"
"v_mfma_f32_16x16x32_bf16 a[48:51], v[124:127], v[28:31], a[48:51]\n"
"ds_read_b64_tr_b16 v[134:135], v182 offset:12800\n"
"v_mfma_f32_16x16x32_bf16 v[12:15], v[96:99], a[176:179], v[140:143]\n"
"buffer_load_dwordx4 v183, s[36:39], s52 offen offset:2048 lds\n"
"v_mfma_f32_16x16x32_bf16 v[12:15], v[100:103], a[180:183], v[12:15]\n"
"v_exp_f32 v5, v5\n"
"v_mfma_f32_16x16x32_bf16 v[12:15], v[112:115], a[184:187], v[12:15]\n"
"v_exp_f32 v6, v6\n"
"v_mfma_f32_16x16x32_bf16 v[12:15], v[116:119], a[188:191], v[12:15]\n"
"v_exp_f32 v7, v7\n"
"v_mfma_f32_16x16x32_bf16 a[80:83], v[120:123], v[32:35], a[80:83]\n"
"v_cvt_pk_bf16_f32 v90, v4, v5\n"
"v_cvt_pk_bf16_f32 v91, v6, v7\n"
"v_mfma_f32_16x16x32_bf16 a[80:83], v[124:127], v[36:39], a[80:83]\n"
"v_exp_f32 v8, v8\n"
"v_mfma_f32_16x16x32_bf16 a[112:115], v[120:123], v[40:43], a[112:115]\n"
"ds_read_b128 v[108:111], v180 offset:28928\n"
"v_mfma_f32_16x16x32_bf16 a[112:115], v[124:127], v[44:47], a[112:115]\n"
"v_exp_f32 v9, v9\n"
"v_mfma_f32_16x16x32_bf16 v[0:3], v[96:99], a[192:195], v[144:147]\n"
"v_exp_f32 v10, v10\n"
"v_mfma_f32_16x16x32_bf16 v[0:3], v[100:103], a[196:199], v[0:3]\n"
"buffer_load_dwordx4 v[236:239], v185, s[44:47], s72 offen offset:192 nt\n"
"v_cvt_pk_bf16_f32 v60, v8, v9\n"
"v_mfma_f32_16x16x32_bf16 v[0:3], v[112:115], a[200:203], v[0:3]\n"
"v_exp_f32 v11, v11\n"
"v_mfma_f32_16x16x32_bf16 v[0:3], v[116:119], a[204:207], v[0:3]\n"
"v_exp_f32 v12, v12\n"
"v_mfma_f32_16x16x32_bf16 a[144:147], v[120:123], v[48:51], a[144:147]\n"
"v_exp_f32 v13, v13\n"
"v_mfma_f32_16x16x32_bf16 a[144:147], v[124:127], v[52:55], a[144:147]\n"
"v_cvt_pk_bf16_f32 v61, v10, v11\n"
"s_waitcnt lgkmcnt(1)\n"
"v_mfma_f32_16x16x32_bf16 a[20:23], v[128:131], v[16:19], a[20:23]\n"
"ds_read_b64_tr_b16 v[120:121], v181 offset:768\n"
"v_cvt_pk_bf16_f32 v68, v12, v13\n"
"v_mfma_f32_16x16x32_bf16 a[20:23], v[132:135], v[20:23], a[20:23]\n"
"v_exp_f32 v14, v14\n"
"v_mfma_f32_16x16x32_bf16 v[172:175], v[176:179], v[48:51], v[172:175]\n"
"v_exp_f32 v15, v15\n"
"v_mfma_f32_16x16x32_bf16 v[172:175], v[176:179], v[52:55], v[172:175]\n"
"ds_read_b64_tr_b16 v[122:123], v181 offset:4864\n"
"v_cvt_pk_bf16_f32 v69, v14, v15\n"
"v_mfma_f32_16x16x32_bf16 v[4:7], v[96:99], a[208:211], v[148:151]\n"
"buffer_load_dwordx4 v184, s[36:39], s52 offen offset:3072 lds\n"
"v_mfma_f32_16x16x32_bf16 v[4:7], v[100:103], a[212:215], v[4:7]\n"
"v_exp_f32 v0, v0\n"
"v_mfma_f32_16x16x32_bf16 v[4:7], v[112:115], a[216:219], v[4:7]\n"
"ds_read_b64_tr_b16 v[124:125], v181 offset:8960\n"
"v_mfma_f32_16x16x32_bf16 v[4:7], v[116:119], a[220:223], v[4:7]\n"
"v_exp_f32 v1, v1\n"
"v_mfma_f32_16x16x32_bf16 a[52:55], v[128:131], v[24:27], a[52:55]\n"
"v_exp_f32 v2, v2\n"
"v_mfma_f32_16x16x32_bf16 a[52:55], v[132:135], v[28:31], a[52:55]\n"
"ds_read_b64_tr_b16 v[126:127], v181 offset:13056\n"
"v_cvt_pk_bf16_f32 v76, v0, v1\n"
"v_mfma_f32_16x16x32_bf16 a[84:87], v[128:131], v[32:35], a[84:87]\n"
"v_exp_f32 v3, v3\n"
"v_mfma_f32_16x16x32_bf16 a[84:87], v[132:135], v[36:39], a[84:87]\n"
"v_cvt_pk_bf16_f32 v77, v2, v3\n"
"v_mfma_f32_16x16x32_bf16 v[8:11], v[112:115], a[232:235], v[152:155]\n"
"ds_read_b128 v[112:115], v180 offset:29184\n"
"v_mfma_f32_16x16x32_bf16 v[8:11], v[116:119], a[236:239], v[8:11]\n"
"ds_read_b128 v[116:119], v180 offset:29440\n"
"v_mfma_f32_16x16x32_bf16 v[8:11], v[96:99], a[224:227], v[8:11]\n"
"buffer_load_dwordx4 v[240:243], v185, s[44:47], s73 offen offset:0 nt\n"
"v_mfma_f32_16x16x32_bf16 v[8:11], v[100:103], a[228:231], v[8:11]\n"
"v_exp_f32 v4, v4\n"
"v_mfma_f32_16x16x32_bf16 a[116:119], v[128:131], v[40:43], a[116:119]\n"
"v_exp_f32 v5, v5\n"
"v_mfma_f32_16x16x32_bf16 a[116:119], v[132:135], v[44:47], a[116:119]\n"
"v_exp_f32 v6, v6\n"
"v_mfma_f32_16x16x32_bf16 a[148:151], v[128:131], v[48:51], a[148:151]\n"
"v_exp_f32 v7, v7\n"
"v_mfma_f32_16x16x32_bf16 a[148:151], v[132:135], v[52:55], a[148:151]\n"
"v_cvt_pk_bf16_f32 v84, v4, v5\n"
"v_cvt_pk_bf16_f32 v85, v6, v7\n"
"v_mfma_f32_16x16x32_bf16 v[164:167], v[176:179], v[32:35], v[164:167]\n"
"ds_read_b64_tr_b16 v[128:129], v182 offset:768\n"
"v_mfma_f32_16x16x32_bf16 v[164:167], v[176:179], v[36:39], v[164:167]\n"
"s_waitcnt lgkmcnt(7)\n"
"v_mfma_f32_16x16x32_bf16 v[12:15], v[104:107], a[160:163], v[136:139]\n"
"s_add_u32 s52, s52, 0x4000\n"
"v_mfma_f32_16x16x32_bf16 v[12:15], v[108:111], a[164:167], v[12:15]\n"
"s_waitcnt lgkmcnt(1)\n"
"v_mfma_f32_16x16x32_bf16 v[12:15], v[112:115], a[168:171], v[12:15]\n"
"ds_read_b64_tr_b16 v[130:131], v182 offset:4864\n"
"v_mfma_f32_16x16x32_bf16 v[12:15], v[116:119], a[172:175], v[12:15]\n"
"v_exp_f32 v8, v8\n"
"v_mfma_f32_16x16x32_bf16 a[24:27], v[120:123], v[16:19], a[24:27]\n"
"ds_read_b64_tr_b16 v[132:133], v182 offset:8960\n"
"v_mfma_f32_16x16x32_bf16 a[24:27], v[124:127], v[20:23], a[24:27]\n"
"v_exp_f32 v9, v9\n"
"v_mfma_f32_16x16x32_bf16 a[56:59], v[120:123], v[24:27], a[56:59]\n"
"v_exp_f32 v10, v10\n"
"v_mfma_f32_16x16x32_bf16 a[56:59], v[124:127], v[28:31], a[56:59]\n"
"ds_read_b64_tr_b16 v[134:135], v182 offset:13056\n"
"v_cvt_pk_bf16_f32 v92, v8, v9\n"
"v_mfma_f32_16x16x32_bf16 v[0:3], v[104:107], a[176:179], v[140:143]\n"
"v_exp_f32 v11, v11\n"
"v_mfma_f32_16x16x32_bf16 v[0:3], v[108:111], a[180:183], v[0:3]\n"
"buffer_load_dwordx4 v[244:247], v185, s[44:47], s73 offen offset:64 nt\n"
"v_cvt_pk_bf16_f32 v93, v10, v11\n"
"v_mfma_f32_16x16x32_bf16 v[0:3], v[112:115], a[184:187], v[0:3]\n"
"v_exp_f32 v12, v12\n"
"v_mfma_f32_16x16x32_bf16 v[0:3], v[116:119], a[188:191], v[0:3]\n"
"v_exp_f32 v13, v13\n"
"v_mfma_f32_16x16x32_bf16 a[88:91], v[120:123], v[32:35], a[88:91]\n"
"v_exp_f32 v14, v14\n"
"v_mfma_f32_16x16x32_bf16 a[88:91], v[124:127], v[36:39], a[88:91]\n"
"v_exp_f32 v15, v15\n"
"v_mfma_f32_16x16x32_bf16 a[120:123], v[120:123], v[40:43], a[120:123]\n"
"v_cvt_pk_bf16_f32 v62, v12, v13\n"
"v_cvt_pk_bf16_f32 v63, v14, v15\n"
"v_mfma_f32_16x16x32_bf16 a[120:123], v[124:127], v[44:47], a[120:123]\n"
"v_mfma_f32_16x16x32_bf16 v[4:7], v[104:107], a[192:195], v[144:147]\n"
"v_exp_f32 v0, v0\n"
"v_mfma_f32_16x16x32_bf16 v[4:7], v[108:111], a[196:199], v[4:7]\n"
"v_exp_f32 v1, v1\n"
"v_mfma_f32_16x16x32_bf16 v[4:7], v[112:115], a[200:203], v[4:7]\n"
"v_exp_f32 v2, v2\n"
"v_mfma_f32_16x16x32_bf16 v[4:7], v[116:119], a[204:207], v[4:7]\n"
"v_exp_f32 v3, v3\n"
"v_mfma_f32_16x16x32_bf16 a[152:155], v[120:123], v[48:51], a[152:155]\n"
"v_cvt_pk_bf16_f32 v70, v0, v1\n"
"v_cvt_pk_bf16_f32 v71, v2, v3\n"
"v_mfma_f32_16x16x32_bf16 a[152:155], v[124:127], v[52:55], a[152:155]\n"
"s_waitcnt vmcnt(15) lgkmcnt(0)\n"
"v_xor_b32 v180, 0x8000, v180\n"
"s_barrier\n"
"v_mfma_f32_16x16x32_bf16 a[28:31], v[128:131], v[16:19], a[28:31]\n"
"ds_read_b128 v[96:99], v180 offset:0\n"
"v_lshlrev_b32 v186, 16, v192\n"
"v_mfma_f32_16x16x32_bf16 a[28:31], v[132:135], v[20:23], a[28:31]\n"
"ds_read_b128 v[100:103], v180 offset:256\n"
"v_and_b32 v192, 0xffff0000, v192\n"
"v_mfma_f32_16x16x32_bf16 v[8:11], v[104:107], a[208:211], v[148:151]\n"
"ds_read_b64_tr_b16 v[120:121], v181 offset:16384\n"
"v_mul_f32 v186, s56, v186\n"
"v_mfma_f32_16x16x32_bf16 v[8:11], v[108:111], a[212:215], v[8:11]\n"
"ds_read_b64_tr_b16 v[122:123], v181 offset:20480\n"
"v_mul_f32 v192, s56, v192\n"
"v_mfma_f32_16x16x32_bf16 v[8:11], v[112:115], a[216:219], v[8:11]\n"
"ds_read_b64_tr_b16 v[124:125], v181 offset:24576\n"
"v_cvt_pk_bf16_f32 v192, v186, v192\n"
"v_mfma_f32_16x16x32_bf16 v[8:11], v[116:119], a[220:223], v[8:11]\n"
"ds_read_b64_tr_b16 v[126:127], v181 offset:28672\n"
"v_lshlrev_b32 v188, 16, v193\n"
"v_mfma_f32_16x16x32_bf16 a[60:63], v[128:131], v[24:27], a[60:63]\n"
"s_add_u32 m0, s54, 0x8000\n"
"v_and_b32 v193, 0xffff0000, v193\n"
"v_mfma_f32_16x16x32_bf16 a[60:63], v[132:135], v[28:31], a[60:63]\n"
"v_exp_f32 v4, v4\n"
"v_mfma_f32_16x16x32_bf16 a[92:95], v[128:131], v[32:35], a[92:95]\n"
"v_exp_f32 v5, v5\n"
"v_mfma_f32_16x16x32_bf16 a[92:95], v[132:135], v[36:39], a[92:95]\n"
"buffer_load_dwordx4 v183, s[40:43], s52 offen offset:0 lds\n"
"v_cvt_pk_bf16_f32 v78, v4, v5\n"
"v_mfma_f32_16x16x32_bf16 v[12:15], v[112:115], a[232:235], v[152:155]\n"
"ds_read_b128 v[112:115], v180 offset:512\n"
"v_mul_f32 v188, s56, v188\n"
"v_mfma_f32_16x16x32_bf16 v[12:15], v[116:119], a[236:239], v[12:15]\n"
"ds_read_b128 v[116:119], v180 offset:768\n"
"v_mul_f32 v193, s56, v193\n"
"v_mfma_f32_16x16x32_bf16 v[12:15], v[104:107], a[224:227], v[12:15]\n"
"v_exp_f32 v6, v6\n"
"v_mfma_f32_16x16x32_bf16 v[12:15], v[108:111], a[228:231], v[12:15]\n"
"v_exp_f32 v7, v7\n"
"v_mfma_f32_16x16x32_bf16 a[124:127], v[128:131], v[40:43], a[124:127]\n"
"v_exp_f32 v8, v8\n"
"v_mfma_f32_16x16x32_bf16 a[124:127], v[132:135], v[44:47], a[124:127]\n"
"v_exp_f32 v9, v9\n"
"v_mfma_f32_16x16x32_bf16 a[156:159], v[128:131], v[48:51], a[156:159]\n"
"v_cvt_pk_bf16_f32 v79, v6, v7\n"
"v_cvt_pk_bf16_f32 v86, v8, v9\n"
"v_mfma_f32_16x16x32_bf16 a[156:159], v[132:135], v[52:55], a[156:159]\n"
"v_exp_f32 v10, v10\n"
"v_mfma_f32_16x16x32_bf16 v[168:171], v[176:179], v[40:43], v[168:171]\n"
"v_exp_f32 v11, v11\n"
"v_mfma_f32_16x16x32_bf16 v[168:171], v[176:179], v[44:47], v[168:171]\n"
"v_cvt_pk_bf16_f32 v87, v10, v11\n"
"s_waitcnt lgkmcnt(2)\n"
"v_mfma_f32_16x16x32_bf16 v[0:3], v[96:99], a[160:163], v[136:139]\n"
"v_exp_f32 v12, v12\n"
"v_mfma_f32_16x16x32_bf16 v[0:3], v[100:103], a[164:167], v[0:3]\n"
"v_cvt_pk_bf16_f32 v193, v188, v193\n"
"s_waitcnt lgkmcnt(0)\n"
"v_mfma_f32_16x16x32_bf16 v[0:3], v[112:115], a[168:171], v[0:3]\n"
"buffer_load_dwordx4 v184, s[40:43], s52 offen offset:1024 lds\n"
"v_lshlrev_b32 v186, 16, v194\n"
"v_mfma_f32_16x16x32_bf16 v[0:3], v[116:119], a[172:175], v[0:3]\n"
"v_exp_f32 v13, v13\n"
"v_mfma_f32_16x16x32_bf16 a[0:3], v[120:123], v[56:59], a[0:3]\n"
"ds_read_b128 v[104:107], v180 offset:4096\n"
"v_cvt_pk_bf16_f32 v94, v12, v13\n"
"v_mfma_f32_16x16x32_bf16 a[0:3], v[124:127], v[60:63], a[0:3]\n"
"ds_read_b64_tr_b16 v[128:129], v182 offset:16384\n"
"v_and_b32 v194, 0xffff0000, v194\n"
"v_mfma_f32_16x16x32_bf16 a[32:35], v[120:123], v[64:67], a[32:35]\n"
"v_exp_f32 v14, v14\n"
"v_mfma_f32_16x16x32_bf16 a[32:35], v[124:127], v[68:71], a[32:35]\n"
"ds_read_b64_tr_b16 v[130:131], v182 offset:20480\n"
"v_mul_f32 v186, s56, v186\n"
"v_mfma_f32_16x16x32_bf16 v[4:7], v[96:99], a[176:179], v[140:143]\n"
"v_exp_f32 v15, v15\n"
"v_mfma_f32_16x16x32_bf16 v[4:7], v[100:103], a[180:183], v[4:7]\n"
"ds_read_b64_tr_b16 v[132:133], v182 offset:24576\n"
"v_cvt_pk_bf16_f32 v95, v14, v15\n"
"v_mfma_f32_16x16x32_bf16 v[4:7], v[112:115], a[184:187], v[4:7]\n"
"v_exp_f32 v0, v0\n"
"v_mfma_f32_16x16x32_bf16 v[4:7], v[116:119], a[188:191], v[4:7]\n"
"ds_read_b64_tr_b16 v[134:135], v182 offset:28672\n"
"v_mul_f32 v194, s56, v194\n"
"v_mfma_f32_16x16x32_bf16 a[64:67], v[120:123], v[72:75], a[64:67]\n"
"buffer_load_dwordx4 v183, s[40:43], s52 offen offset:2048 lds\n"
"v_cvt_pk_bf16_f32 v194, v186, v194\n"
"v_mfma_f32_16x16x32_bf16 a[64:67], v[124:127], v[76:79], a[64:67]\n"
"v_exp_f32 v1, v1\n"
"v_mfma_f32_16x16x32_bf16 a[96:99], v[120:123], v[80:83], a[96:99]\n"
"ds_read_b128 v[108:111], v180 offset:4352\n"
"v_cvt_pk_bf16_f32 v16, v0, v1\n"
"v_mfma_f32_16x16x32_bf16 a[96:99], v[124:127], v[84:87], a[96:99]\n"
"v_exp_f32 v2, v2\n"
"v_mfma_f32_16x16x32_bf16 v[8:11], v[96:99], a[192:195], v[144:147]\n"
"v_exp_f32 v3, v3\n"
"v_mfma_f32_16x16x32_bf16 v[8:11], v[100:103], a[196:199], v[8:11]\n"
"v_exp_f32 v4, v4\n"
"v_mfma_f32_16x16x32_bf16 v[8:11], v[112:115], a[200:203], v[8:11]\n"
"v_exp_f32 v5, v5\n"
"v_mfma_f32_16x16x32_bf16 v[8:11], v[116:119], a[204:207], v[8:11]\n"
"v_cvt_pk_bf16_f32 v17, v2, v3\n"
"v_cvt_pk_bf16_f32 v24, v4, v5\n"
"v_mfma_f32_16x16x32_bf16 a[128:131], v[120:123], v[88:91], a[128:131]\n"
"v_exp_f32 v6, v6\n"
"v_mfma_f32_16x16x32_bf16 a[128:131], v[124:127], v[92:95], a[128:131]\n"
"v_lshlrev_b32 v188, 16, v195\n"
"s_waitcnt lgkmcnt(1)\n"
"v_mfma_f32_16x16x32_bf16 a[4:7], v[128:131], v[56:59], a[4:7]\n"
"ds_read_b64_tr_b16 v[120:121], v181 offset:16640\n"
"v_and_b32 v195, 0xffff0000, v195\n"
"v_mfma_f32_16x16x32_bf16 a[4:7], v[132:135], v[60:63], a[4:7]\n"
"buffer_load_dwordx4 v[248:251], v185, s[44:47], s73 offen offset:128 nt\n"
"v_mul_f32 v188, s56, v188\n"
"v_mfma_f32_16x16x32_bf16 v[12:15], v[96:99], a[208:211], v[148:151]\n"
"v_exp_f32 v7, v7\n"
"v_mfma_f32_16x16x32_bf16 v[12:15], v[100:103], a[212:215], v[12:15]\n"
"ds_read_b64_tr_b16 v[122:123], v181 offset:20736\n"
"v_cvt_pk_bf16_f32 v25, v6, v7\n"
"v_mfma_f32_16x16x32_bf16 v[12:15], v[112:115], a[216:219], v[12:15]\n"
"v_exp_f32 v8, v8\n"
"v_mfma_f32_16x16x32_bf16 v[12:15], v[116:119], a[220:223], v[12:15]\n"
"v_exp_f32 v9, v9\n"
"v_mfma_f32_16x16x32_bf16 a[36:39], v[128:131], v[64:67], a[36:39]\n"
"ds_read_b64_tr_b16 v[124:125], v181 offset:24832\n"
"v_cvt_pk_bf16_f32 v32, v8, v9\n"
"v_mfma_f32_16x16x32_bf16 a[36:39], v[132:135], v[68:71], a[36:39]\n"
"v_exp_f32 v10, v10\n"
"v_mfma_f32_16x16x32_bf16 a[68:71], v[128:131], v[72:75], a[68:71]\n"
"v_exp_f32 v11, v11\n"
"v_mfma_f32_16x16x32_bf16 a[68:71], v[132:135], v[76:79], a[68:71]\n"
"ds_read_b64_tr_b16 v[126:127], v181 offset:28928\n"
"v_cvt_pk_bf16_f32 v33, v10, v11\n"
"v_mfma_f32_16x16x32_bf16 v[0:3], v[112:115], a[232:235], v[152:155]\n"
"ds_read_b128 v[112:115], v180 offset:4608\n"
"v_mul_f32 v195, s56, v195\n"
"v_mfma_f32_16x16x32_bf16 v[0:3], v[116:119], a[236:239], v[0:3]\n"
"ds_read_b128 v[116:119], v180 offset:4864\n"
"v_cvt_pk_bf16_f32 v195, v188, v195\n"
"v_mfma_f32_16x16x32_bf16 v[0:3], v[96:99], a[224:227], v[0:3]\n"
"buffer_load_dwordx4 v184, s[40:43], s52 offen offset:3072 lds\n"
"v_lshlrev_b32 v186, 16, v196\n"
"v_mfma_f32_16x16x32_bf16 v[0:3], v[100:103], a[228:231], v[0:3]\n"
"v_exp_f32 v12, v12\n"
"v_mfma_f32_16x16x32_bf16 a[100:103], v[128:131], v[80:83], a[100:103]\n"
"v_exp_f32 v13, v13\n"
"v_mfma_f32_16x16x32_bf16 a[100:103], v[132:135], v[84:87], a[100:103]\n"
"v_exp_f32 v14, v14\n"
"v_mfma_f32_16x16x32_bf16 a[132:135], v[128:131], v[88:91], a[132:135]\n"
"v_exp_f32 v15, v15\n"
"v_mfma_f32_16x16x32_bf16 a[132:135], v[132:135], v[92:95], a[132:135]\n"
"v_cvt_pk_bf16_f32 v40, v12, v13\n"
"v_cvt_pk_bf16_f32 v41, v14, v15\n"
"v_mfma_f32_16x16x32_bf16 v[156:159], v[176:179], v[56:59], v[156:159]\n"
"ds_read_b64_tr_b16 v[128:129], v182 offset:16640\n"
"v_and_b32 v196, 0xffff0000, v196\n"
"v_mfma_f32_16x16x32_bf16 v[156:159], v[176:179], v[60:63], v[156:159]\n"
"v_mul_f32 v186, s56, v186\n"
"s_waitcnt lgkmcnt(7)\n"
"v_mfma_f32_16x16x32_bf16 v[4:7], v[104:107], a[160:163], v[136:139]\n"
"buffer_load_dwordx4 v[252:255], v185, s[44:47], s73 offen offset:192 nt\n"
"v_mul_f32 v196, s56, v196\n"
"v_mfma_f32_16x16x32_bf16 v[4:7], v[108:111], a[164:167], v[4:7]\n"
"v_cvt_pk_bf16_f32 v196, v186, v196\n"
"s_waitcnt lgkmcnt(1)\n"
"v_mfma_f32_16x16x32_bf16 v[4:7], v[112:115], a[168:171], v[4:7]\n"
"ds_read_b64_tr_b16 v[130:131], v182 offset:20736\n"
"v_lshlrev_b32 v188, 16, v197\n"
"v_mfma_f32_16x16x32_bf16 v[4:7], v[116:119], a[172:175], v[4:7]\n"
"v_exp_f32 v0, v0\n"
"v_mfma_f32_16x16x32_bf16 a[8:11], v[120:123], v[56:59], a[8:11]\n"
"ds_read_b128 v[96:99], v180 offset:8192\n"
"v_and_b32 v197, 0xffff0000, v197\n"
"v_mfma_f32_16x16x32_bf16 a[8:11], v[124:127], v[60:63], a[8:11]\n"
"ds_read_b64_tr_b16 v[132:133], v182 offset:24832\n"
"v_mul_f32 v188, s56, v188\n"
"v_mfma_f32_16x16x32_bf16 a[40:43], v[120:123], v[64:67], a[40:43]\n"
"v_exp_f32 v1, v1\n"
"v_mfma_f32_16x16x32_bf16 a[40:43], v[124:127], v[68:71], a[40:43]\n"
"ds_read_b64_tr_b16 v[134:135], v182 offset:28928\n"
"v_cvt_pk_bf16_f32 v48, v0, v1\n"
"v_mfma_f32_16x16x32_bf16 v[8:11], v[104:107], a[176:179], v[140:143]\n"
"s_add_u32 m0, s53, 0xc000\n"
"v_mul_f32 v197, s56, v197\n"
"v_mfma_f32_16x16x32_bf16 v[8:11], v[108:111], a[180:183], v[8:11]\n"
"v_exp_f32 v2, v2\n"
"v_mfma_f32_16x16x32_bf16 v[8:11], v[112:115], a[184:187], v[8:11]\n"
"v_exp_f32 v3, v3\n"
"v_mfma_f32_16x16x32_bf16 v[8:11], v[116:119], a[188:191], v[8:11]\n"
"v_exp_f32 v4, v4\n"
"v_mfma_f32_16x16x32_bf16 a[72:75], v[120:123], v[72:75], a[72:75]\n"
"v_exp_f32 v5, v5\n"
"v_mfma_f32_16x16x32_bf16 a[72:75], v[124:127], v[76:79], a[72:75]\n"
"v_cvt_pk_bf16_f32 v49, v2, v3\n"
"v_cvt_pk_bf16_f32 v18, v4, v5\n"
"v_mfma_f32_16x16x32_bf16 a[104:107], v[120:123], v[80:83], a[104:107]\n"
"ds_read_b128 v[100:103], v180 offset:8448\n"
"v_cvt_pk_bf16_f32 v197, v188, v197\n"
"v_mfma_f32_16x16x32_bf16 a[104:107], v[124:127], v[84:87], a[104:107]\n"
"v_exp_f32 v6, v6\n"
"v_mfma_f32_16x16x32_bf16 v[12:15], v[104:107], a[192:195], v[144:147]\n"
"v_exp_f32 v7, v7\n"
"v_mfma_f32_16x16x32_bf16 v[12:15], v[108:111], a[196:199], v[12:15]\n"
"v_exp_f32 v8, v8\n"
"v_mfma_f32_16x16x32_bf16 v[12:15], v[112:115], a[200:203], v[12:15]\n"
"buffer_load_dwordx4 v183, s[36:39], s52 offen offset:0 lds\n"
"v_cvt_pk_bf16_f32 v19, v6, v7\n"
"v_mfma_f32_16x16x32_bf16 v[12:15], v[116:119], a[204:207], v[12:15]\n"
"v_exp_f32 v9, v9\n"
"v_mfma_f32_16x16x32_bf16 a[136:139], v[120:123], v[88:91], a[136:139]\n"
"v_exp_f32 v10, v10\n"
"v_mfma_f32_16x16x32_bf16 a[136:139], v[124:127], v[92:95], a[136:139]\n"
"v_cvt_pk_bf16_f32 v26, v8, v9\n"
"s_waitcnt lgkmcnt(1)\n"
"v_mfma_f32_16x16x32_bf16 a[12:15], v[128:131], v[56:59], a[12:15]\n"
"ds_read_b64_tr_b16 v[120:121], v181 offset:16896\n"
"v_lshlrev_b32 v186, 16, v198\n"
"v_mfma_f32_16x16x32_bf16 a[12:15], v[132:135], v[60:63], a[12:15]\n"
"v_exp_f32 v11, v11\n"
"v_mfma_f32_16x16x32_bf16 v[0:3], v[104:107], a[208:211], v[148:151]\n"
"v_exp_f32 v12, v12\n"
"v_mfma_f32_16x16x32_bf16 v[0:3], v[108:111], a[212:215], v[0:3]\n"
"ds_read_b64_tr_b16 v[122:123], v181 offset:20992\n"
"v_cvt_pk_bf16_f32 v27, v10, v11\n"
"v_mfma_f32_16x16x32_bf16 v[0:3], v[112:115], a[216:219], v[0:3]\n"
"v_exp_f32 v13, v13\n"
"v_mfma_f32_16x16x32_bf16 v[0:3], v[116:119], a[220:223], v[0:3]\n"
"buffer_load_dwordx4 a[240:243], v185, s[44:47], s74 offen offset:0 nt\n"
"v_cvt_pk_bf16_f32 v34, v12, v13\n"
"v_mfma_f32_16x16x32_bf16 a[44:47], v[128:131], v[64:67], a[44:47]\n"
"ds_read_b64_tr_b16 v[124:125], v181 offset:25088\n"
"v_and_b32 v198, 0xffff0000, v198\n"
"v_mfma_f32_16x16x32_bf16 a[44:47], v[132:135], v[68:71], a[44:47]\n"
"v_exp_f32 v14, v14\n"
"v_mfma_f32_16x16x32_bf16 a[76:79], v[128:131], v[72:75], a[76:79]\n"
"v_exp_f32 v15, v15\n"
"v_mfma_f32_16x16x32_bf16 a[76:79], v[132:135], v[76:79], a[76:79]\n"
"ds_read_b64_tr_b16 v[126:127], v181 offset:29184\n"
"v_cvt_pk_bf16_f32 v35, v14, v15\n"
"v_mfma_f32_16x16x32_bf16 v[4:7], v[112:115], a[232:235], v[152:155]\n"
"ds_read_b128 v[112:115], v180 offset:8704\n"
"v_mul_f32 v186, s56, v186\n"
"v_mfma_f32_16x16x32_bf16 v[4:7], v[116:119], a[236:239], v[4:7]\n"
"ds_read_b128 v[116:119], v180 offset:8960\n"
"v_mul_f32 v198, s56, v198\n"
"v_mfma_f32_16x16x32_bf16 v[4:7], v[104:107], a[224:227], v[4:7]\n"
"v_exp_f32 v0, v0\n"
"v_mfma_f32_16x16x32_bf16 v[4:7], v[108:111], a[228:231], v[4:7]\n"
"v_exp_f32 v1, v1\n"
"v_mfma_f32_16x16x32_bf16 a[108:111], v[128:131], v[80:83], a[108:111]\n"
"v_exp_f32 v2, v2\n"
"v_mfma_f32_16x16x32_bf16 a[108:111], v[132:135], v[84:87], a[108:111]\n"
"buffer_load_dwordx4 v184, s[36:39], s52 offen offset:1024 lds\n"
"v_cvt_pk_bf16_f32 v42, v0, v1\n"
"v_mfma_f32_16x16x32_bf16 a[140:143], v[128:131], v[88:91], a[140:143]\n"
"v_exp_f32 v3, v3\n"
"v_mfma_f32_16x16x32_bf16 a[140:143], v[132:135], v[92:95], a[140:143]\n"
"v_cvt_pk_bf16_f32 v43, v2, v3\n"
"v_cvt_pk_bf16_f32 v198, v186, v198\n"
"v_mfma_f32_16x16x32_bf16 v[160:163], v[176:179], v[64:67], v[160:163]\n"
"ds_read_b64_tr_b16 v[128:129], v182 offset:16896\n"
"v_lshlrev_b32 v188, 16, v199\n"
"v_mfma_f32_16x16x32_bf16 v[160:163], v[176:179], v[68:71], v[160:163]\n"
"v_and_b32 v199, 0xffff0000, v199\n"
"s_waitcnt lgkmcnt(7)\n"
"v_mfma_f32_16x16x32_bf16 v[8:11], v[96:99], a[160:163], v[136:139]\n"
"v_exp_f32 v4, v4\n"
"v_mfma_f32_16x16x32_bf16 v[8:11], v[100:103], a[164:167], v[8:11]\n"
"v_mul_f32 v188, s56, v188\n"
"s_waitcnt lgkmcnt(1)\n"
"v_mfma_f32_16x16x32_bf16 v[8:11], v[112:115], a[168:171], v[8:11]\n"
"ds_read_b64_tr_b16 v[130:131], v182 offset:20992\n"
"v_mul_f32 v199, s56, v199\n"
"v_mfma_f32_16x16x32_bf16 v[8:11], v[116:119], a[172:175], v[8:11]\n"
"v_exp_f32 v5, v5\n"
"v_mfma_f32_16x16x32_bf16 a[16:19], v[120:123], v[56:59], a[16:19]\n"
"ds_read_b128 v[104:107], v180 offset:12288\n"
"v_cvt_pk_bf16_f32 v50, v4, v5\n"
"v_mfma_f32_16x16x32_bf16 a[16:19], v[124:127], v[60:63], a[16:19]\n"
"ds_read_b64_tr_b16 v[132:133], v182 offset:25088\n"
"v_cvt_pk_bf16_f32 v199, v188, v199\n"
"v_mfma_f32_16x16x32_bf16 a[48:51], v[120:123], v[64:67], a[48:51]\n"
"buffer_load_dwordx4 a[244:247], v185, s[44:47], s74 offen offset:64 nt\n"
"v_lshlrev_b32 v186, 16, v200\n"
"v_mfma_f32_16x16x32_bf16 a[48:51], v[124:127], v[68:71], a[48:51]\n"
"ds_read_b64_tr_b16 v[134:135], v182 offset:29184\n"
"v_and_b32 v200, 0xffff0000, v200\n"
"v_mfma_f32_16x16x32_bf16 v[12:15], v[96:99], a[176:179], v[140:143]\n"
"v_exp_f32 v6, v6\n"
"v_mfma_f32_16x16x32_bf16 v[12:15], v[100:103], a[180:183], v[12:15]\n"
"v_exp_f32 v7, v7\n"
"v_mfma_f32_16x16x32_bf16 v[12:15], v[112:115], a[184:187], v[12:15]\n"
"v_exp_f32 v8, v8\n"
"v_mfma_f32_16x16x32_bf16 v[12:15], v[116:119], a[188:191], v[12:15]\n"
"v_exp_f32 v9, v9\n"
"v_mfma_f32_16x16x32_bf16 a[80:83], v[120:123], v[72:75], a[80:83]\n"
"v_cvt_pk_bf16_f32 v51, v6, v7\n"
"v_cvt_pk_bf16_f32 v20, v8, v9\n"
"v_mfma_f32_16x16x32_bf16 a[80:83], v[124:127], v[76:79], a[80:83]\n"
"v_exp_f32 v10, v10\n"
"v_mfma_f32_16x16x32_bf16 a[112:115], v[120:123], v[80:83], a[112:115]\n"
"ds_read_b128 v[108:111], v180 offset:12544\n"
"v_mul_f32 v186, s56, v186\n"
"v_mfma_f32_16x16x32_bf16 a[112:115], v[124:127], v[84:87], a[112:115]\n"
"buffer_load_dwordx4 v183, s[36:39], s52 offen offset:2048 lds\n"
"v_mul_f32 v200, s56, v200\n"
"v_mfma_f32_16x16x32_bf16 v[0:3], v[96:99], a[192:195], v[144:147]\n"
"v_exp_f32 v11, v11\n"
"v_mfma_f32_16x16x32_bf16 v[0:3], v[100:103], a[196:199], v[0:3]\n"
"v_exp_f32 v12, v12\n"
"v_mfma_f32_16x16x32_bf16 v[0:3], v[112:115], a[200:203], v[0:3]\n"
"v_exp_f32 v13, v13\n"
"v_mfma_f32_16x16x32_bf16 v[0:3], v[116:119], a[204:207], v[0:3]\n"
"v_cvt_pk_bf16_f32 v21, v10, v11\n"
"v_cvt_pk_bf16_f32 v28, v12, v13\n"
"v_mfma_f32_16x16x32_bf16 a[144:147], v[120:123], v[88:91], a[144:147]\n"
"v_exp_f32 v14, v14\n"
"v_mfma_f32_16x16x32_bf16 a[144:147], v[124:127], v[92:95], a[144:147]\n"
"v_cvt_pk_bf16_f32 v200, v186, v200\n"
"s_waitcnt lgkmcnt(1)\n"
"v_mfma_f32_16x16x32_bf16 a[20:23], v[128:131], v[56:59], a[20:23]\n"
"ds_read_b64_tr_b16 v[120:121], v181 offset:17152\n"
"v_lshlrev_b32 v188, 16, v201\n"
"v_mfma_f32_16x16x32_bf16 a[20:23], v[132:135], v[60:63], a[20:23]\n"
"v_exp_f32 v15, v15\n"
"v_mfma_f32_16x16x32_bf16 v[172:175], v[176:179], v[88:91], v[172:175]\n"
"buffer_load_dwordx4 v184, s[36:39], s52 offen offset:3072 lds\n"
"v_cvt_pk_bf16_f32 v29, v14, v15\n"
"v_mfma_f32_16x16x32_bf16 v[172:175], v[176:179], v[92:95], v[172:175]\n"
"ds_read_b64_tr_b16 v[122:123], v181 offset:21248\n"
"v_and_b32 v201, 0xffff0000, v201\n"
"v_mfma_f32_16x16x32_bf16 v[4:7], v[96:99], a[208:211], v[148:151]\n"
"v_exp_f32 v0, v0\n"
"v_mfma_f32_16x16x32_bf16 v[4:7], v[100:103], a[212:215], v[4:7]\n"
"v_exp_f32 v1, v1\n"
"v_mfma_f32_16x16x32_bf16 v[4:7], v[112:115], a[216:219], v[4:7]\n"
"ds_read_b64_tr_b16 v[124:125], v181 offset:25344\n"
"v_cvt_pk_bf16_f32 v36, v0, v1\n"
"v_mfma_f32_16x16x32_bf16 v[4:7], v[116:119], a[220:223], v[4:7]\n"
"v_exp_f32 v2, v2\n"
"v_mfma_f32_16x16x32_bf16 a[52:55], v[128:131], v[64:67], a[52:55]\n"
"v_exp_f32 v3, v3\n"
"v_mfma_f32_16x16x32_bf16 a[52:55], v[132:135], v[68:71], a[52:55]\n"
"ds_read_b64_tr_b16 v[126:127], v181 offset:29440\n"
"v_cvt_pk_bf16_f32 v37, v2, v3\n"
"v_mfma_f32_16x16x32_bf16 a[84:87], v[128:131], v[72:75], a[84:87]\n"
"v_mul_f32 v188, s56, v188\n"
"v_mul_f32 v201, s56, v201\n"
"v_mfma_f32_16x16x32_bf16 a[84:87], v[132:135], v[76:79], a[84:87]\n"
"v_cvt_pk_bf16_f32 v201, v188, v201\n"
"v_lshlrev_b32 v186, 16, v202\n"
"v_mfma_f32_16x16x32_bf16 v[8:11], v[112:115], a[232:235], v[152:155]\n"
"ds_read_b128 v[112:115], v180 offset:12800\n"
"v_and_b32 v202, 0xffff0000, v202\n"
"v_mfma_f32_16x16x32_bf16 v[8:11], v[116:119], a[236:239], v[8:11]\n"
"ds_read_b128 v[116:119], v180 offset:13056\n"
"v_mul_f32 v186, s56, v186\n"
"v_mfma_f32_16x16x32_bf16 v[8:11], v[96:99], a[224:227], v[8:11]\n"
"buffer_load_dwordx4 a[248:251], v185, s[44:47], s74 offen offset:128 nt\n"
"v_mul_f32 v202, s56, v202\n"
"v_mfma_f32_16x16x32_bf16 v[8:11], v[100:103], a[228:231], v[8:11]\n"
"v_exp_f32 v4, v4\n"
"v_mfma_f32_16x16x32_bf16 a[116:119], v[128:131], v[80:83], a[116:119]\n"
"v_exp_f32 v5, v5\n"
"v_mfma_f32_16x16x32_bf16 a[116:119], v[132:135], v[84:87], a[116:119]\n"
"v_exp_f32 v6, v6\n"
"v_mfma_f32_16x16x32_bf16 a[148:151], v[128:131], v[88:91], a[148:151]\n"
"v_exp_f32 v7, v7\n"
"v_mfma_f32_16x16x32_bf16 a[148:151], v[132:135], v[92:95], a[148:151]\n"
"v_cvt_pk_bf16_f32 v44, v4, v5\n"
"v_cvt_pk_bf16_f32 v45, v6, v7\n"
"v_mfma_f32_16x16x32_bf16 v[164:167], v[176:179], v[72:75], v[164:167]\n"
"ds_read_b64_tr_b16 v[128:129], v182 offset:17152\n"
"v_cvt_pk_bf16_f32 v202, v186, v202\n"
"v_mfma_f32_16x16x32_bf16 v[164:167], v[176:179], v[76:79], v[164:167]\n"
"v_lshlrev_b32 v188, 16, v203\n"
"s_waitcnt lgkmcnt(7)\n"
"v_mfma_f32_16x16x32_bf16 v[12:15], v[104:107], a[160:163], v[136:139]\n"
"s_add_u32 s52, s52, 0x4000\n"
"v_and_b32 v203, 0xffff0000, v203\n"
"v_mfma_f32_16x16x32_bf16 v[12:15], v[108:111], a[164:167], v[12:15]\n"
"v_mul_f32 v188, s56, v188\n"
"s_waitcnt lgkmcnt(1)\n"
"v_mfma_f32_16x16x32_bf16 v[12:15], v[112:115], a[168:171], v[12:15]\n"
"ds_read_b64_tr_b16 v[130:131], v182 offset:21248\n"
"v_mul_f32 v203, s56, v203\n"
"v_mfma_f32_16x16x32_bf16 v[12:15], v[116:119], a[172:175], v[12:15]\n"
"v_exp_f32 v8, v8\n"
"v_mfma_f32_16x16x32_bf16 a[24:27], v[120:123], v[56:59], a[24:27]\n"
"ds_read_b64_tr_b16 v[132:133], v182 offset:25344\n"
"v_cvt_pk_bf16_f32 v203, v188, v203\n"
"v_mfma_f32_16x16x32_bf16 a[24:27], v[124:127], v[60:63], a[24:27]\n"
"v_exp_f32 v9, v9\n"
"v_mfma_f32_16x16x32_bf16 a[56:59], v[120:123], v[64:67], a[56:59]\n"
"v_exp_f32 v10, v10\n"
"v_mfma_f32_16x16x32_bf16 a[56:59], v[124:127], v[68:71], a[56:59]\n"
"ds_read_b64_tr_b16 v[134:135], v182 offset:29440\n"
"v_cvt_pk_bf16_f32 v52, v8, v9\n"
"v_mfma_f32_16x16x32_bf16 v[0:3], v[104:107], a[176:179], v[140:143]\n"
"v_exp_f32 v11, v11\n"
"v_mfma_f32_16x16x32_bf16 v[0:3], v[108:111], a[180:183], v[0:3]\n"
"buffer_load_dwordx4 a[252:255], v185, s[44:47], s74 offen offset:192 nt\n"
"v_cvt_pk_bf16_f32 v53, v10, v11\n"
"v_mfma_f32_16x16x32_bf16 v[0:3], v[112:115], a[184:187], v[0:3]\n"
"v_exp_f32 v12, v12\n"
"v_mfma_f32_16x16x32_bf16 v[0:3], v[116:119], a[188:191], v[0:3]\n"
"v_exp_f32 v13, v13\n"
"v_mfma_f32_16x16x32_bf16 a[88:91], v[120:123], v[72:75], a[88:91]\n"
"v_exp_f32 v14, v14\n"
"v_mfma_f32_16x16x32_bf16 a[88:91], v[124:127], v[76:79], a[88:91]\n"
"v_exp_f32 v15, v15\n"
"v_mfma_f32_16x16x32_bf16 a[120:123], v[120:123], v[80:83], a[120:123]\n"
"v_cvt_pk_bf16_f32 v22, v12, v13\n"
"v_cvt_pk_bf16_f32 v23, v14, v15\n"
"v_mfma_f32_16x16x32_bf16 a[120:123], v[124:127], v[84:87], a[120:123]\n"
"v_lshlrev_b32 v186, 16, v204\n"
"v_and_b32 v204, 0xffff0000, v204\n"
"v_mfma_f32_16x16x32_bf16 v[4:7], v[104:107], a[192:195], v[144:147]\n"
"v_exp_f32 v0, v0\n"
"v_mfma_f32_16x16x32_bf16 v[4:7], v[108:111], a[196:199], v[4:7]\n"
"v_exp_f32 v1, v1\n"
"v_mfma_f32_16x16x32_bf16 v[4:7], v[112:115], a[200:203], v[4:7]\n"
"v_exp_f32 v2, v2\n"
"v_mfma_f32_16x16x32_bf16 v[4:7], v[116:119], a[204:207], v[4:7]\n"
"v_exp_f32 v3, v3\n"
"v_mfma_f32_16x16x32_bf16 a[152:155], v[120:123], v[88:91], a[152:155]\n"
"v_cvt_pk_bf16_f32 v30, v0, v1\n"
"v_cvt_pk_bf16_f32 v31, v2, v3\n"
"v_mfma_f32_16x16x32_bf16 a[152:155], v[124:127], v[92:95], a[152:155]\n"
"s_waitcnt vmcnt(14) lgkmcnt(0)\n"
"v_xor_b32 v181, 0x8000, v181\n"
"v_xor_b32 v182, 0x8000, v182\n"
"s_barrier\n"
"v_mfma_f32_16x16x32_bf16 a[28:31], v[128:131], v[56:59], a[28:31]\n"
"ds_read_b128 v[96:99], v180 offset:16384\n"
"v_mul_f32 v186, s56, v186\n"
"v_mfma_f32_16x16x32_bf16 a[28:31], v[132:135], v[60:63], a[28:31]\n"
"ds_read_b128 v[100:103], v180 offset:16640\n"
"v_mul_f32 v204, s56, v204\n"
"v_mfma_f32_16x16x32_bf16 v[8:11], v[104:107], a[208:211], v[148:151]\n"
"ds_read_b64_tr_b16 v[120:121], v181 offset:0\n"
"v_cvt_pk_bf16_f32 v204, v186, v204\n"
"v_mfma_f32_16x16x32_bf16 v[8:11], v[108:111], a[212:215], v[8:11]\n"
"ds_read_b64_tr_b16 v[122:123], v181 offset:4096\n"
"v_lshlrev_b32 v188, 16, v205\n"
"v_mfma_f32_16x16x32_bf16 v[8:11], v[112:115], a[216:219], v[8:11]\n"
"ds_read_b64_tr_b16 v[124:125], v181 offset:8192\n"
"v_and_b32 v205, 0xffff0000, v205\n"
"v_mfma_f32_16x16x32_bf16 v[8:11], v[116:119], a[220:223], v[8:11]\n"
"ds_read_b64_tr_b16 v[126:127], v181 offset:12288\n"
"v_mul_f32 v188, s56, v188\n"
"v_mfma_f32_16x16x32_bf16 a[60:63], v[128:131], v[64:67], a[60:63]\n"
"s_add_u32 m0, s54, 0xc000\n"
"v_mul_f32 v205, s56, v205\n"
"v_mfma_f32_16x16x32_bf16 a[60:63], v[132:135], v[68:71], a[60:63]\n"
"v_exp_f32 v4, v4\n"
"v_mfma_f32_16x16x32_bf16 a[92:95], v[128:131], v[72:75], a[92:95]\n"
"v_exp_f32 v5, v5\n"
"v_mfma_f32_16x16x32_bf16 a[92:95], v[132:135], v[76:79], a[92:95]\n"
"buffer_load_dwordx4 v183, s[40:43], s52 offen offset:0 lds\n"
"v_cvt_pk_bf16_f32 v38, v4, v5\n"
"v_mfma_f32_16x16x32_bf16 v[12:15], v[112:115], a[232:235], v[152:155]\n"
"ds_read_b128 v[112:115], v180 offset:16896\n"
"v_cvt_pk_bf16_f32 v205, v188, v205\n"
"v_mfma_f32_16x16x32_bf16 v[12:15], v[116:119], a[236:239], v[12:15]\n"
"ds_read_b128 v[116:119], v180 offset:17152\n"
"v_lshlrev_b32 v186, 16, v206\n"
"v_mfma_f32_16x16x32_bf16 v[12:15], v[104:107], a[224:227], v[12:15]\n"
"v_exp_f32 v6, v6\n"
"v_mfma_f32_16x16x32_bf16 v[12:15], v[108:111], a[228:231], v[12:15]\n"
"v_exp_f32 v7, v7\n"
"v_mfma_f32_16x16x32_bf16 a[124:127], v[128:131], v[80:83], a[124:127]\n"
"v_exp_f32 v8, v8\n"
"v_mfma_f32_16x16x32_bf16 a[124:127], v[132:135], v[84:87], a[124:127]\n"
"v_exp_f32 v9, v9\n"
"v_mfma_f32_16x16x32_bf16 a[156:159], v[128:131], v[88:91], a[156:159]\n"
"v_cvt_pk_bf16_f32 v39, v6, v7\n"
"v_cvt_pk_bf16_f32 v46, v8, v9\n"
"v_mfma_f32_16x16x32_bf16 a[156:159], v[132:135], v[92:95], a[156:159]\n"
"v_exp_f32 v10, v10\n"
"v_mfma_f32_16x16x32_bf16 v[168:171], v[176:179], v[80:83], v[168:171]\n"
"v_exp_f32 v11, v11\n"
"v_mfma_f32_16x16x32_bf16 v[168:171], v[176:179], v[84:87], v[168:171]\n"
"v_cvt_pk_bf16_f32 v47, v10, v11\n"
"s_waitcnt lgkmcnt(2)\n"
"v_mfma_f32_16x16x32_bf16 v[0:3], v[96:99], a[160:163], v[136:139]\n"
"v_exp_f32 v12, v12\n"
"v_mfma_f32_16x16x32_bf16 v[0:3], v[100:103], a[164:167], v[0:3]\n"
"v_and_b32 v206, 0xffff0000, v206\n"
"s_waitcnt lgkmcnt(0)\n"
"v_mfma_f32_16x16x32_bf16 v[0:3], v[112:115], a[168:171], v[0:3]\n"
"buffer_load_dwordx4 v184, s[40:43], s52 offen offset:1024 lds\n"
"v_mul_f32 v186, s56, v186\n"
"v_mfma_f32_16x16x32_bf16 v[0:3], v[116:119], a[172:175], v[0:3]\n"
"v_exp_f32 v13, v13\n"
"v_mfma_f32_16x16x32_bf16 a[0:3], v[120:123], v[16:19], a[0:3]\n"
"ds_read_b128 v[104:107], v180 offset:20480\n"
"v_cvt_pk_bf16_f32 v54, v12, v13\n"
"v_mfma_f32_16x16x32_bf16 a[0:3], v[124:127], v[20:23], a[0:3]\n"
"ds_read_b64_tr_b16 v[128:129], v182 offset:0\n"
"v_mul_f32 v206, s56, v206\n"
"v_mfma_f32_16x16x32_bf16 a[32:35], v[120:123], v[24:27], a[32:35]\n"
"v_exp_f32 v14, v14\n"
"v_mfma_f32_16x16x32_bf16 a[32:35], v[124:127], v[28:31], a[32:35]\n"
"ds_read_b64_tr_b16 v[130:131], v182 offset:4096\n"
"v_cvt_pk_bf16_f32 v206, v186, v206\n"
"v_mfma_f32_16x16x32_bf16 v[4:7], v[96:99], a[176:179], v[140:143]\n"
"v_exp_f32 v15, v15\n"
"v_mfma_f32_16x16x32_bf16 v[4:7], v[100:103], a[180:183], v[4:7]\n"
"ds_read_b64_tr_b16 v[132:133], v182 offset:8192\n"
"v_cvt_pk_bf16_f32 v55, v14, v15\n"
"v_mfma_f32_16x16x32_bf16 v[4:7], v[112:115], a[184:187], v[4:7]\n"
"v_exp_f32 v0, v0\n"
"v_mfma_f32_16x16x32_bf16 v[4:7], v[116:119], a[188:191], v[4:7]\n"
"ds_read_b64_tr_b16 v[134:135], v182 offset:12288\n"
"v_lshlrev_b32 v188, 16, v207\n"
"v_mfma_f32_16x16x32_bf16 a[64:67], v[120:123], v[32:35], a[64:67]\n"
"v_exp_f32 v1, v1\n"
"v_mfma_f32_16x16x32_bf16 a[64:67], v[124:127], v[36:39], a[64:67]\n"
"v_exp_f32 v2, v2\n"
"v_mfma_f32_16x16x32_bf16 a[96:99], v[120:123], v[40:43], a[96:99]\n"
"ds_read_b128 v[108:111], v180 offset:20736\n"
"v_cvt_pk_bf16_f32 v56, v0, v1\n"
"v_mfma_f32_16x16x32_bf16 a[96:99], v[124:127], v[44:47], a[96:99]\n"
"v_exp_f32 v3, v3\n"
"v_mfma_f32_16x16x32_bf16 v[8:11], v[96:99], a[192:195], v[144:147]\n"
"v_exp_f32 v4, v4\n"
"v_mfma_f32_16x16x32_bf16 v[8:11], v[100:103], a[196:199], v[8:11]\n"
"v_exp_f32 v5, v5\n"
"v_mfma_f32_16x16x32_bf16 v[8:11], v[112:115], a[200:203], v[8:11]\n"
"buffer_load_dwordx4 v183, s[40:43], s52 offen offset:2048 lds\n"
"v_cvt_pk_bf16_f32 v57, v2, v3\n"
"v_mfma_f32_16x16x32_bf16 v[8:11], v[116:119], a[204:207], v[8:11]\n"
"v_exp_f32 v6, v6\n"
"v_mfma_f32_16x16x32_bf16 a[128:131], v[120:123], v[48:51], a[128:131]\n"
"v_exp_f32 v7, v7\n"
"v_mfma_f32_16x16x32_bf16 a[128:131], v[124:127], v[52:55], a[128:131]\n"
"v_cvt_pk_bf16_f32 v64, v4, v5\n"
"s_waitcnt lgkmcnt(1)\n"
"v_mfma_f32_16x16x32_bf16 a[4:7], v[128:131], v[16:19], a[4:7]\n"
"ds_read_b64_tr_b16 v[120:121], v181 offset:256\n"
"v_cvt_pk_bf16_f32 v65, v6, v7\n"
"v_mfma_f32_16x16x32_bf16 a[4:7], v[132:135], v[20:23], a[4:7]\n"
"v_and_b32 v207, 0xffff0000, v207\n"
"v_mul_f32 v188, s56, v188\n"
"v_mfma_f32_16x16x32_bf16 v[12:15], v[96:99], a[208:211], v[148:151]\n"
"v_exp_f32 v8, v8\n"
"v_mfma_f32_16x16x32_bf16 v[12:15], v[100:103], a[212:215], v[12:15]\n"
"ds_read_b64_tr_b16 v[122:123], v181 offset:4352\n"
"v_mul_f32 v207, s56, v207\n"
"v_mfma_f32_16x16x32_bf16 v[12:15], v[112:115], a[216:219], v[12:15]\n"
"v_exp_f32 v9, v9\n"
"v_mfma_f32_16x16x32_bf16 v[12:15], v[116:119], a[220:223], v[12:15]\n"
"v_exp_f32 v10, v10\n"
"v_mfma_f32_16x16x32_bf16 a[36:39], v[128:131], v[24:27], a[36:39]\n"
"ds_read_b64_tr_b16 v[124:125], v181 offset:8448\n"
"v_cvt_pk_bf16_f32 v72, v8, v9\n"
"v_mfma_f32_16x16x32_bf16 a[36:39], v[132:135], v[28:31], a[36:39]\n"
"v_exp_f32 v11, v11\n"
"v_mfma_f32_16x16x32_bf16 a[68:71], v[128:131], v[32:35], a[68:71]\n"
"v_cvt_pk_bf16_f32 v73, v10, v11\n"
"v_cvt_pk_bf16_f32 v207, v188, v207\n"
"v_mfma_f32_16x16x32_bf16 a[68:71], v[132:135], v[36:39], a[68:71]\n"
"ds_read_b64_tr_b16 v[126:127], v181 offset:12544\n"
"v_lshlrev_b32 v186, 16, v208\n"
"v_mfma_f32_16x16x32_bf16 v[0:3], v[112:115], a[232:235], v[152:155]\n"
"ds_read_b128 v[112:115], v180 offset:20992\n"
"v_and_b32 v208, 0xffff0000, v208\n"
"v_mfma_f32_16x16x32_bf16 v[0:3], v[116:119], a[236:239], v[0:3]\n"
"ds_read_b128 v[116:119], v180 offset:21248\n"
"v_mul_f32 v186, s56, v186\n"
"v_mfma_f32_16x16x32_bf16 v[0:3], v[96:99], a[224:227], v[0:3]\n"
"buffer_load_dwordx4 v184, s[40:43], s52 offen offset:3072 lds\n"
"v_mul_f32 v208, s56, v208\n"
"v_mfma_f32_16x16x32_bf16 v[0:3], v[100:103], a[228:231], v[0:3]\n"
"v_exp_f32 v12, v12\n"
"v_mfma_f32_16x16x32_bf16 a[100:103], v[128:131], v[40:43], a[100:103]\n"
"v_exp_f32 v13, v13\n"
"v_mfma_f32_16x16x32_bf16 a[100:103], v[132:135], v[44:47], a[100:103]\n"
"v_exp_f32 v14, v14\n"
"v_mfma_f32_16x16x32_bf16 a[132:135], v[128:131], v[48:51], a[132:135]\n"
"v_exp_f32 v15, v15\n"
"v_mfma_f32_16x16x32_bf16 a[132:135], v[132:135], v[52:55], a[132:135]\n"
"v_cvt_pk_bf16_f32 v80, v12, v13\n"
"v_cvt_pk_bf16_f32 v81, v14, v15\n"
"v_mfma_f32_16x16x32_bf16 v[156:159], v[176:179], v[16:19], v[156:159]\n"
"ds_read_b64_tr_b16 v[128:129], v182 offset:256\n"
"v_cvt_pk_bf16_f32 v208, v186, v208\n"
"v_mfma_f32_16x16x32_bf16 v[156:159], v[176:179], v[20:23], v[156:159]\n"
"v_lshlrev_b32 v188, 16, v209\n"
"s_waitcnt lgkmcnt(7)\n"
"v_mfma_f32_16x16x32_bf16 v[4:7], v[104:107], a[160:163], v[136:139]\n"
"v_exp_f32 v0, v0\n"
"v_mfma_f32_16x16x32_bf16 v[4:7], v[108:111], a[164:167], v[4:7]\n"
"v_and_b32 v209, 0xffff0000, v209\n"
"s_waitcnt lgkmcnt(1)\n"
"v_mfma_f32_16x16x32_bf16 v[4:7], v[112:115], a[168:171], v[4:7]\n"
"ds_read_b64_tr_b16 v[130:131], v182 offset:4352\n"
"v_mul_f32 v188, s56, v188\n"
"v_mfma_f32_16x16x32_bf16 v[4:7], v[116:119], a[172:175], v[4:7]\n"
"v_exp_f32 v1, v1\n"
"v_mfma_f32_16x16x32_bf16 a[8:11], v[120:123], v[16:19], a[8:11]\n"
"ds_read_b128 v[96:99], v180 offset:24576\n"
"v_cvt_pk_bf16_f32 v88, v0, v1\n"
"v_mfma_f32_16x16x32_bf16 a[8:11], v[124:127], v[20:23], a[8:11]\n"
"ds_read_b64_tr_b16 v[132:133], v182 offset:8448\n"
"v_mul_f32 v209, s56, v209\n"
"v_mfma_f32_16x16x32_bf16 a[40:43], v[120:123], v[24:27], a[40:43]\n"
"v_exp_f32 v2, v2\n"
"v_mfma_f32_16x16x32_bf16 a[40:43], v[124:127], v[28:31], a[40:43]\n"
"ds_read_b64_tr_b16 v[134:135], v182 offset:12544\n"
"v_cvt_pk_bf16_f32 v209, v188, v209\n"
"v_mfma_f32_16x16x32_bf16 v[8:11], v[104:107], a[176:179], v[140:143]\n"
"s_add_u32 m0, s53, 0x0\n"
"v_lshlrev_b32 v186, 16, v210\n"
"v_mfma_f32_16x16x32_bf16 v[8:11], v[108:111], a[180:183], v[8:11]\n"
"v_exp_f32 v3, v3\n"
"v_mfma_f32_16x16x32_bf16 v[8:11], v[112:115], a[184:187], v[8:11]\n"
"v_exp_f32 v4, v4\n"
"v_mfma_f32_16x16x32_bf16 v[8:11], v[116:119], a[188:191], v[8:11]\n"
"v_exp_f32 v5, v5\n"
"v_mfma_f32_16x16x32_bf16 a[72:75], v[120:123], v[32:35], a[72:75]\n"
"v_cvt_pk_bf16_f32 v89, v2, v3\n"
"v_cvt_pk_bf16_f32 v58, v4, v5\n"
"v_mfma_f32_16x16x32_bf16 a[72:75], v[124:127], v[36:39], a[72:75]\n"
"v_exp_f32 v6, v6\n"
"v_mfma_f32_16x16x32_bf16 a[104:107], v[120:123], v[40:43], a[104:107]\n"
"ds_read_b128 v[100:103], v180 offset:24832\n"
"v_and_b32 v210, 0xffff0000, v210\n"
"v_mfma_f32_16x16x32_bf16 a[104:107], v[124:127], v[44:47], a[104:107]\n"
"v_exp_f32 v7, v7\n"
"v_mfma_f32_16x16x32_bf16 v[12:15], v[104:107], a[192:195], v[144:147]\n"
"v_exp_f32 v8, v8\n"
"v_mfma_f32_16x16x32_bf16 v[12:15], v[108:111], a[196:199], v[12:15]\n"
"v_exp_f32 v9, v9\n"
"v_mfma_f32_16x16x32_bf16 v[12:15], v[112:115], a[200:203], v[12:15]\n"
"v_cvt_pk_bf16_f32 v59, v6, v7\n"
"v_cvt_pk_bf16_f32 v66, v8, v9\n"
"v_mfma_f32_16x16x32_bf16 v[12:15], v[116:119], a[204:207], v[12:15]\n"
"v_exp_f32 v10, v10\n"
"v_mfma_f32_16x16x32_bf16 a[136:139], v[120:123], v[48:51], a[136:139]\n"
"v_exp_f32 v11, v11\n"
"v_mfma_f32_16x16x32_bf16 a[136:139], v[124:127], v[52:55], a[136:139]\n"
"v_cvt_pk_bf16_f32 v67, v10, v11\n"
"s_waitcnt lgkmcnt(1)\n"
"v_mfma_f32_16x16x32_bf16 a[12:15], v[128:131], v[16:19], a[12:15]\n"
"ds_read_b64_tr_b16 v[120:121], v181 offset:512\n"
"v_mul_f32 v186, s56, v186\n"
"v_mfma_f32_16x16x32_bf16 a[12:15], v[132:135], v[20:23], a[12:15]\n"
"v_mul_f32 v210, s56, v210\n"
"v_cvt_pk_bf16_f32 v210, v186, v210\n"
"v_mfma_f32_16x16x32_bf16 v[0:3], v[104:107], a[208:211], v[148:151]\n"
"buffer_load_dwordx4 v183, s[36:39], s52 offen offset:0 lds\n"
"v_lshlrev_b32 v188, 16, v211\n"
"v_mfma_f32_16x16x32_bf16 v[0:3], v[108:111], a[212:215], v[0:3]\n"
"ds_read_b64_tr_b16 v[122:123], v181 offset:4608\n"
"v_and_b32 v211, 0xffff0000, v211\n"
"v_mfma_f32_16x16x32_bf16 v[0:3], v[112:115], a[216:219], v[0:3]\n"
"v_exp_f32 v12, v12\n"
"v_mfma_f32_16x16x32_bf16 v[0:3], v[116:119], a[220:223], v[0:3]\n"
"v_exp_f32 v13, v13\n"
"v_mfma_f32_16x16x32_bf16 a[44:47], v[128:131], v[24:27], a[44:47]\n"
"ds_read_b64_tr_b16 v[124:125], v181 offset:8704\n"
"v_cvt_pk_bf16_f32 v74, v12, v13\n"
"v_mfma_f32_16x16x32_bf16 a[44:47], v[132:135], v[28:31], a[44:47]\n"
"v_exp_f32 v14, v14\n"
"v_mfma_f32_16x16x32_bf16 a[76:79], v[128:131], v[32:35], a[76:79]\n"
"v_exp_f32 v15, v15\n"
"v_mfma_f32_16x16x32_bf16 a[76:79], v[132:135], v[36:39], a[76:79]\n"
"ds_read_b64_tr_b16 v[126:127], v181 offset:12800\n"
"v_cvt_pk_bf16_f32 v75, v14, v15\n"
"v_mfma_f32_16x16x32_bf16 v[4:7], v[112:115], a[232:235], v[152:155]\n"
"ds_read_b128 v[112:115], v180 offset:25088\n"
"v_mul_f32 v188, s56, v188\n"
"v_mfma_f32_16x16x32_bf16 v[4:7], v[116:119], a[236:239], v[4:7]\n"
"ds_read_b128 v[116:119], v180 offset:25344\n"
"v_mul_f32 v211, s56, v211\n"
"v_mfma_f32_16x16x32_bf16 v[4:7], v[104:107], a[224:227], v[4:7]\n"
"v_exp_f32 v0, v0\n"
"v_mfma_f32_16x16x32_bf16 v[4:7], v[108:111], a[228:231], v[4:7]\n"
"v_exp_f32 v1, v1\n"
"v_mfma_f32_16x16x32_bf16 a[108:111], v[128:131], v[40:43], a[108:111]\n"
"v_exp_f32 v2, v2\n"
"v_mfma_f32_16x16x32_bf16 a[108:111], v[132:135], v[44:47], a[108:111]\n"
"v_exp_f32 v3, v3\n"
"v_mfma_f32_16x16x32_bf16 a[140:143], v[128:131], v[48:51], a[140:143]\n"
"v_cvt_pk_bf16_f32 v82, v0, v1\n"
"v_cvt_pk_bf16_f32 v83, v2, v3\n"
"v_mfma_f32_16x16x32_bf16 a[140:143], v[132:135], v[52:55], a[140:143]\n"
"v_cvt_pk_bf16_f32 v211, v188, v211\n"
"v_lshlrev_b32 v186, 16, v212\n"
"v_mfma_f32_16x16x32_bf16 v[160:163], v[176:179], v[24:27], v[160:163]\n"
"ds_read_b64_tr_b16 v[128:129], v182 offset:512\n"
"v_and_b32 v212, 0xffff0000, v212\n"
"v_mfma_f32_16x16x32_bf16 v[160:163], v[176:179], v[28:31], v[160:163]\n"
"v_mul_f32 v186, s56, v186\n"
"s_waitcnt lgkmcnt(7)\n"
"v_mfma_f32_16x16x32_bf16 v[8:11], v[96:99], a[160:163], v[136:139]\n"
"buffer_load_dwordx4 v184, s[36:39], s52 offen offset:1024 lds\n"
"v_mul_f32 v212, s56, v212\n"
"v_mfma_f32_16x16x32_bf16 v[8:11], v[100:103], a[164:167], v[8:11]\n"
"v_cvt_pk_bf16_f32 v212, v186, v212\n"
"s_waitcnt lgkmcnt(1)\n"
"v_mfma_f32_16x16x32_bf16 v[8:11], v[112:115], a[168:171], v[8:11]\n"
"ds_read_b64_tr_b16 v[130:131], v182 offset:4608\n"
"v_lshlrev_b32 v188, 16, v213\n"
"v_mfma_f32_16x16x32_bf16 v[8:11], v[116:119], a[172:175], v[8:11]\n"
"v_exp_f32 v4, v4\n"
"v_mfma_f32_16x16x32_bf16 a[16:19], v[120:123], v[16:19], a[16:19]\n"
"ds_read_b128 v[104:107], v180 offset:28672\n"
"v_and_b32 v213, 0xffff0000, v213\n"
"v_mfma_f32_16x16x32_bf16 a[16:19], v[124:127], v[20:23], a[16:19]\n"
"ds_read_b64_tr_b16 v[132:133], v182 offset:8704\n"
"v_mul_f32 v188, s56, v188\n"
"v_mfma_f32_16x16x32_bf16 a[48:51], v[120:123], v[24:27], a[48:51]\n"
"v_exp_f32 v5, v5\n"
"v_mfma_f32_16x16x32_bf16 a[48:51], v[124:127], v[28:31], a[48:51]\n"
"ds_read_b64_tr_b16 v[134:135], v182 offset:12800\n"
"v_cvt_pk_bf16_f32 v90, v4, v5\n"
"v_mfma_f32_16x16x32_bf16 v[12:15], v[96:99], a[176:179], v[140:143]\n"
"v_exp_f32 v6, v6\n"
"v_mfma_f32_16x16x32_bf16 v[12:15], v[100:103], a[180:183], v[12:15]\n"
"v_exp_f32 v7, v7\n"
"v_mfma_f32_16x16x32_bf16 v[12:15], v[112:115], a[184:187], v[12:15]\n"
"v_exp_f32 v8, v8\n"
"v_mfma_f32_16x16x32_bf16 v[12:15], v[116:119], a[188:191], v[12:15]\n"
"v_exp_f32 v9, v9\n"
"v_mfma_f32_16x16x32_bf16 a[80:83], v[120:123], v[32:35], a[80:83]\n"
"v_cvt_pk_bf16_f32 v91, v6, v7\n"
"v_cvt_pk_bf16_f32 v60, v8, v9\n"
"v_mfma_f32_16x16x32_bf16 a[80:83], v[124:127], v[36:39], a[80:83]\n"
"v_exp_f32 v10, v10\n"
"v_mfma_f32_16x16x32_bf16 a[112:115], v[120:123], v[40:43], a[112:115]\n"
"ds_read_b128 v[108:111], v180 offset:28928\n"
"v_mul_f32 v213, s56, v213\n"
"v_mfma_f32_16x16x32_bf16 a[112:115], v[124:127], v[44:47], a[112:115]\n"
"buffer_load_dwordx4 v183, s[36:39], s52 offen offset:2048 lds\n"
"v_cvt_pk_bf16_f32 v213, v188, v213\n"
"v_mfma_f32_16x16x32_bf16 v[0:3], v[96:99], a[192:195], v[144:147]\n"
"v_exp_f32 v11, v11\n"
"v_mfma_f32_16x16x32_bf16 v[0:3], v[100:103], a[196:199], v[0:3]\n"
"v_exp_f32 v12, v12\n"
"v_mfma_f32_16x16x32_bf16 v[0:3], v[112:115], a[200:203], v[0:3]\n"
"v_exp_f32 v13, v13\n"
"v_mfma_f32_16x16x32_bf16 v[0:3], v[116:119], a[204:207], v[0:3]\n"
"v_cvt_pk_bf16_f32 v61, v10, v11\n"
"v_cvt_pk_bf16_f32 v68, v12, v13\n"
"v_mfma_f32_16x16x32_bf16 a[144:147], v[120:123], v[48:51], a[144:147]\n"
"v_exp_f32 v14, v14\n"
"v_mfma_f32_16x16x32_bf16 a[144:147], v[124:127], v[52:55], a[144:147]\n"
"v_lshlrev_b32 v186, 16, v214\n"
"s_waitcnt lgkmcnt(1)\n"
"v_mfma_f32_16x16x32_bf16 a[20:23], v[128:131], v[16:19], a[20:23]\n"
"ds_read_b64_tr_b16 v[120:121], v181 offset:768\n"
"v_and_b32 v214, 0xffff0000, v214\n"
"v_mfma_f32_16x16x32_bf16 a[20:23], v[132:135], v[20:23], a[20:23]\n"
"v_exp_f32 v15, v15\n"
"v_mfma_f32_16x16x32_bf16 v[172:175], v[176:179], v[48:51], v[172:175]\n"
"v_exp_f32 v0, v0\n"
"v_mfma_f32_16x16x32_bf16 v[172:175], v[176:179], v[52:55], v[172:175]\n"
"ds_read_b64_tr_b16 v[122:123], v181 offset:4864\n"
"v_cvt_pk_bf16_f32 v69, v14, v15\n"
"v_mfma_f32_16x16x32_bf16 v[4:7], v[96:99], a[208:211], v[148:151]\n"
"v_exp_f32 v1, v1\n"
"v_mfma_f32_16x16x32_bf16 v[4:7], v[100:103], a[212:215], v[4:7]\n"
"v_exp_f32 v2, v2\n"
"v_mfma_f32_16x16x32_bf16 v[4:7], v[112:115], a[216:219], v[4:7]\n"
"ds_read_b64_tr_b16 v[124:125], v181 offset:8960\n"
"v_cvt_pk_bf16_f32 v76, v0, v1\n"
"v_mfma_f32_16x16x32_bf16 v[4:7], v[116:119], a[220:223], v[4:7]\n"
"v_exp_f32 v3, v3\n"
"v_mfma_f32_16x16x32_bf16 a[52:55], v[128:131], v[24:27], a[52:55]\n"
"buffer_load_dwordx4 v184, s[36:39], s52 offen offset:3072 lds\n"
"v_cvt_pk_bf16_f32 v77, v2, v3\n"
"v_mfma_f32_16x16x32_bf16 a[52:55], v[132:135], v[28:31], a[52:55]\n"
"ds_read_b64_tr_b16 v[126:127], v181 offset:13056\n"
"v_mul_f32 v186, s56, v186\n"
"v_mfma_f32_16x16x32_bf16 a[84:87], v[128:131], v[32:35], a[84:87]\n"
"v_mul_f32 v214, s56, v214\n"
"v_cvt_pk_bf16_f32 v214, v186, v214\n"
"v_mfma_f32_16x16x32_bf16 a[84:87], v[132:135], v[36:39], a[84:87]\n"
"v_lshlrev_b32 v188, 16, v215\n"
"v_and_b32 v215, 0xffff0000, v215\n"
"v_mfma_f32_16x16x32_bf16 v[8:11], v[112:115], a[232:235], v[152:155]\n"
"ds_read_b128 v[112:115], v180 offset:29184\n"
"v_mul_f32 v188, s56, v188\n"
"v_mfma_f32_16x16x32_bf16 v[8:11], v[116:119], a[236:239], v[8:11]\n"
"ds_read_b128 v[116:119], v180 offset:29440\n"
"v_mul_f32 v215, s56, v215\n"
"v_mfma_f32_16x16x32_bf16 v[8:11], v[96:99], a[224:227], v[8:11]\n"
"v_exp_f32 v4, v4\n"
"v_mfma_f32_16x16x32_bf16 v[8:11], v[100:103], a[228:231], v[8:11]\n"
"v_exp_f32 v5, v5\n"
"v_mfma_f32_16x16x32_bf16 a[116:119], v[128:131], v[40:43], a[116:119]\n"
"v_exp_f32 v6, v6\n"
"v_mfma_f32_16x16x32_bf16 a[116:119], v[132:135], v[44:47], a[116:119]\n"
"v_exp_f32 v7, v7\n"
"v_mfma_f32_16x16x32_bf16 a[148:151], v[128:131], v[48:51], a[148:151]\n"
"v_cvt_pk_bf16_f32 v84, v4, v5\n"
"v_cvt_pk_bf16_f32 v85, v6, v7\n"
"v_mfma_f32_16x16x32_bf16 a[148:151], v[132:135], v[52:55], a[148:151]\n"
"v_cvt_pk_bf16_f32 v215, v188, v215\n"
"v_lshlrev_b32 v186, 16, v216\n"
"v_mfma_f32_16x16x32_bf16 v[164:167], v[176:179], v[32:35], v[164:167]\n"
"ds_read_b64_tr_b16 v[128:129], v182 offset:768\n"
"v_and_b32 v216, 0xffff0000, v216\n"
"v_mfma_f32_16x16x32_bf16 v[164:167], v[176:179], v[36:39], v[164:167]\n"
"v_mul_f32 v186, s56, v186\n"
"s_waitcnt lgkmcnt(7)\n"
"v_mfma_f32_16x16x32_bf16 v[12:15], v[104:107], a[160:163], v[136:139]\n"
"v_exp_f32 v8, v8\n"
"v_mfma_f32_16x16x32_bf16 v[12:15], v[108:111], a[164:167], v[12:15]\n"
"v_mul_f32 v216, s56, v216\n"
"s_waitcnt lgkmcnt(1)\n"
"v_mfma_f32_16x16x32_bf16 v[12:15], v[112:115], a[168:171], v[12:15]\n"
"ds_read_b64_tr_b16 v[130:131], v182 offset:4864\n"
"v_cvt_pk_bf16_f32 v216, v186, v216\n"
"v_mfma_f32_16x16x32_bf16 v[12:15], v[116:119], a[172:175], v[12:15]\n"
"s_add_u32 s52, s52, 0x4000\n"
"v_lshlrev_b32 v188, 16, v217\n"
"v_mfma_f32_16x16x32_bf16 a[24:27], v[120:123], v[16:19], a[24:27]\n"
"ds_read_b64_tr_b16 v[132:133], v182 offset:8960\n"
"v_and_b32 v217, 0xffff0000, v217\n"
"v_mfma_f32_16x16x32_bf16 a[24:27], v[124:127], v[20:23], a[24:27]\n"
"v_exp_f32 v9, v9\n"
"v_mfma_f32_16x16x32_bf16 a[56:59], v[120:123], v[24:27], a[56:59]\n"
"v_exp_f32 v10, v10\n"
"v_mfma_f32_16x16x32_bf16 a[56:59], v[124:127], v[28:31], a[56:59]\n"
"ds_read_b64_tr_b16 v[134:135], v182 offset:13056\n"
"v_cvt_pk_bf16_f32 v92, v8, v9\n"
"v_mfma_f32_16x16x32_bf16 v[0:3], v[104:107], a[176:179], v[140:143]\n"
"v_exp_f32 v11, v11\n"
"v_mfma_f32_16x16x32_bf16 v[0:3], v[108:111], a[180:183], v[0:3]\n"
"v_exp_f32 v12, v12\n"
"v_mfma_f32_16x16x32_bf16 v[0:3], v[112:115], a[184:187], v[0:3]\n"
"v_exp_f32 v13, v13\n"
"v_mfma_f32_16x16x32_bf16 v[0:3], v[116:119], a[188:191], v[0:3]\n"
"v_cvt_pk_bf16_f32 v93, v10, v11\n"
"v_cvt_pk_bf16_f32 v62, v12, v13\n"
"v_mfma_f32_16x16x32_bf16 a[88:91], v[120:123], v[32:35], a[88:91]\n"
"v_exp_f32 v14, v14\n"
"v_mfma_f32_16x16x32_bf16 a[88:91], v[124:127], v[36:39], a[88:91]\n"
"v_exp_f32 v15, v15\n"
"v_mfma_f32_16x16x32_bf16 a[120:123], v[120:123], v[40:43], a[120:123]\n"
"v_cvt_pk_bf16_f32 v63, v14, v15\n"
"v_mul_f32 v188, s56, v188\n"
"v_mfma_f32_16x16x32_bf16 a[120:123], v[124:127], v[44:47], a[120:123]\n"
"v_mul_f32 v217, s56, v217\n"
"v_cvt_pk_bf16_f32 v217, v188, v217\n"
"v_mfma_f32_16x16x32_bf16 v[4:7], v[104:107], a[192:195], v[144:147]\n"
"v_exp_f32 v0, v0\n"
"v_mfma_f32_16x16x32_bf16 v[4:7], v[108:111], a[196:199], v[4:7]\n"
"v_exp_f32 v1, v1\n"
"v_mfma_f32_16x16x32_bf16 v[4:7], v[112:115], a[200:203], v[4:7]\n"
"v_exp_f32 v2, v2\n"
"v_mfma_f32_16x16x32_bf16 v[4:7], v[116:119], a[204:207], v[4:7]\n"
"v_exp_f32 v3, v3\n"
"v_mfma_f32_16x16x32_bf16 a[152:155], v[120:123], v[48:51], a[152:155]\n"
"v_cvt_pk_bf16_f32 v70, v0, v1\n"
"v_cvt_pk_bf16_f32 v71, v2, v3\n"
"v_mfma_f32_16x16x32_bf16 a[152:155], v[124:127], v[52:55], a[152:155]\n"
"s_waitcnt vmcnt(16) lgkmcnt(0)\n"
"v_xor_b32 v180, 0x8000, v180\n"
"s_barrier\n"
"v_mfma_f32_16x16x32_bf16 a[28:31], v[128:131], v[16:19], a[28:31]\n"
"ds_read_b128 v[96:99], v180 offset:0\n"
"v_lshlrev_b32 v186, 16, v218\n"
"v_mfma_f32_16x16x32_bf16 a[28:31], v[132:135], v[20:23], a[28:31]\n"
"ds_read_b128 v[100:103], v180 offset:256\n"
"v_and_b32 v218, 0xffff0000, v218\n"
"v_mfma_f32_16x16x32_bf16 v[8:11], v[104:107], a[208:211], v[148:151]\n"
"ds_read_b64_tr_b16 v[120:121], v181 offset:16384\n"
"v_mul_f32 v186, s56, v186\n"
"v_mfma_f32_16x16x32_bf16 v[8:11], v[108:111], a[212:215], v[8:11]\n"
"ds_read_b64_tr_b16 v[122:123], v181 offset:20480\n"
"v_mul_f32 v218, s56, v218\n"
"v_mfma_f32_16x16x32_bf16 v[8:11], v[112:115], a[216:219], v[8:11]\n"
"ds_read_b64_tr_b16 v[124:125], v181 offset:24576\n"
"v_cvt_pk_bf16_f32 v218, v186, v218\n"
"v_mfma_f32_16x16x32_bf16 v[8:11], v[116:119], a[220:223], v[8:11]\n"
"ds_read_b64_tr_b16 v[126:127], v181 offset:28672\n"
"v_lshlrev_b32 v188, 16, v219\n"
"v_mfma_f32_16x16x32_bf16 a[60:63], v[128:131], v[24:27], a[60:63]\n"
"s_add_u32 m0, s54, 0x0\n"
"v_and_b32 v219, 0xffff0000, v219\n"
"v_mfma_f32_16x16x32_bf16 a[60:63], v[132:135], v[28:31], a[60:63]\n"
"v_exp_f32 v4, v4\n"
"v_mfma_f32_16x16x32_bf16 a[92:95], v[128:131], v[32:35], a[92:95]\n"
"v_exp_f32 v5, v5\n"
"v_mfma_f32_16x16x32_bf16 a[92:95], v[132:135], v[36:39], a[92:95]\n"
"buffer_load_dwordx4 v183, s[40:43], s52 offen offset:0 lds\n"
"v_cvt_pk_bf16_f32 v78, v4, v5\n"
"v_mfma_f32_16x16x32_bf16 v[12:15], v[112:115], a[232:235], v[152:155]\n"
"ds_read_b128 v[112:115], v180 offset:512\n"
"v_mul_f32 v188, s56, v188\n"
"v_mfma_f32_16x16x32_bf16 v[12:15], v[116:119], a[236:239], v[12:15]\n"
"ds_read_b128 v[116:119], v180 offset:768\n"
"v_mul_f32 v219, s56, v219\n"
"v_mfma_f32_16x16x32_bf16 v[12:15], v[104:107], a[224:227], v[12:15]\n"
"v_exp_f32 v6, v6\n"
"v_mfma_f32_16x16x32_bf16 v[12:15], v[108:111], a[228:231], v[12:15]\n"
"v_exp_f32 v7, v7\n"
"v_mfma_f32_16x16x32_bf16 a[124:127], v[128:131], v[40:43], a[124:127]\n"
"v_exp_f32 v8, v8\n"
"v_mfma_f32_16x16x32_bf16 a[124:127], v[132:135], v[44:47], a[124:127]\n"
"v_exp_f32 v9, v9\n"
"v_mfma_f32_16x16x32_bf16 a[156:159], v[128:131], v[48:51], a[156:159]\n"
"v_cvt_pk_bf16_f32 v79, v6, v7\n"
"v_cvt_pk_bf16_f32 v86, v8, v9\n"
"v_mfma_f32_16x16x32_bf16 a[156:159], v[132:135], v[52:55], a[156:159]\n"
"v_exp_f32 v10, v10\n"
"v_mfma_f32_16x16x32_bf16 v[168:171], v[176:179], v[40:43], v[168:171]\n"
"v_exp_f32 v11, v11\n"
"v_mfma_f32_16x16x32_bf16 v[168:171], v[176:179], v[44:47], v[168:171]\n"
"v_cvt_pk_bf16_f32 v87, v10, v11\n"
"s_waitcnt lgkmcnt(2)\n"
"s_mov_b32 s55, 3\n"
"s_branch L_loop_a_%=\n"
"L_seam_a_%=:\n"
"s_cmp_lg_u32 s55, 0\n"
"s_cbranch_scc1 L_final_a_%=\n"
"s_nop 3\n"
"v_exp_f32 v12, v12\n"
"v_exp_f32 v13, v13\n"
"v_exp_f32 v14, v14\n"
"v_exp_f32 v15, v15\n"
"s_nop 0\n"
"v_cvt_pk_bf16_f32 v94, v12, v13\n"
"v_cvt_pk_bf16_f32 v95, v14, v15\n"
"s_nop 1\n"
"v_mfma_f32_16x16x32_bf16 a[0:3], v[120:123], v[56:59], a[0:3]\n"
"v_cvt_pk_bf16_f32 v219, v188, v219\n"
"v_accvgpr_write_b32 a160, v192\n"
"v_mfma_f32_16x16x32_bf16 a[0:3], v[124:127], v[60:63], a[0:3]\n"
"ds_read_b64_tr_b16 v[128:129], v182 offset:16384\n"
"v_accvgpr_write_b32 a161, v193\n"
"v_mfma_f32_16x16x32_bf16 a[32:35], v[120:123], v[64:67], a[32:35]\n"
"ds_read_b64_tr_b16 v[130:131], v182 offset:20480\n"
"v_accvgpr_write_b32 a162, v194\n"
"v_mfma_f32_16x16x32_bf16 a[32:35], v[124:127], v[68:71], a[32:35]\n"
"ds_read_b64_tr_b16 v[132:133], v182 offset:24576\n"
"v_accvgpr_write_b32 a163, v195\n"
"v_mfma_f32_16x16x32_bf16 a[64:67], v[120:123], v[72:75], a[64:67]\n"
"ds_read_b64_tr_b16 v[134:135], v182 offset:28672\n"
"v_accvgpr_write_b32 a164, v196\n"
"v_mfma_f32_16x16x32_bf16 a[64:67], v[124:127], v[76:79], a[64:67]\n"
"buffer_load_dwordx4 v184, s[40:43], s52 offen offset:1024 lds\n"
"v_accvgpr_write_b32 a165, v197\n"
"v_mfma_f32_16x16x32_bf16 a[96:99], v[120:123], v[80:83], a[96:99]\n"
"v_accvgpr_write_b32 a166, v198\n"
"v_accvgpr_write_b32 a167, v199\n"
"v_mfma_f32_16x16x32_bf16 a[96:99], v[124:127], v[84:87], a[96:99]\n"
"v_accvgpr_write_b32 a168, v200\n"
"v_accvgpr_write_b32 a169, v201\n"
"v_mfma_f32_16x16x32_bf16 a[128:131], v[120:123], v[88:91], a[128:131]\n"
"v_accvgpr_write_b32 a170, v202\n"
"v_accvgpr_write_b32 a171, v203\n"
"v_mfma_f32_16x16x32_bf16 a[128:131], v[124:127], v[92:95], a[128:131]\n"
"v_accvgpr_write_b32 a172, v204\n"
"s_waitcnt lgkmcnt(0)\n"
"v_mfma_f32_16x16x32_bf16 a[4:7], v[128:131], v[56:59], a[4:7]\n"
"ds_read_b64_tr_b16 v[120:121], v181 offset:16640\n"
"v_accvgpr_write_b32 a173, v205\n"
"v_mfma_f32_16x16x32_bf16 a[4:7], v[132:135], v[60:63], a[4:7]\n"
"ds_read_b64_tr_b16 v[122:123], v181 offset:20736\n"
"v_accvgpr_write_b32 a174, v206\n"
"v_mfma_f32_16x16x32_bf16 a[36:39], v[128:131], v[64:67], a[36:39]\n"
"buffer_load_dwordx4 v183, s[40:43], s52 offen offset:2048 lds\n"
"v_accvgpr_write_b32 a175, v207\n"
"v_mfma_f32_16x16x32_bf16 a[36:39], v[132:135], v[68:71], a[36:39]\n"
"ds_read_b64_tr_b16 v[124:125], v181 offset:24832\n"
"v_accvgpr_write_b32 a176, v208\n"
"v_mfma_f32_16x16x32_bf16 a[68:71], v[128:131], v[72:75], a[68:71]\n"
"ds_read_b64_tr_b16 v[126:127], v181 offset:28928\n"
"v_accvgpr_write_b32 a177, v209\n"
"v_mfma_f32_16x16x32_bf16 a[68:71], v[132:135], v[76:79], a[68:71]\n"
"v_accvgpr_write_b32 a178, v210\n"
"v_accvgpr_write_b32 a179, v211\n"
"v_mfma_f32_16x16x32_bf16 a[100:103], v[128:131], v[80:83], a[100:103]\n"
"v_accvgpr_write_b32 a180, v212\n"
"v_accvgpr_write_b32 a181, v213\n"
"v_mfma_f32_16x16x32_bf16 a[100:103], v[132:135], v[84:87], a[100:103]\n"
"v_accvgpr_write_b32 a182, v214\n"
"v_accvgpr_write_b32 a183, v215\n"
"v_mfma_f32_16x16x32_bf16 a[132:135], v[128:131], v[88:91], a[132:135]\n"
"v_accvgpr_write_b32 a184, v216\n"
"v_accvgpr_write_b32 a185, v217\n"
"v_mfma_f32_16x16x32_bf16 a[132:135], v[132:135], v[92:95], a[132:135]\n"
"v_accvgpr_write_b32 a186, v218\n"
"v_accvgpr_write_b32 a187, v219\n"
"v_mfma_f32_16x16x32_bf16 v[156:159], v[176:179], v[56:59], v[156:159]\n"
"ds_read_b64_tr_b16 v[128:129], v182 offset:16640\n"
"v_lshlrev_b32 v0, 16, v220\n"
"v_mfma_f32_16x16x32_bf16 v[156:159], v[176:179], v[60:63], v[156:159]\n"
"v_and_b32 v1, 0xffff0000, v220\n"
"s_waitcnt lgkmcnt(1)\n"
"v_mfma_f32_16x16x32_bf16 a[8:11], v[120:123], v[56:59], a[8:11]\n"
"ds_read_b64_tr_b16 v[130:131], v182 offset:20736\n"
"v_mul_f32 v0, s56, v0\n"
"v_mfma_f32_16x16x32_bf16 a[8:11], v[124:127], v[60:63], a[8:11]\n"
"ds_read_b64_tr_b16 v[132:133], v182 offset:24832\n"
"v_mul_f32 v1, s56, v1\n"
"v_mfma_f32_16x16x32_bf16 a[40:43], v[120:123], v[64:67], a[40:43]\n"
"ds_read_b64_tr_b16 v[134:135], v182 offset:28928\n"
"v_cvt_pk_bf16_f32 v2, v0, v1\n"
"v_mfma_f32_16x16x32_bf16 a[40:43], v[124:127], v[68:71], a[40:43]\n"
"buffer_load_dwordx4 v184, s[40:43], s52 offen offset:3072 lds\n"
"v_accvgpr_write_b32 a188, v2\n"
"v_mfma_f32_16x16x32_bf16 a[72:75], v[120:123], v[72:75], a[72:75]\n"
"v_lshlrev_b32 v4, 16, v221\n"
"v_and_b32 v5, 0xffff0000, v221\n"
"v_mfma_f32_16x16x32_bf16 a[72:75], v[124:127], v[76:79], a[72:75]\n"
"v_mul_f32 v4, s56, v4\n"
"v_mul_f32 v5, s56, v5\n"
"v_mfma_f32_16x16x32_bf16 a[104:107], v[120:123], v[80:83], a[104:107]\n"
"v_cvt_pk_bf16_f32 v6, v4, v5\n"
"v_accvgpr_write_b32 a189, v6\n"
"v_mfma_f32_16x16x32_bf16 a[104:107], v[124:127], v[84:87], a[104:107]\n"
"v_lshlrev_b32 v0, 16, v222\n"
"v_and_b32 v1, 0xffff0000, v222\n"
"v_mfma_f32_16x16x32_bf16 a[136:139], v[120:123], v[88:91], a[136:139]\n"
"s_add_u32 m0, s53, 0xc000\n"
"v_mul_f32 v0, s56, v0\n"
"v_mfma_f32_16x16x32_bf16 a[136:139], v[124:127], v[92:95], a[136:139]\n"
"v_mul_f32 v1, s56, v1\n"
"s_waitcnt lgkmcnt(0)\n"
"v_mfma_f32_16x16x32_bf16 a[12:15], v[128:131], v[56:59], a[12:15]\n"
"ds_read_b64_tr_b16 v[120:121], v181 offset:16896\n"
"v_cvt_pk_bf16_f32 v2, v0, v1\n"
"v_mfma_f32_16x16x32_bf16 a[12:15], v[132:135], v[60:63], a[12:15]\n"
"ds_read_b64_tr_b16 v[122:123], v181 offset:20992\n"
"v_accvgpr_write_b32 a190, v2\n"
"v_mfma_f32_16x16x32_bf16 a[44:47], v[128:131], v[64:67], a[44:47]\n"
"v_lshlrev_b32 v4, 16, v223\n"
"v_and_b32 v5, 0xffff0000, v223\n"
"v_mfma_f32_16x16x32_bf16 a[44:47], v[132:135], v[68:71], a[44:47]\n"
"ds_read_b64_tr_b16 v[124:125], v181 offset:25088\n"
"v_mul_f32 v4, s56, v4\n"
"v_mfma_f32_16x16x32_bf16 a[76:79], v[128:131], v[72:75], a[76:79]\n"
"ds_read_b64_tr_b16 v[126:127], v181 offset:29184\n"
"v_mul_f32 v5, s56, v5\n"
"v_mfma_f32_16x16x32_bf16 a[76:79], v[132:135], v[76:79], a[76:79]\n"
"v_cvt_pk_bf16_f32 v6, v4, v5\n"
"v_accvgpr_write_b32 a191, v6\n"
"v_mfma_f32_16x16x32_bf16 a[108:111], v[128:131], v[80:83], a[108:111]\n"
"v_lshlrev_b32 v0, 16, v224\n"
"v_and_b32 v1, 0xffff0000, v224\n"
"v_mfma_f32_16x16x32_bf16 a[108:111], v[132:135], v[84:87], a[108:111]\n"
"buffer_load_dwordx4 v183, s[36:39], s52 offen offset:0 lds\n"
"v_mul_f32 v0, s56, v0\n"
"v_mfma_f32_16x16x32_bf16 a[140:143], v[128:131], v[88:91], a[140:143]\n"
"v_mul_f32 v1, s56, v1\n"
"v_cvt_pk_bf16_f32 v2, v0, v1\n"
"v_mfma_f32_16x16x32_bf16 a[140:143], v[132:135], v[92:95], a[140:143]\n"
"v_accvgpr_write_b32 a192, v2\n"
"v_lshlrev_b32 v4, 16, v225\n"
"v_mfma_f32_16x16x32_bf16 v[160:163], v[176:179], v[64:67], v[160:163]\n"
"ds_read_b64_tr_b16 v[128:129], v182 offset:16896\n"
"v_and_b32 v5, 0xffff0000, v225\n"
"v_mfma_f32_16x16x32_bf16 v[160:163], v[176:179], v[68:71], v[160:163]\n"
"v_mul_f32 v4, s56, v4\n"
"s_waitcnt lgkmcnt(1)\n"
"v_mfma_f32_16x16x32_bf16 a[16:19], v[120:123], v[56:59], a[16:19]\n"
"ds_read_b64_tr_b16 v[130:131], v182 offset:20992\n"
"v_mul_f32 v5, s56, v5\n"
"v_mfma_f32_16x16x32_bf16 a[16:19], v[124:127], v[60:63], a[16:19]\n"
"ds_read_b64_tr_b16 v[132:133], v182 offset:25088\n"
"v_cvt_pk_bf16_f32 v6, v4, v5\n"
"v_mfma_f32_16x16x32_bf16 a[48:51], v[120:123], v[64:67], a[48:51]\n"
"ds_read_b64_tr_b16 v[134:135], v182 offset:29184\n"
"v_accvgpr_write_b32 a193, v6\n"
"v_mfma_f32_16x16x32_bf16 a[48:51], v[124:127], v[68:71], a[48:51]\n"
"v_lshlrev_b32 v0, 16, v226\n"
"v_and_b32 v1, 0xffff0000, v226\n"
"v_mfma_f32_16x16x32_bf16 a[80:83], v[120:123], v[72:75], a[80:83]\n"
"v_mul_f32 v0, s56, v0\n"
"v_mul_f32 v1, s56, v1\n"
"v_mfma_f32_16x16x32_bf16 a[80:83], v[124:127], v[76:79], a[80:83]\n"
"buffer_load_dwordx4 v184, s[36:39], s52 offen offset:1024 lds\n"
"v_cvt_pk_bf16_f32 v2, v0, v1\n"
"v_mfma_f32_16x16x32_bf16 a[112:115], v[120:123], v[80:83], a[112:115]\n"
"v_accvgpr_write_b32 a194, v2\n"
"v_lshlrev_b32 v4, 16, v227\n"
"v_mfma_f32_16x16x32_bf16 a[112:115], v[124:127], v[84:87], a[112:115]\n"
"v_and_b32 v5, 0xffff0000, v227\n"
"v_mul_f32 v4, s56, v4\n"
"v_mfma_f32_16x16x32_bf16 a[144:147], v[120:123], v[88:91], a[144:147]\n"
"v_mul_f32 v5, s56, v5\n"
"v_cvt_pk_bf16_f32 v6, v4, v5\n"
"v_mfma_f32_16x16x32_bf16 a[144:147], v[124:127], v[92:95], a[144:147]\n"
"v_accvgpr_write_b32 a195, v6\n"
"s_waitcnt lgkmcnt(0)\n"
"v_mfma_f32_16x16x32_bf16 a[20:23], v[128:131], v[56:59], a[20:23]\n"
"ds_read_b64_tr_b16 v[120:121], v181 offset:17152\n"
"v_lshlrev_b32 v0, 16, v228\n"
"v_mfma_f32_16x16x32_bf16 a[20:23], v[132:135], v[60:63], a[20:23]\n"
"ds_read_b64_tr_b16 v[122:123], v181 offset:21248\n"
"v_and_b32 v1, 0xffff0000, v228\n"
"v_mfma_f32_16x16x32_bf16 v[172:175], v[176:179], v[88:91], v[172:175]\n"
"v_mul_f32 v0, s56, v0\n"
"v_mul_f32 v1, s56, v1\n"
"v_mfma_f32_16x16x32_bf16 v[172:175], v[176:179], v[92:95], v[172:175]\n"
"ds_read_b64_tr_b16 v[124:125], v181 offset:25344\n"
"v_cvt_pk_bf16_f32 v2, v0, v1\n"
"v_mfma_f32_16x16x32_bf16 a[52:55], v[128:131], v[64:67], a[52:55]\n"
"buffer_load_dwordx4 v183, s[36:39], s52 offen offset:2048 lds\n"
"v_accvgpr_write_b32 a196, v2\n"
"v_mfma_f32_16x16x32_bf16 a[52:55], v[132:135], v[68:71], a[52:55]\n"
"ds_read_b64_tr_b16 v[126:127], v181 offset:29440\n"
"v_lshlrev_b32 v4, 16, v229\n"
"v_mfma_f32_16x16x32_bf16 a[84:87], v[128:131], v[72:75], a[84:87]\n"
"v_and_b32 v5, 0xffff0000, v229\n"
"v_mul_f32 v4, s56, v4\n"
"v_mfma_f32_16x16x32_bf16 a[84:87], v[132:135], v[76:79], a[84:87]\n"
"v_mul_f32 v5, s56, v5\n"
"v_cvt_pk_bf16_f32 v6, v4, v5\n"
"v_mfma_f32_16x16x32_bf16 a[116:119], v[128:131], v[80:83], a[116:119]\n"
"v_accvgpr_write_b32 a197, v6\n"
"v_lshlrev_b32 v0, 16, v230\n"
"v_mfma_f32_16x16x32_bf16 a[116:119], v[132:135], v[84:87], a[116:119]\n"
"v_and_b32 v1, 0xffff0000, v230\n"
"v_mul_f32 v0, s56, v0\n"
"v_mfma_f32_16x16x32_bf16 a[148:151], v[128:131], v[88:91], a[148:151]\n"
"v_mul_f32 v1, s56, v1\n"
"v_cvt_pk_bf16_f32 v2, v0, v1\n"
"v_mfma_f32_16x16x32_bf16 a[148:151], v[132:135], v[92:95], a[148:151]\n"
"v_accvgpr_write_b32 a198, v2\n"
"v_lshlrev_b32 v4, 16, v231\n"
"v_mfma_f32_16x16x32_bf16 v[164:167], v[176:179], v[72:75], v[164:167]\n"
"ds_read_b64_tr_b16 v[128:129], v182 offset:17152\n"
"v_and_b32 v5, 0xffff0000, v231\n"
"v_mfma_f32_16x16x32_bf16 v[164:167], v[176:179], v[76:79], v[164:167]\n"
"v_mul_f32 v4, s56, v4\n"
"s_waitcnt lgkmcnt(1)\n"
"v_mfma_f32_16x16x32_bf16 a[24:27], v[120:123], v[56:59], a[24:27]\n"
"ds_read_b64_tr_b16 v[130:131], v182 offset:21248\n"
"v_mul_f32 v5, s56, v5\n"
"v_mfma_f32_16x16x32_bf16 a[24:27], v[124:127], v[60:63], a[24:27]\n"
"ds_read_b64_tr_b16 v[132:133], v182 offset:25344\n"
"v_cvt_pk_bf16_f32 v6, v4, v5\n"
"v_mfma_f32_16x16x32_bf16 a[56:59], v[120:123], v[64:67], a[56:59]\n"
"ds_read_b64_tr_b16 v[134:135], v182 offset:29440\n"
"v_accvgpr_write_b32 a199, v6\n"
"v_mfma_f32_16x16x32_bf16 a[56:59], v[124:127], v[68:71], a[56:59]\n"
"buffer_load_dwordx4 v184, s[36:39], s52 offen offset:3072 lds\n"
"v_lshlrev_b32 v0, 16, v232\n"
"v_mfma_f32_16x16x32_bf16 a[88:91], v[120:123], v[72:75], a[88:91]\n"
"v_and_b32 v1, 0xffff0000, v232\n"
"v_mul_f32 v0, s56, v0\n"
"v_mfma_f32_16x16x32_bf16 a[88:91], v[124:127], v[76:79], a[88:91]\n"
"v_mul_f32 v1, s56, v1\n"
"v_cvt_pk_bf16_f32 v2, v0, v1\n"
"v_mfma_f32_16x16x32_bf16 a[120:123], v[120:123], v[80:83], a[120:123]\n"
"v_accvgpr_write_b32 a200, v2\n"
"v_lshlrev_b32 v4, 16, v233\n"
"v_mfma_f32_16x16x32_bf16 a[120:123], v[124:127], v[84:87], a[120:123]\n"
"v_and_b32 v5, 0xffff0000, v233\n"
"v_mul_f32 v4, s56, v4\n"
"v_mfma_f32_16x16x32_bf16 a[152:155], v[120:123], v[88:91], a[152:155]\n"
"v_mul_f32 v5, s56, v5\n"
"v_cvt_pk_bf16_f32 v6, v4, v5\n"
"v_mfma_f32_16x16x32_bf16 a[152:155], v[124:127], v[92:95], a[152:155]\n"
"v_accvgpr_write_b32 a201, v6\n"
"s_waitcnt lgkmcnt(0)\n"
"v_mfma_f32_16x16x32_bf16 a[28:31], v[128:131], v[56:59], a[28:31]\n"
"s_add_u32 s52, s52, 0x4000\n"
"v_lshlrev_b32 v0, 16, v234\n"
"v_mfma_f32_16x16x32_bf16 a[28:31], v[132:135], v[60:63], a[28:31]\n"
"v_and_b32 v1, 0xffff0000, v234\n"
"v_mul_f32 v0, s56, v0\n"
"v_mfma_f32_16x16x32_bf16 a[60:63], v[128:131], v[64:67], a[60:63]\n"
"v_mul_f32 v1, s56, v1\n"
"v_cvt_pk_bf16_f32 v2, v0, v1\n"
"v_mfma_f32_16x16x32_bf16 a[60:63], v[132:135], v[68:71], a[60:63]\n"
"v_accvgpr_write_b32 a202, v2\n"
"v_lshlrev_b32 v4, 16, v235\n"
"v_mfma_f32_16x16x32_bf16 a[92:95], v[128:131], v[72:75], a[92:95]\n"
"v_and_b32 v5, 0xffff0000, v235\n"
"v_mul_f32 v4, s56, v4\n"
"v_mfma_f32_16x16x32_bf16 a[92:95], v[132:135], v[76:79], a[92:95]\n"
"v_mul_f32 v5, s56, v5\n"
"v_cvt_pk_bf16_f32 v6, v4, v5\n"
"v_mfma_f32_16x16x32_bf16 a[124:127], v[128:131], v[80:83], a[124:127]\n"
"v_accvgpr_write_b32 a203, v6\n"
"v_lshlrev_b32 v0, 16, v236\n"
"v_mfma_f32_16x16x32_bf16 a[124:127], v[132:135], v[84:87], a[124:127]\n"
"v_and_b32 v1, 0xffff0000, v236\n"
"v_mul_f32 v0, s56, v0\n"
"v_mfma_f32_16x16x32_bf16 a[156:159], v[128:131], v[88:91], a[156:159]\n"
"v_mul_f32 v1, s56, v1\n"
"v_cvt_pk_bf16_f32 v2, v0, v1\n"
"v_mfma_f32_16x16x32_bf16 a[156:159], v[132:135], v[92:95], a[156:159]\n"
"v_accvgpr_write_b32 a204, v2\n"
"v_mfma_f32_16x16x32_bf16 v[168:171], v[176:179], v[80:83], v[168:171]\n"
"v_mfma_f32_16x16x32_bf16 v[168:171], v[176:179], v[84:87], v[168:171]\n"
"v_lshlrev_b32 v0, 16, v237\n"
"v_and_b32 v1, 0xffff0000, v237\n"
"v_pk_mul_f32 v[0:1], v[0:1], s[56:57]\n"
"v_cvt_pk_bf16_f32 v2, v0, v1\n"
"v_accvgpr_write_b32 a205, v2\n"
"v_lshlrev_b32 v4, 16, v238\n"
"v_and_b32 v5, 0xffff0000, v238\n"
"v_pk_mul_f32 v[4:5], v[4:5], s[56:57]\n"
"v_cvt_pk_bf16_f32 v6, v4, v5\n"
"v_accvgpr_write_b32 a206, v6\n"
"v_lshlrev_b32 v0, 16, v239\n"
"v_and_b32 v1, 0xffff0000, v239\n"
"v_pk_mul_f32 v[0:1], v[0:1], s[56:57]\n"
"v_cvt_pk_bf16_f32 v2, v0, v1\n"
"v_accvgpr_write_b32 a207, v2\n"
"v_lshlrev_b32 v4, 16, v240\n"
"v_and_b32 v5, 0xffff0000, v240\n"
"v_pk_mul_f32 v[4:5], v[4:5], s[56:57]\n"
"v_cvt_pk_bf16_f32 v6, v4, v5\n"
"v_accvgpr_write_b32 a208, v6\n"
"v_lshlrev_b32 v0, 16, v241\n"
"v_and_b32 v1, 0xffff0000, v241\n"
"v_pk_mul_f32 v[0:1], v[0:1], s[56:57]\n"
"v_cvt_pk_bf16_f32 v2, v0, v1\n"
"v_accvgpr_write_b32 a209, v2\n"
"v_lshlrev_b32 v4, 16, v242\n"
"v_and_b32 v5, 0xffff0000, v242\n"
"v_pk_mul_f32 v[4:5], v[4:5], s[56:57]\n"
"v_cvt_pk_bf16_f32 v6, v4, v5\n"
"v_accvgpr_write_b32 a210, v6\n"
"v_lshlrev_b32 v0, 16, v243\n"
"v_and_b32 v1, 0xffff0000, v243\n"
"v_pk_mul_f32 v[0:1], v[0:1], s[56:57]\n"
"v_cvt_pk_bf16_f32 v2, v0, v1\n"
"v_accvgpr_write_b32 a211, v2\n"
"v_lshlrev_b32 v4, 16, v244\n"
"v_and_b32 v5, 0xffff0000, v244\n"
"v_pk_mul_f32 v[4:5], v[4:5], s[56:57]\n"
"v_cvt_pk_bf16_f32 v6, v4, v5\n"
"v_accvgpr_write_b32 a212, v6\n"
"v_lshlrev_b32 v0, 16, v245\n"
"v_and_b32 v1, 0xffff0000, v245\n"
"v_pk_mul_f32 v[0:1], v[0:1], s[56:57]\n"
"v_cvt_pk_bf16_f32 v2, v0, v1\n"
"v_accvgpr_write_b32 a213, v2\n"
"v_lshlrev_b32 v4, 16, v246\n"
"v_and_b32 v5, 0xffff0000, v246\n"
"v_pk_mul_f32 v[4:5], v[4:5], s[56:57]\n"
"v_cvt_pk_bf16_f32 v6, v4, v5\n"
"v_accvgpr_write_b32 a214, v6\n"
"v_lshlrev_b32 v0, 16, v247\n"
"v_and_b32 v1, 0xffff0000, v247\n"
"v_pk_mul_f32 v[0:1], v[0:1], s[56:57]\n"
"v_cvt_pk_bf16_f32 v2, v0, v1\n"
"v_accvgpr_write_b32 a215, v2\n"
"v_lshlrev_b32 v4, 16, v248\n"
"v_and_b32 v5, 0xffff0000, v248\n"
"v_pk_mul_f32 v[4:5], v[4:5], s[56:57]\n"
"v_cvt_pk_bf16_f32 v6, v4, v5\n"
"v_accvgpr_write_b32 a216, v6\n"
"v_lshlrev_b32 v0, 16, v249\n"
"v_and_b32 v1, 0xffff0000, v249\n"
"v_pk_mul_f32 v[0:1], v[0:1], s[56:57]\n"
"v_cvt_pk_bf16_f32 v2, v0, v1\n"
"v_accvgpr_write_b32 a217, v2\n"
"v_lshlrev_b32 v4, 16, v250\n"
"v_and_b32 v5, 0xffff0000, v250\n"
"v_pk_mul_f32 v[4:5], v[4:5], s[56:57]\n"
"v_cvt_pk_bf16_f32 v6, v4, v5\n"
"v_accvgpr_write_b32 a218, v6\n"
"v_lshlrev_b32 v0, 16, v251\n"
"v_and_b32 v1, 0xffff0000, v251\n"
"v_pk_mul_f32 v[0:1], v[0:1], s[56:57]\n"
"v_cvt_pk_bf16_f32 v2, v0, v1\n"
"v_accvgpr_write_b32 a219, v2\n"
"v_lshlrev_b32 v4, 16, v252\n"
"v_and_b32 v5, 0xffff0000, v252\n"
"v_pk_mul_f32 v[4:5], v[4:5], s[56:57]\n"
"v_cvt_pk_bf16_f32 v6, v4, v5\n"
"v_accvgpr_write_b32 a220, v6\n"
"v_lshlrev_b32 v0, 16, v253\n"
"v_and_b32 v1, 0xffff0000, v253\n"
"v_pk_mul_f32 v[0:1], v[0:1], s[56:57]\n"
"v_cvt_pk_bf16_f32 v2, v0, v1\n"
"v_accvgpr_write_b32 a221, v2\n"
"v_lshlrev_b32 v4, 16, v254\n"
"v_and_b32 v5, 0xffff0000, v254\n"
"v_pk_mul_f32 v[4:5], v[4:5], s[56:57]\n"
"v_cvt_pk_bf16_f32 v6, v4, v5\n"
"v_accvgpr_write_b32 a222, v6\n"
"v_lshlrev_b32 v0, 16, v255\n"
"v_and_b32 v1, 0xffff0000, v255\n"
"v_pk_mul_f32 v[0:1], v[0:1], s[56:57]\n"
"v_cvt_pk_bf16_f32 v2, v0, v1\n"
"v_accvgpr_write_b32 a223, v2\n"
"v_accvgpr_read_b32 v7, a240\n"
"v_lshlrev_b32 v4, 16, v7\n"
"v_and_b32 v5, 0xffff0000, v7\n"
"v_pk_mul_f32 v[4:5], v[4:5], s[56:57]\n"
"v_cvt_pk_bf16_f32 v6, v4, v5\n"
"v_accvgpr_write_b32 a224, v6\n"
"v_accvgpr_read_b32 v3, a241\n"
"v_lshlrev_b32 v0, 16, v3\n"
"v_and_b32 v1, 0xffff0000, v3\n"
"v_pk_mul_f32 v[0:1], v[0:1], s[56:57]\n"
"v_cvt_pk_bf16_f32 v2, v0, v1\n"
"v_accvgpr_write_b32 a225, v2\n"
"v_accvgpr_read_b32 v7, a242\n"
"v_lshlrev_b32 v4, 16, v7\n"
"v_and_b32 v5, 0xffff0000, v7\n"
"v_pk_mul_f32 v[4:5], v[4:5], s[56:57]\n"
"v_cvt_pk_bf16_f32 v6, v4, v5\n"
"v_accvgpr_write_b32 a226, v6\n"
"v_accvgpr_read_b32 v3, a243\n"
"v_lshlrev_b32 v0, 16, v3\n"
"v_and_b32 v1, 0xffff0000, v3\n"
"v_pk_mul_f32 v[0:1], v[0:1], s[56:57]\n"
"v_cvt_pk_bf16_f32 v2, v0, v1\n"
"v_accvgpr_write_b32 a227, v2\n"
"v_accvgpr_read_b32 v7, a244\n"
"v_lshlrev_b32 v4, 16, v7\n"
"v_and_b32 v5, 0xffff0000, v7\n"
"v_pk_mul_f32 v[4:5], v[4:5], s[56:57]\n"
"v_cvt_pk_bf16_f32 v6, v4, v5\n"
"v_accvgpr_write_b32 a228, v6\n"
"v_accvgpr_read_b32 v3, a245\n"
"v_lshlrev_b32 v0, 16, v3\n"
"v_and_b32 v1, 0xffff0000, v3\n"
"v_pk_mul_f32 v[0:1], v[0:1], s[56:57]\n"
"v_cvt_pk_bf16_f32 v2, v0, v1\n"
"v_accvgpr_write_b32 a229, v2\n"
"v_accvgpr_read_b32 v7, a246\n"
"v_lshlrev_b32 v4, 16, v7\n"
"v_and_b32 v5, 0xffff0000, v7\n"
"v_pk_mul_f32 v[4:5], v[4:5], s[56:57]\n"
"v_cvt_pk_bf16_f32 v6, v4, v5\n"
"v_accvgpr_write_b32 a230, v6\n"
"v_accvgpr_read_b32 v3, a247\n"
"v_lshlrev_b32 v0, 16, v3\n"
"v_and_b32 v1, 0xffff0000, v3\n"
"v_pk_mul_f32 v[0:1], v[0:1], s[56:57]\n"
"v_cvt_pk_bf16_f32 v2, v0, v1\n"
"v_accvgpr_write_b32 a231, v2\n"
"v_accvgpr_read_b32 v7, a248\n"
"v_lshlrev_b32 v4, 16, v7\n"
"v_and_b32 v5, 0xffff0000, v7\n"
"v_pk_mul_f32 v[4:5], v[4:5], s[56:57]\n"
"v_cvt_pk_bf16_f32 v6, v4, v5\n"
"v_accvgpr_write_b32 a232, v6\n"
"v_accvgpr_read_b32 v3, a249\n"
"v_lshlrev_b32 v0, 16, v3\n"
"v_and_b32 v1, 0xffff0000, v3\n"
"v_pk_mul_f32 v[0:1], v[0:1], s[56:57]\n"
"v_cvt_pk_bf16_f32 v2, v0, v1\n"
"v_accvgpr_write_b32 a233, v2\n"
"v_accvgpr_read_b32 v7, a250\n"
"v_lshlrev_b32 v4, 16, v7\n"
"v_and_b32 v5, 0xffff0000, v7\n"
"v_pk_mul_f32 v[4:5], v[4:5], s[56:57]\n"
"v_cvt_pk_bf16_f32 v6, v4, v5\n"
"v_accvgpr_write_b32 a234, v6\n"
"v_accvgpr_read_b32 v3, a251\n"
"v_lshlrev_b32 v0, 16, v3\n"
"v_and_b32 v1, 0xffff0000, v3\n"
"v_pk_mul_f32 v[0:1], v[0:1], s[56:57]\n"
"v_cvt_pk_bf16_f32 v2, v0, v1\n"
"v_accvgpr_write_b32 a235, v2\n"
"v_accvgpr_read_b32 v7, a252\n"
"v_lshlrev_b32 v4, 16, v7\n"
"v_and_b32 v5, 0xffff0000, v7\n"
"v_pk_mul_f32 v[4:5], v[4:5], s[56:57]\n"
"v_cvt_pk_bf16_f32 v6, v4, v5\n"
"v_accvgpr_write_b32 a236, v6\n"
"v_accvgpr_read_b32 v3, a253\n"
"v_lshlrev_b32 v0, 16, v3\n"
"v_and_b32 v1, 0xffff0000, v3\n"
"v_pk_mul_f32 v[0:1], v[0:1], s[56:57]\n"
"v_cvt_pk_bf16_f32 v2, v0, v1\n"
"v_accvgpr_write_b32 a237, v2\n"
"v_accvgpr_read_b32 v7, a254\n"
"v_lshlrev_b32 v4, 16, v7\n"
"v_and_b32 v5, 0xffff0000, v7\n"
"v_pk_mul_f32 v[4:5], v[4:5], s[56:57]\n"
"v_cvt_pk_bf16_f32 v6, v4, v5\n"
"v_accvgpr_write_b32 a238, v6\n"
"v_accvgpr_read_b32 v3, a255\n"
"v_lshlrev_b32 v0, 16, v3\n"
"v_and_b32 v1, 0xffff0000, v3\n"
"v_pk_mul_f32 v[0:1], v[0:1], s[56:57]\n"
"v_cvt_pk_bf16_f32 v2, v0, v1\n"
"v_accvgpr_write_b32 a239, v2\n"
"v_mbcnt_lo_u32_b32 v0, -1, 0\n"
"v_mbcnt_hi_u32_b32 v0, -1, v0\n"
"v_and_b32 v1, 15, v0\n"
"v_lshrrev_b32 v2, 4, v0\n"
"v_and_b32 v3, 1, v2\n"
"v_lshrrev_b32 v4, 1, v2\n"
"v_lshl_add_u32 v3, v3, 1, v4\n"
"v_mov_b32 v7, 1.0\n"
"s_nop 1\n"
"v_mfma_f32_16x16x4_f32 v[152:155], v7, v172, 0\n"
"v_mfma_f32_16x16x4_f32 v[136:139], v7, v156, 0\n"
"v_mfma_f32_16x16x4_f32 v[140:143], v7, v160, 0\n"
"v_mfma_f32_16x16x4_f32 v[144:147], v7, v164, 0\n"
"v_mfma_f32_16x16x4_f32 v[148:151], v7, v168, 0\n"
"v_mov_b32 v6, v172\n"
"v_add_f32 v6, v6, v156\n"
"v_add_f32 v6, v6, v160\n"
"v_add_f32 v6, v6, v164\n"
"v_add_f32 v6, v6, v168\n"
"v_cmp_ngt_f32 vcc, s65, v6\n"
"s_or_b64 s[60:61], s[60:61], vcc\n"
"v_lshlrev_b32 v8, 4, v3\n"
"v_lshl_add_u32 v185, v1, 8, v8\n"
"s_nop 4\n"
"v_rcp_f32 v152, v152\n"
"v_rcp_f32 v136, v136\n"
"v_rcp_f32 v140, v140\n"
"v_rcp_f32 v144, v144\n"
"v_rcp_f32 v148, v148\n"
"v_accvgpr_read_b32 v16, a128\n"
"v_accvgpr_read_b32 v17, a129\n"
"v_accvgpr_read_b32 v18, a130\n"
"v_accvgpr_read_b32 v19, a131\n"
"v_accvgpr_read_b32 v20, a132\n"
"v_accvgpr_read_b32 v21, a133\n"
"v_accvgpr_read_b32 v22, a134\n"
"v_accvgpr_read_b32 v23, a135\n"
"v_pk_mul_f32 v[16:17], v[16:17], v[152:153] op_sel_hi:[1,0]\n"
"v_pk_mul_f32 v[18:19], v[18:19], v[152:153] op_sel_hi:[1,0]\n"
"v_pk_mul_f32 v[20:21], v[20:21], v[152:153] op_sel_hi:[1,0]\n"
"v_pk_mul_f32 v[22:23], v[22:23], v[152:153] op_sel_hi:[1,0]\n"
"v_cvt_pk_bf16_f32 v76, v16, v17\n"
"v_cvt_pk_bf16_f32 v77, v18, v19\n"
"v_cvt_pk_bf16_f32 v78, v20, v21\n"
"v_cvt_pk_bf16_f32 v79, v22, v23\n"
"v_accvgpr_read_b32 v24, a136\n"
"v_accvgpr_read_b32 v25, a137\n"
"v_accvgpr_read_b32 v26, a138\n"
"v_permlane16_swap_b32 v76, v78\n"
"v_permlane16_swap_b32 v77, v79\n"
"v_accvgpr_read_b32 v27, a139\n"
"v_accvgpr_read_b32 v28, a140\n"
"v_accvgpr_read_b32 v29, a141\n"
"v_accvgpr_read_b32 v30, a142\n"
"v_accvgpr_read_b32 v31, a143\n"
"v_pk_mul_f32 v[24:25], v[24:25], v[152:153] op_sel_hi:[1,0]\n"
"v_pk_mul_f32 v[26:27], v[26:27], v[152:153] op_sel_hi:[1,0]\n"
"v_pk_mul_f32 v[28:29], v[28:29], v[152:153] op_sel_hi:[1,0]\n"
"v_pk_mul_f32 v[30:31], v[30:31], v[152:153] op_sel_hi:[1,0]\n"
"v_cvt_pk_bf16_f32 v80, v24, v25\n"
"v_cvt_pk_bf16_f32 v81, v26, v27\n"
"v_cvt_pk_bf16_f32 v82, v28, v29\n"
"v_cvt_pk_bf16_f32 v83, v30, v31\n"
"v_accvgpr_read_b32 v16, a144\n"
"v_accvgpr_read_b32 v17, a145\n"
"v_accvgpr_read_b32 v18, a146\n"
"v_permlane16_swap_b32 v80, v82\n"
"v_permlane16_swap_b32 v81, v83\n"
"v_accvgpr_read_b32 v19, a147\n"
"v_accvgpr_read_b32 v20, a148\n"
"v_accvgpr_read_b32 v21, a149\n"
"v_accvgpr_read_b32 v22, a150\n"
"v_accvgpr_read_b32 v23, a151\n"
"v_pk_mul_f32 v[16:17], v[16:17], v[152:153] op_sel_hi:[1,0]\n"
"v_pk_mul_f32 v[18:19], v[18:19], v[152:153] op_sel_hi:[1,0]\n"
"v_pk_mul_f32 v[20:21], v[20:21], v[152:153] op_sel_hi:[1,0]\n"
"v_pk_mul_f32 v[22:23], v[22:23], v[152:153] op_sel_hi:[1,0]\n"
"v_cvt_pk_bf16_f32 v84, v16, v17\n"
"v_cvt_pk_bf16_f32 v85, v18, v19\n"
"v_cvt_pk_bf16_f32 v86, v20, v21\n"
"v_cvt_pk_bf16_f32 v87, v22, v23\n"
"v_accvgpr_read_b32 v24, a152\n"
"v_accvgpr_read_b32 v25, a153\n"
"v_accvgpr_read_b32 v26, a154\n"
"v_permlane16_swap_b32 v84, v86\n"
"v_permlane16_swap_b32 v85, v87\n"
"v_accvgpr_read_b32 v27, a155\n"
"v_accvgpr_read_b32 v28, a156\n"
"v_accvgpr_read_b32 v29, a157\n"
"v_accvgpr_read_b32 v30, a158\n"
"v_accvgpr_read_b32 v31, a159\n"
"v_pk_mul_f32 v[24:25], v[24:25], v[152:153] op_sel_hi:[1,0]\n"
"v_pk_mul_f32 v[26:27], v[26:27], v[152:153] op_sel_hi:[1,0]\n"
"v_pk_mul_f32 v[28:29], v[28:29], v[152:153] op_sel_hi:[1,0]\n"
"v_pk_mul_f32 v[30:31], v[30:31], v[152:153] op_sel_hi:[1,0]\n"
"v_cvt_pk_bf16_f32 v88, v24, v25\n"
"v_cvt_pk_bf16_f32 v89, v26, v27\n"
"v_cvt_pk_bf16_f32 v90, v28, v29\n"
"v_cvt_pk_bf16_f32 v91, v30, v31\n"
"s_nop 1\n"
"v_permlane16_swap_b32 v88, v90\n"
"v_permlane16_swap_b32 v89, v91\n"
"v_accvgpr_read_b32 v16, a0\n"
"v_accvgpr_read_b32 v17, a1\n"
"v_accvgpr_read_b32 v18, a2\n"
"v_accvgpr_read_b32 v19, a3\n"
"v_accvgpr_read_b32 v20, a4\n"
"v_accvgpr_read_b32 v21, a5\n"
"v_accvgpr_read_b32 v22, a6\n"
"v_accvgpr_read_b32 v23, a7\n"
"v_pk_mul_f32 v[16:17], v[16:17], v[136:137] op_sel_hi:[1,0]\n"
"v_pk_mul_f32 v[18:19], v[18:19], v[136:137] op_sel_hi:[1,0]\n"
"v_pk_mul_f32 v[20:21], v[20:21], v[136:137] op_sel_hi:[1,0]\n"
"v_pk_mul_f32 v[22:23], v[22:23], v[136:137] op_sel_hi:[1,0]\n"
"v_cvt_pk_bf16_f32 v192, v16, v17\n"
"v_cvt_pk_bf16_f32 v193, v18, v19\n"
"v_cvt_pk_bf16_f32 v194, v20, v21\n"
"v_cvt_pk_bf16_f32 v195, v22, v23\n"
"v_accvgpr_read_b32 v24, a8\n"
"v_accvgpr_read_b32 v25, a9\n"
"v_accvgpr_read_b32 v26, a10\n"
"v_permlane16_swap_b32 v192, v194\n"
"v_permlane16_swap_b32 v193, v195\n"
"v_accvgpr_read_b32 v27, a11\n"
"v_accvgpr_read_b32 v28, a12\n"
"v_accvgpr_read_b32 v29, a13\n"
"v_accvgpr_read_b32 v30, a14\n"
"v_accvgpr_read_b32 v31, a15\n"
"v_pk_mul_f32 v[24:25], v[24:25], v[136:137] op_sel_hi:[1,0]\n"
"v_pk_mul_f32 v[26:27], v[26:27], v[136:137] op_sel_hi:[1,0]\n"
"v_pk_mul_f32 v[28:29], v[28:29], v[136:137] op_sel_hi:[1,0]\n"
"v_pk_mul_f32 v[30:31], v[30:31], v[136:137] op_sel_hi:[1,0]\n"
"v_cvt_pk_bf16_f32 v196, v24, v25\n"
"v_cvt_pk_bf16_f32 v197, v26, v27\n"
"v_cvt_pk_bf16_f32 v198, v28, v29\n"
"v_cvt_pk_bf16_f32 v199, v30, v31\n"
"v_accvgpr_read_b32 v16, a16\n"
"v_accvgpr_read_b32 v17, a17\n"
"v_accvgpr_read_b32 v18, a18\n"
"v_permlane16_swap_b32 v196, v198\n"
"v_permlane16_swap_b32 v197, v199\n"
"v_accvgpr_read_b32 v19, a19\n"
"v_accvgpr_read_b32 v20, a20\n"
"v_accvgpr_read_b32 v21, a21\n"
"v_accvgpr_read_b32 v22, a22\n"
"v_accvgpr_read_b32 v23, a23\n"
"v_pk_mul_f32 v[16:17], v[16:17], v[136:137] op_sel_hi:[1,0]\n"
"v_pk_mul_f32 v[18:19], v[18:19], v[136:137] op_sel_hi:[1,0]\n"
"v_pk_mul_f32 v[20:21], v[20:21], v[136:137] op_sel_hi:[1,0]\n"
"v_pk_mul_f32 v[22:23], v[22:23], v[136:137] op_sel_hi:[1,0]\n"
"v_cvt_pk_bf16_f32 v200, v16, v17\n"
"v_cvt_pk_bf16_f32 v201, v18, v19\n"
"v_cvt_pk_bf16_f32 v202, v20, v21\n"
"v_cvt_pk_bf16_f32 v203, v22, v23\n"
"v_accvgpr_read_b32 v24, a24\n"
"v_accvgpr_read_b32 v25, a25\n"
"v_accvgpr_read_b32 v26, a26\n"
"v_permlane16_swap_b32 v200, v202\n"
"v_permlane16_swap_b32 v201, v203\n"
"v_accvgpr_read_b32 v27, a27\n"
"buffer_store_dwordx4 v[76:79], v185, s[48:51], s74 offen offset:0\n"
"v_accvgpr_read_b32 v28, a28\n"
"v_accvgpr_read_b32 v29, a29\n"
"v_accvgpr_read_b32 v30, a30\n"
"v_accvgpr_read_b32 v31, a31\n"
"v_pk_mul_f32 v[24:25], v[24:25], v[136:137] op_sel_hi:[1,0]\n"
"v_pk_mul_f32 v[26:27], v[26:27], v[136:137] op_sel_hi:[1,0]\n"
"v_pk_mul_f32 v[28:29], v[28:29], v[136:137] op_sel_hi:[1,0]\n"
"v_pk_mul_f32 v[30:31], v[30:31], v[136:137] op_sel_hi:[1,0]\n"
"v_cvt_pk_bf16_f32 v204, v24, v25\n"
"v_cvt_pk_bf16_f32 v205, v26, v27\n"
"v_cvt_pk_bf16_f32 v206, v28, v29\n"
"v_cvt_pk_bf16_f32 v207, v30, v31\n"
"s_nop 1\n"
"v_permlane16_swap_b32 v204, v206\n"
"v_permlane16_swap_b32 v205, v207\n"
"v_accvgpr_read_b32 v16, a32\n"
"v_accvgpr_read_b32 v17, a33\n"
"v_accvgpr_read_b32 v18, a34\n"
"v_accvgpr_read_b32 v19, a35\n"
"v_accvgpr_read_b32 v20, a36\n"
"v_accvgpr_read_b32 v21, a37\n"
"v_accvgpr_read_b32 v22, a38\n"
"v_accvgpr_read_b32 v23, a39\n"
"v_pk_mul_f32 v[16:17], v[16:17], v[140:141] op_sel_hi:[1,0]\n"
"v_pk_mul_f32 v[18:19], v[18:19], v[140:141] op_sel_hi:[1,0]\n"
"v_pk_mul_f32 v[20:21], v[20:21], v[140:141] op_sel_hi:[1,0]\n"
"v_pk_mul_f32 v[22:23], v[22:23], v[140:141] op_sel_hi:[1,0]\n"
"v_cvt_pk_bf16_f32 v208, v16, v17\n"
"v_cvt_pk_bf16_f32 v209, v18, v19\n"
"v_cvt_pk_bf16_f32 v210, v20, v21\n"
"v_cvt_pk_bf16_f32 v211, v22, v23\n"
"v_accvgpr_read_b32 v24, a40\n"
"v_accvgpr_read_b32 v25, a41\n"
"v_accvgpr_read_b32 v26, a42\n"
"v_permlane16_swap_b32 v208, v210\n"
"v_permlane16_swap_b32 v209, v211\n"
"v_accvgpr_read_b32 v27, a43\n"
"v_accvgpr_read_b32 v28, a44\n"
"v_accvgpr_read_b32 v29, a45\n"
"v_accvgpr_read_b32 v30, a46\n"
"v_accvgpr_read_b32 v31, a47\n"
"v_pk_mul_f32 v[24:25], v[24:25], v[140:141] op_sel_hi:[1,0]\n"
"v_pk_mul_f32 v[26:27], v[26:27], v[140:141] op_sel_hi:[1,0]\n"
"v_pk_mul_f32 v[28:29], v[28:29], v[140:141] op_sel_hi:[1,0]\n"
"v_pk_mul_f32 v[30:31], v[30:31], v[140:141] op_sel_hi:[1,0]\n"
"v_cvt_pk_bf16_f32 v212, v24, v25\n"
"v_cvt_pk_bf16_f32 v213, v26, v27\n"
"v_cvt_pk_bf16_f32 v214, v28, v29\n"
"v_cvt_pk_bf16_f32 v215, v30, v31\n"
"v_accvgpr_read_b32 v16, a48\n"
"v_accvgpr_read_b32 v17, a49\n"
"v_accvgpr_read_b32 v18, a50\n"
"v_permlane16_swap_b32 v212, v214\n"
"v_permlane16_swap_b32 v213, v215\n"
"v_accvgpr_read_b32 v19, a51\n"
"v_accvgpr_read_b32 v20, a52\n"
"v_accvgpr_read_b32 v21, a53\n"
"v_accvgpr_read_b32 v22, a54\n"
"v_accvgpr_read_b32 v23, a55\n"
"v_pk_mul_f32 v[16:17], v[16:17], v[140:141] op_sel_hi:[1,0]\n"
"v_pk_mul_f32 v[18:19], v[18:19], v[140:141] op_sel_hi:[1,0]\n"
"v_pk_mul_f32 v[20:21], v[20:21], v[140:141] op_sel_hi:[1,0]\n"
"v_pk_mul_f32 v[22:23], v[22:23], v[140:141] op_sel_hi:[1,0]\n"
"v_cvt_pk_bf16_f32 v216, v16, v17\n"
"v_cvt_pk_bf16_f32 v217, v18, v19\n"
"v_cvt_pk_bf16_f32 v218, v20, v21\n"
"v_cvt_pk_bf16_f32 v219, v22, v23\n"
"v_accvgpr_read_b32 v24, a56\n"
"v_accvgpr_read_b32 v25, a57\n"
"v_accvgpr_read_b32 v26, a58\n"
"v_permlane16_swap_b32 v216, v218\n"
"v_permlane16_swap_b32 v217, v219\n"
"v_accvgpr_read_b32 v27, a59\n"
"buffer_store_dwordx4 v[80:83], v185, s[48:51], s74 offen offset:64\n"
"v_accvgpr_read_b32 v28, a60\n"
"v_accvgpr_read_b32 v29, a61\n"
"v_accvgpr_read_b32 v30, a62\n"
"v_accvgpr_read_b32 v31, a63\n"
"v_pk_mul_f32 v[24:25], v[24:25], v[140:141] op_sel_hi:[1,0]\n"
"v_pk_mul_f32 v[26:27], v[26:27], v[140:141] op_sel_hi:[1,0]\n"
"v_pk_mul_f32 v[28:29], v[28:29], v[140:141] op_sel_hi:[1,0]\n"
"v_pk_mul_f32 v[30:31], v[30:31], v[140:141] op_sel_hi:[1,0]\n"
"v_cvt_pk_bf16_f32 v220, v24, v25\n"
"v_cvt_pk_bf16_f32 v221, v26, v27\n"
"v_cvt_pk_bf16_f32 v222, v28, v29\n"
"v_cvt_pk_bf16_f32 v223, v30, v31\n"
"s_nop 1\n"
"v_permlane16_swap_b32 v220, v222\n"
"v_permlane16_swap_b32 v221, v223\n"
"v_accvgpr_read_b32 v16, a64\n"
"v_accvgpr_read_b32 v17, a65\n"
"v_accvgpr_read_b32 v18, a66\n"
"v_accvgpr_read_b32 v19, a67\n"
"v_accvgpr_read_b32 v20, a68\n"
"v_accvgpr_read_b32 v21, a69\n"
"v_accvgpr_read_b32 v22, a70\n"
"v_accvgpr_read_b32 v23, a71\n"
"v_pk_mul_f32 v[16:17], v[16:17], v[144:145] op_sel_hi:[1,0]\n"
"v_pk_mul_f32 v[18:19], v[18:19], v[144:145] op_sel_hi:[1,0]\n"
"v_pk_mul_f32 v[20:21], v[20:21], v[144:145] op_sel_hi:[1,0]\n"
"v_pk_mul_f32 v[22:23], v[22:23], v[144:145] op_sel_hi:[1,0]\n"
"v_cvt_pk_bf16_f32 v224, v16, v17\n"
"v_cvt_pk_bf16_f32 v225, v18, v19\n"
"v_cvt_pk_bf16_f32 v226, v20, v21\n"
"v_cvt_pk_bf16_f32 v227, v22, v23\n"
"v_accvgpr_read_b32 v24, a72\n"
"v_accvgpr_read_b32 v25, a73\n"
"v_accvgpr_read_b32 v26, a74\n"
"v_permlane16_swap_b32 v224, v226\n"
"v_permlane16_swap_b32 v225, v227\n"
"v_accvgpr_read_b32 v27, a75\n"
"v_accvgpr_read_b32 v28, a76\n"
"v_accvgpr_read_b32 v29, a77\n"
"v_accvgpr_read_b32 v30, a78\n"
"v_accvgpr_read_b32 v31, a79\n"
"v_pk_mul_f32 v[24:25], v[24:25], v[144:145] op_sel_hi:[1,0]\n"
"v_pk_mul_f32 v[26:27], v[26:27], v[144:145] op_sel_hi:[1,0]\n"
"v_pk_mul_f32 v[28:29], v[28:29], v[144:145] op_sel_hi:[1,0]\n"
"v_pk_mul_f32 v[30:31], v[30:31], v[144:145] op_sel_hi:[1,0]\n"
"v_cvt_pk_bf16_f32 v228, v24, v25\n"
"v_cvt_pk_bf16_f32 v229, v26, v27\n"
"v_cvt_pk_bf16_f32 v230, v28, v29\n"
"v_cvt_pk_bf16_f32 v231, v30, v31\n"
"v_accvgpr_read_b32 v16, a80\n"
"v_accvgpr_read_b32 v17, a81\n"
"v_accvgpr_read_b32 v18, a82\n"
"v_permlane16_swap_b32 v228, v230\n"
"v_permlane16_swap_b32 v229, v231\n"
"v_accvgpr_read_b32 v19, a83\n"
"v_accvgpr_read_b32 v20, a84\n"
"v_accvgpr_read_b32 v21, a85\n"
"v_accvgpr_read_b32 v22, a86\n"
"v_accvgpr_read_b32 v23, a87\n"
"v_pk_mul_f32 v[16:17], v[16:17], v[144:145] op_sel_hi:[1,0]\n"
"v_pk_mul_f32 v[18:19], v[18:19], v[144:145] op_sel_hi:[1,0]\n"
"v_pk_mul_f32 v[20:21], v[20:21], v[144:145] op_sel_hi:[1,0]\n"
"v_pk_mul_f32 v[22:23], v[22:23], v[144:145] op_sel_hi:[1,0]\n"
"v_cvt_pk_bf16_f32 v232, v16, v17\n"
"v_cvt_pk_bf16_f32 v233, v18, v19\n"
"v_cvt_pk_bf16_f32 v234, v20, v21\n"
"v_cvt_pk_bf16_f32 v235, v22, v23\n"
"v_accvgpr_read_b32 v24, a88\n"
"v_accvgpr_read_b32 v25, a89\n"
"v_accvgpr_read_b32 v26, a90\n"
"v_permlane16_swap_b32 v232, v234\n"
"v_permlane16_swap_b32 v233, v235\n"
"v_accvgpr_read_b32 v27, a91\n"
"buffer_store_dwordx4 v[84:87], v185, s[48:51], s74 offen offset:128\n"
"v_accvgpr_read_b32 v28, a92\n"
"v_accvgpr_read_b32 v29, a93\n"
"v_accvgpr_read_b32 v30, a94\n"
"v_accvgpr_read_b32 v31, a95\n"
"v_pk_mul_f32 v[24:25], v[24:25], v[144:145] op_sel_hi:[1,0]\n"
"v_pk_mul_f32 v[26:27], v[26:27], v[144:145] op_sel_hi:[1,0]\n"
"v_pk_mul_f32 v[28:29], v[28:29], v[144:145] op_sel_hi:[1,0]\n"
"v_pk_mul_f32 v[30:31], v[30:31], v[144:145] op_sel_hi:[1,0]\n"
"v_cvt_pk_bf16_f32 v236, v24, v25\n"
"v_cvt_pk_bf16_f32 v237, v26, v27\n"
"v_cvt_pk_bf16_f32 v238, v28, v29\n"
"v_cvt_pk_bf16_f32 v239, v30, v31\n"
"s_nop 1\n"
"v_permlane16_swap_b32 v236, v238\n"
"v_permlane16_swap_b32 v237, v239\n"
"v_accvgpr_read_b32 v16, a96\n"
"v_accvgpr_read_b32 v17, a97\n"
"v_accvgpr_read_b32 v18, a98\n"
"v_accvgpr_read_b32 v19, a99\n"
"v_accvgpr_read_b32 v20, a100\n"
"v_accvgpr_read_b32 v21, a101\n"
"v_accvgpr_read_b32 v22, a102\n"
"v_accvgpr_read_b32 v23, a103\n"
"v_pk_mul_f32 v[16:17], v[16:17], v[148:149] op_sel_hi:[1,0]\n"
"v_pk_mul_f32 v[18:19], v[18:19], v[148:149] op_sel_hi:[1,0]\n"
"v_pk_mul_f32 v[20:21], v[20:21], v[148:149] op_sel_hi:[1,0]\n"
"v_pk_mul_f32 v[22:23], v[22:23], v[148:149] op_sel_hi:[1,0]\n"
"v_cvt_pk_bf16_f32 v240, v16, v17\n"
"v_cvt_pk_bf16_f32 v241, v18, v19\n"
"v_cvt_pk_bf16_f32 v242, v20, v21\n"
"v_cvt_pk_bf16_f32 v243, v22, v23\n"
"v_accvgpr_read_b32 v24, a104\n"
"v_accvgpr_read_b32 v25, a105\n"
"v_accvgpr_read_b32 v26, a106\n"
"v_permlane16_swap_b32 v240, v242\n"
"v_permlane16_swap_b32 v241, v243\n"
"v_accvgpr_read_b32 v27, a107\n"
"v_accvgpr_read_b32 v28, a108\n"
"v_accvgpr_read_b32 v29, a109\n"
"v_accvgpr_read_b32 v30, a110\n"
"v_accvgpr_read_b32 v31, a111\n"
"v_pk_mul_f32 v[24:25], v[24:25], v[148:149] op_sel_hi:[1,0]\n"
"v_pk_mul_f32 v[26:27], v[26:27], v[148:149] op_sel_hi:[1,0]\n"
"v_pk_mul_f32 v[28:29], v[28:29], v[148:149] op_sel_hi:[1,0]\n"
"v_pk_mul_f32 v[30:31], v[30:31], v[148:149] op_sel_hi:[1,0]\n"
"v_cvt_pk_bf16_f32 v244, v24, v25\n"
"v_cvt_pk_bf16_f32 v245, v26, v27\n"
"v_cvt_pk_bf16_f32 v246, v28, v29\n"
"v_cvt_pk_bf16_f32 v247, v30, v31\n"
"v_accvgpr_read_b32 v16, a112\n"
"v_accvgpr_read_b32 v17, a113\n"
"v_accvgpr_read_b32 v18, a114\n"
"v_permlane16_swap_b32 v244, v246\n"
"v_permlane16_swap_b32 v245, v247\n"
"v_accvgpr_read_b32 v19, a115\n"
"v_accvgpr_read_b32 v20, a116\n"
"v_accvgpr_read_b32 v21, a117\n"
"v_accvgpr_read_b32 v22, a118\n"
"v_accvgpr_read_b32 v23, a119\n"
"v_pk_mul_f32 v[16:17], v[16:17], v[148:149] op_sel_hi:[1,0]\n"
"v_pk_mul_f32 v[18:19], v[18:19], v[148:149] op_sel_hi:[1,0]\n"
"v_pk_mul_f32 v[20:21], v[20:21], v[148:149] op_sel_hi:[1,0]\n"
"v_pk_mul_f32 v[22:23], v[22:23], v[148:149] op_sel_hi:[1,0]\n"
"v_cvt_pk_bf16_f32 v248, v16, v17\n"
"v_cvt_pk_bf16_f32 v249, v18, v19\n"
"v_cvt_pk_bf16_f32 v250, v20, v21\n"
"v_cvt_pk_bf16_f32 v251, v22, v23\n"
"v_accvgpr_read_b32 v24, a120\n"
"v_accvgpr_read_b32 v25, a121\n"
"v_accvgpr_read_b32 v26, a122\n"
"v_permlane16_swap_b32 v248, v250\n"
"v_permlane16_swap_b32 v249, v251\n"
"v_accvgpr_read_b32 v27, a123\n"
"buffer_store_dwordx4 v[88:91], v185, s[48:51], s74 offen offset:192\n"
"v_accvgpr_read_b32 v28, a124\n"
"v_accvgpr_read_b32 v29, a125\n"
"v_accvgpr_read_b32 v30, a126\n"
"v_accvgpr_read_b32 v31, a127\n"
"v_pk_mul_f32 v[24:25], v[24:25], v[148:149] op_sel_hi:[1,0]\n"
"v_pk_mul_f32 v[26:27], v[26:27], v[148:149] op_sel_hi:[1,0]\n"
"v_pk_mul_f32 v[28:29], v[28:29], v[148:149] op_sel_hi:[1,0]\n"
"v_pk_mul_f32 v[30:31], v[30:31], v[148:149] op_sel_hi:[1,0]\n"
"v_cvt_pk_bf16_f32 v252, v24, v25\n"
"v_cvt_pk_bf16_f32 v253, v26, v27\n"
"v_cvt_pk_bf16_f32 v254, v28, v29\n"
"v_cvt_pk_bf16_f32 v255, v30, v31\n"
"s_nop 1\n"
"v_permlane16_swap_b32 v252, v254\n"
"v_permlane16_swap_b32 v253, v255\n"
"ds_read_b128 v[96:99], v180 offset:0\n"
"ds_read_b128 v[100:103], v180 offset:256\n"
"ds_read_b128 v[112:115], v180 offset:512\n"
"ds_read_b128 v[116:119], v180 offset:768\n"
"ds_read_b128 v[104:107], v180 offset:4096\n"
"ds_read_b128 v[108:111], v180 offset:4352\n"
"s_waitcnt lgkmcnt(2)\n"
"v_mfma_f32_16x16x32_bf16 v[56:59], v[96:99], a[160:163], 0\n"
"v_mfma_f32_16x16x32_bf16 v[56:59], v[100:103], a[164:167], v[56:59]\n"
"v_mfma_f32_16x16x32_bf16 v[56:59], v[112:115], a[168:171], v[56:59]\n"
"v_mfma_f32_16x16x32_bf16 v[56:59], v[116:119], a[172:175], v[56:59]\n"
"v_mfma_f32_16x16x32_bf16 v[60:63], v[96:99], a[176:179], 0\n"
"v_mfma_f32_16x16x32_bf16 v[60:63], v[100:103], a[180:183], v[60:63]\n"
"v_mfma_f32_16x16x32_bf16 v[60:63], v[112:115], a[184:187], v[60:63]\n"
"v_mfma_f32_16x16x32_bf16 v[60:63], v[116:119], a[188:191], v[60:63]\n"
"buffer_store_dwordx4 v[192:195], v185, s[48:51], s70 offen offset:0\n"
"v_mfma_f32_16x16x32_bf16 v[64:67], v[96:99], a[192:195], 0\n"
"v_mfma_f32_16x16x32_bf16 v[64:67], v[100:103], a[196:199], v[64:67]\n"
"v_mfma_f32_16x16x32_bf16 v[64:67], v[112:115], a[200:203], v[64:67]\n"
"v_mfma_f32_16x16x32_bf16 v[64:67], v[116:119], a[204:207], v[64:67]\n"
"v_mfma_f32_16x16x32_bf16 v[68:71], v[96:99], a[208:211], 0\n"
"v_mfma_f32_16x16x32_bf16 v[68:71], v[100:103], a[212:215], v[68:71]\n"
"v_mfma_f32_16x16x32_bf16 v[68:71], v[112:115], a[216:219], v[68:71]\n"
"v_mfma_f32_16x16x32_bf16 v[68:71], v[116:119], a[220:223], v[68:71]\n"
"v_mfma_f32_16x16x32_bf16 v[72:75], v[112:115], a[232:235], 0\n"
"ds_read_b128 v[112:115], v180 offset:4608\n"
"v_mfma_f32_16x16x32_bf16 v[72:75], v[116:119], a[236:239], v[72:75]\n"
"ds_read_b128 v[116:119], v180 offset:4864\n"
"v_mfma_f32_16x16x32_bf16 v[72:75], v[96:99], a[224:227], v[72:75]\n"
"v_mfma_f32_16x16x32_bf16 v[72:75], v[100:103], a[228:231], v[72:75]\n"
"s_nop 5\n"
"s_cmp_lg_u32 s66, 0\n"
"s_cbranch_scc1 L_given_max_a_%=\n"
"v_max3_f32 v136, v56, v57, v58\n"
"v_max_f32 v137, v136, v59\n"
"v_max_f32 v136, v136, v59\n"
"v_max3_f32 v140, v60, v61, v62\n"
"v_max_f32 v141, v140, v63\n"
"v_max_f32 v140, v140, v63\n"
"v_max3_f32 v144, v64, v65, v66\n"
"v_max_f32 v145, v144, v67\n"
"v_max_f32 v144, v144, v67\n"
"v_max3_f32 v148, v68, v69, v70\n"
"v_max_f32 v149, v148, v71\n"
"v_max_f32 v148, v148, v71\n"
"v_max3_f32 v152, v72, v73, v74\n"
"v_max_f32 v153, v152, v75\n"
"v_max_f32 v152, v152, v75\n"
"s_nop 1\n"
"v_permlane32_swap_b32 v136, v137\n"
"v_permlane32_swap_b32 v140, v141\n"
"v_permlane32_swap_b32 v144, v145\n"
"v_permlane32_swap_b32 v148, v149\n"
"v_permlane32_swap_b32 v152, v153\n"
"s_nop 1\n"
"v_max_f32 v138, v136, v137\n"
"v_max_f32 v139, v136, v137\n"
"v_max_f32 v142, v140, v141\n"
"v_max_f32 v143, v140, v141\n"
"v_max_f32 v146, v144, v145\n"
"v_max_f32 v147, v144, v145\n"
"v_max_f32 v150, v148, v149\n"
"v_max_f32 v151, v148, v149\n"
"v_max_f32 v154, v152, v153\n"
"v_max_f32 v155, v152, v153\n"
"s_nop 1\n"
"v_permlane16_swap_b32 v138, v139\n"
"v_permlane16_swap_b32 v142, v143\n"
"v_permlane16_swap_b32 v146, v147\n"
"v_permlane16_swap_b32 v150, v151\n"
"v_permlane16_swap_b32 v154, v155\n"
"s_nop 1\n"
"v_max_f32 v136, v138, v139\n"
"v_max_f32 v140, v142, v143\n"
"v_max_f32 v144, v146, v147\n"
"v_max_f32 v148, v150, v151\n"
"v_max_f32 v152, v154, v155\n"
"v_sub_f32 v136, 0, v136\n"
"v_sub_f32 v140, 0, v140\n"
"v_sub_f32 v144, 0, v144\n"
"v_sub_f32 v148, 0, v148\n"
"v_sub_f32 v152, 0, v152\n"
"s_branch L_have_max_a_%=\n"
"L_given_max_a_%=:\n"
"v_mbcnt_lo_u32_b32 v186, -1, 0\n"
"v_mbcnt_hi_u32_b32 v186, -1, v186\n"
"v_lshlrev_b32 v186, 2, v186\n"
"global_load_dword v136, v186, s[68:69] offset:1280\n"
"global_load_dword v140, v186, s[68:69] offset:1536\n"
"global_load_dword v144, v186, s[68:69] offset:1792\n"
"global_load_dword v148, v186, s[68:69] offset:2048\n"
"global_load_dword v152, v186, s[68:69] offset:2304\n"
"s_waitcnt vmcnt(0)\n"
"L_have_max_a_%=:\n"
"v_mov_b32 v137, v136\n"
"v_mov_b32 v138, v136\n"
"v_mov_b32 v139, v136\n"
"v_pk_add_f32 v[56:57], v[56:57], v[136:137]\n"
"v_pk_add_f32 v[58:59], v[58:59], v[136:137]\n"
"v_mov_b32 v141, v140\n"
"v_mov_b32 v142, v140\n"
"v_mov_b32 v143, v140\n"
"v_pk_add_f32 v[60:61], v[60:61], v[140:141]\n"
"v_pk_add_f32 v[62:63], v[62:63], v[140:141]\n"
"v_mov_b32 v145, v144\n"
"v_mov_b32 v146, v144\n"
"v_mov_b32 v147, v144\n"
"v_pk_add_f32 v[64:65], v[64:65], v[144:145]\n"
"v_pk_add_f32 v[66:67], v[66:67], v[144:145]\n"
"v_mov_b32 v149, v148\n"
"v_mov_b32 v150, v148\n"
"v_mov_b32 v151, v148\n"
"v_pk_add_f32 v[68:69], v[68:69], v[148:149]\n"
"v_pk_add_f32 v[70:71], v[70:71], v[148:149]\n"
"v_mov_b32 v153, v152\n"
"v_mov_b32 v154, v152\n"
"v_mov_b32 v155, v152\n"
"v_pk_add_f32 v[72:73], v[72:73], v[152:153]\n"
"v_pk_add_f32 v[74:75], v[74:75], v[152:153]\n"
"s_waitcnt lgkmcnt(0)\n"
"v_mfma_f32_16x16x32_bf16 v[4:7], v[104:107], a[160:163], v[136:139]\n"
"v_exp_f32 v56, v56\n"
"v_exp_f32 v57, v57\n"
"v_mfma_f32_16x16x32_bf16 v[4:7], v[108:111], a[164:167], v[4:7]\n"
"v_exp_f32 v58, v58\n"
"v_cvt_pk_bf16_f32 v16, v56, v57\n"
"v_mfma_f32_16x16x32_bf16 v[4:7], v[112:115], a[168:171], v[4:7]\n"
"buffer_store_dwordx4 v[196:199], v185, s[48:51], s70 offen offset:64\n"
"v_exp_f32 v59, v59\n"
"v_mfma_f32_16x16x32_bf16 v[4:7], v[116:119], a[172:175], v[4:7]\n"
"v_exp_f32 v60, v60\n"
"v_exp_f32 v61, v61\n"
"v_mfma_f32_16x16x32_bf16 v[8:11], v[104:107], a[176:179], v[140:143]\n"
"ds_read_b128 v[96:99], v180 offset:8192\n"
"v_cvt_pk_bf16_f32 v17, v58, v59\n"
"v_exp_f32 v62, v62\n"
"v_mfma_f32_16x16x32_bf16 v[8:11], v[108:111], a[180:183], v[8:11]\n"
"v_exp_f32 v63, v63\n"
"v_exp_f32 v64, v64\n"
"v_mfma_f32_16x16x32_bf16 v[8:11], v[112:115], a[184:187], v[8:11]\n"
"v_cvt_pk_bf16_f32 v24, v60, v61\n"
"v_exp_f32 v65, v65\n"
"v_cvt_pk_bf16_f32 v25, v62, v63\n"
"v_mfma_f32_16x16x32_bf16 v[8:11], v[116:119], a[188:191], v[8:11]\n"
"v_exp_f32 v66, v66\n"
"v_exp_f32 v67, v67\n"
"v_mfma_f32_16x16x32_bf16 v[12:15], v[104:107], a[192:195], v[144:147]\n"
"ds_read_b128 v[100:103], v180 offset:8448\n"
"v_cvt_pk_bf16_f32 v32, v64, v65\n"
"v_exp_f32 v68, v68\n"
"v_mfma_f32_16x16x32_bf16 v[12:15], v[108:111], a[196:199], v[12:15]\n"
"v_exp_f32 v69, v69\n"
"v_exp_f32 v70, v70\n"
"v_mfma_f32_16x16x32_bf16 v[12:15], v[112:115], a[200:203], v[12:15]\n"
"v_cvt_pk_bf16_f32 v33, v66, v67\n"
"v_exp_f32 v71, v71\n"
"v_cvt_pk_bf16_f32 v40, v68, v69\n"
"v_mfma_f32_16x16x32_bf16 v[12:15], v[116:119], a[204:207], v[12:15]\n"
"v_exp_f32 v72, v72\n"
"v_exp_f32 v73, v73\n"
"v_mfma_f32_16x16x32_bf16 v[0:3], v[104:107], a[208:211], v[148:151]\n"
"v_cvt_pk_bf16_f32 v41, v70, v71\n"
"v_exp_f32 v74, v74\n"
"v_cvt_pk_bf16_f32 v48, v72, v73\n"
"v_mfma_f32_16x16x32_bf16 v[0:3], v[108:111], a[212:215], v[0:3]\n"
"v_exp_f32 v75, v75\n"
"v_exp_f32 v4, v4\n"
"v_mfma_f32_16x16x32_bf16 v[0:3], v[112:115], a[216:219], v[0:3]\n"
"buffer_store_dwordx4 v[200:203], v185, s[48:51], s70 offen offset:128\n"
"v_exp_f32 v5, v5\n"
"v_cvt_pk_bf16_f32 v49, v74, v75\n"
"v_mfma_f32_16x16x32_bf16 v[0:3], v[116:119], a[220:223], v[0:3]\n"
"v_exp_f32 v6, v6\n"
"v_exp_f32 v7, v7\n"
"v_cvt_pk_bf16_f32 v18, v4, v5\n"
"s_nop 0\n"
"v_cvt_pk_bf16_f32 v19, v6, v7\n"
"v_mfma_f32_16x16x32_bf16 v[4:7], v[112:115], a[232:235], v[152:155]\n"
"ds_read_b128 v[112:115], v180 offset:8704\n"
"v_exp_f32 v8, v8\n"
"v_mfma_f32_16x16x32_bf16 v[4:7], v[116:119], a[236:239], v[4:7]\n"
"ds_read_b128 v[116:119], v180 offset:8960\n"
"v_exp_f32 v9, v9\n"
"v_mfma_f32_16x16x32_bf16 v[4:7], v[104:107], a[224:227], v[4:7]\n"
"v_exp_f32 v10, v10\n"
"v_exp_f32 v11, v11\n"
"v_mfma_f32_16x16x32_bf16 v[4:7], v[108:111], a[228:231], v[4:7]\n"
"v_cvt_pk_bf16_f32 v26, v8, v9\n"
"v_exp_f32 v12, v12\n"
"s_waitcnt lgkmcnt(2)\n"
"v_cvt_pk_bf16_f32 v27, v10, v11\n"
"v_mfma_f32_16x16x32_bf16 v[8:11], v[96:99], a[160:163], v[136:139]\n"
"v_exp_f32 v13, v13\n"
"v_exp_f32 v14, v14\n"
"v_mfma_f32_16x16x32_bf16 v[8:11], v[100:103], a[164:167], v[8:11]\n"
"v_exp_f32 v15, v15\n"
"v_cvt_pk_bf16_f32 v34, v12, v13\n"
"s_waitcnt lgkmcnt(0)\n"
"v_mfma_f32_16x16x32_bf16 v[8:11], v[112:115], a[168:171], v[8:11]\n"
"v_exp_f32 v0, v0\n"
"v_exp_f32 v1, v1\n"
"v_mfma_f32_16x16x32_bf16 v[8:11], v[116:119], a[172:175], v[8:11]\n"
"v_cvt_pk_bf16_f32 v35, v14, v15\n"
"v_exp_f32 v2, v2\n"
"v_cvt_pk_bf16_f32 v42, v0, v1\n"
"v_mfma_f32_16x16x32_bf16 v[12:15], v[96:99], a[176:179], v[140:143]\n"
"ds_read_b128 v[104:107], v180 offset:12288\n"
"v_exp_f32 v3, v3\n"
"v_mfma_f32_16x16x32_bf16 v[12:15], v[100:103], a[180:183], v[12:15]\n"
"v_exp_f32 v4, v4\n"
"v_exp_f32 v5, v5\n"
"v_mfma_f32_16x16x32_bf16 v[12:15], v[112:115], a[184:187], v[12:15]\n"
"buffer_store_dwordx4 v[204:207], v185, s[48:51], s70 offen offset:192\n"
"v_cvt_pk_bf16_f32 v43, v2, v3\n"
"v_exp_f32 v6, v6\n"
"v_mfma_f32_16x16x32_bf16 v[12:15], v[116:119], a[188:191], v[12:15]\n"
"v_exp_f32 v7, v7\n"
"v_cvt_pk_bf16_f32 v50, v4, v5\n"
"v_mfma_f32_16x16x32_bf16 v[0:3], v[96:99], a[192:195], v[144:147]\n"
"ds_read_b128 v[108:111], v180 offset:12544\n"
"v_exp_f32 v8, v8\n"
"v_cvt_pk_bf16_f32 v51, v6, v7\n"
"v_mfma_f32_16x16x32_bf16 v[0:3], v[100:103], a[196:199], v[0:3]\n"
"v_exp_f32 v9, v9\n"
"v_exp_f32 v10, v10\n"
"v_mfma_f32_16x16x32_bf16 v[0:3], v[112:115], a[200:203], v[0:3]\n"
"v_exp_f32 v11, v11\n"
"v_cvt_pk_bf16_f32 v20, v8, v9\n"
"v_mfma_f32_16x16x32_bf16 v[0:3], v[116:119], a[204:207], v[0:3]\n"
"v_cvt_pk_bf16_f32 v21, v10, v11\n"
"v_mfma_f32_16x16x32_bf16 v[4:7], v[96:99], a[208:211], v[148:151]\n"
"v_exp_f32 v12, v12\n"
"v_exp_f32 v13, v13\n"
"v_mfma_f32_16x16x32_bf16 v[4:7], v[100:103], a[212:215], v[4:7]\n"
"v_exp_f32 v14, v14\n"
"v_exp_f32 v15, v15\n"
"v_mfma_f32_16x16x32_bf16 v[4:7], v[112:115], a[216:219], v[4:7]\n"
"v_cvt_pk_bf16_f32 v28, v12, v13\n"
"v_cvt_pk_bf16_f32 v29, v14, v15\n"
"v_mfma_f32_16x16x32_bf16 v[4:7], v[116:119], a[220:223], v[4:7]\n"
"v_mfma_f32_16x16x32_bf16 v[8:11], v[112:115], a[232:235], v[152:155]\n"
"ds_read_b128 v[112:115], v180 offset:12800\n"
"v_exp_f32 v0, v0\n"
"v_mfma_f32_16x16x32_bf16 v[8:11], v[116:119], a[236:239], v[8:11]\n"
"ds_read_b128 v[116:119], v180 offset:13056\n"
"v_exp_f32 v1, v1\n"
"v_mfma_f32_16x16x32_bf16 v[8:11], v[96:99], a[224:227], v[8:11]\n"
"v_exp_f32 v2, v2\n"
"v_exp_f32 v3, v3\n"
"v_mfma_f32_16x16x32_bf16 v[8:11], v[100:103], a[228:231], v[8:11]\n"
"v_cvt_pk_bf16_f32 v36, v0, v1\n"
"v_cvt_pk_bf16_f32 v37, v2, v3\n"
"s_waitcnt lgkmcnt(2)\n"
"v_mfma_f32_16x16x32_bf16 v[12:15], v[104:107], a[160:163], v[136:139]\n"
"v_exp_f32 v4, v4\n"
"v_exp_f32 v5, v5\n"
"v_mfma_f32_16x16x32_bf16 v[12:15], v[108:111], a[164:167], v[12:15]\n"
"s_waitcnt vmcnt(24) lgkmcnt(0)\n"
"v_xor_b32 v181, 0x8000, v181\n"
"v_xor_b32 v182, 0x8000, v182\n"
"s_barrier\n"
"v_mfma_f32_16x16x32_bf16 v[12:15], v[112:115], a[168:171], v[12:15]\n"
"v_exp_f32 v6, v6\n"
"v_exp_f32 v7, v7\n"
"v_mfma_f32_16x16x32_bf16 v[12:15], v[116:119], a[172:175], v[12:15]\n"
"ds_read_b64_tr_b16 v[120:121], v181 offset:0\n"
"v_cvt_pk_bf16_f32 v44, v4, v5\n"
"v_cvt_pk_bf16_f32 v45, v6, v7\n"
"v_mfma_f32_16x16x32_bf16 v[0:3], v[104:107], a[176:179], v[140:143]\n"
"ds_read_b128 v[96:99], v180 offset:16384\n"
"v_exp_f32 v8, v8\n"
"v_mfma_f32_16x16x32_bf16 v[0:3], v[108:111], a[180:183], v[0:3]\n"
"ds_read_b64_tr_b16 v[122:123], v181 offset:4096\n"
"v_exp_f32 v9, v9\n"
"v_mfma_f32_16x16x32_bf16 v[0:3], v[112:115], a[184:187], v[0:3]\n"
"s_add_u32 m0, s54, 0xc000\n"
"v_exp_f32 v10, v10\n"
"v_cvt_pk_bf16_f32 v52, v8, v9\n"
"v_mfma_f32_16x16x32_bf16 v[0:3], v[116:119], a[188:191], v[0:3]\n"
"ds_read_b64_tr_b16 v[124:125], v181 offset:8192\n"
"v_exp_f32 v11, v11\n"
"v_mfma_f32_16x16x32_bf16 v[4:7], v[104:107], a[192:195], v[144:147]\n"
"ds_read_b128 v[100:103], v180 offset:16640\n"
"v_exp_f32 v12, v12\n"
"v_cvt_pk_bf16_f32 v53, v10, v11\n"
"v_mfma_f32_16x16x32_bf16 v[4:7], v[108:111], a[196:199], v[4:7]\n"
"ds_read_b64_tr_b16 v[126:127], v181 offset:12288\n"
"v_exp_f32 v13, v13\n"
"v_mfma_f32_16x16x32_bf16 v[4:7], v[112:115], a[200:203], v[4:7]\n"
"buffer_load_dwordx4 v183, s[40:43], s52 offen offset:0 lds\n"
"v_exp_f32 v14, v14\n"
"v_cvt_pk_bf16_f32 v22, v12, v13\n"
"v_mfma_f32_16x16x32_bf16 v[4:7], v[116:119], a[204:207], v[4:7]\n"
"buffer_store_dwordx4 v[208:211], v185, s[48:51], s71 offen offset:0\n"
"v_exp_f32 v15, v15\n"
"v_mfma_f32_16x16x32_bf16 v[8:11], v[104:107], a[208:211], v[148:151]\n"
"v_exp_f32 v0, v0\n"
"v_exp_f32 v1, v1\n"
"v_mfma_f32_16x16x32_bf16 v[8:11], v[108:111], a[212:215], v[8:11]\n"
"buffer_store_dwordx4 v[212:215], v185, s[48:51], s71 offen offset:64\n"
"v_cvt_pk_bf16_f32 v23, v14, v15\n"
"v_exp_f32 v2, v2\n"
"v_mfma_f32_16x16x32_bf16 v[8:11], v[112:115], a[216:219], v[8:11]\n"
"v_exp_f32 v3, v3\n"
"v_cvt_pk_bf16_f32 v30, v0, v1\n"
"v_mfma_f32_16x16x32_bf16 v[8:11], v[116:119], a[220:223], v[8:11]\n"
"v_cvt_pk_bf16_f32 v31, v2, v3\n"
"v_mfma_f32_16x16x32_bf16 v[12:15], v[112:115], a[232:235], v[152:155]\n"
"ds_read_b128 v[112:115], v180 offset:16896\n"
"v_exp_f32 v4, v4\n"
"v_mfma_f32_16x16x32_bf16 v[12:15], v[116:119], a[236:239], v[12:15]\n"
"ds_read_b128 v[116:119], v180 offset:17152\n"
"v_exp_f32 v5, v5\n"
"v_mfma_f32_16x16x32_bf16 v[12:15], v[104:107], a[224:227], v[12:15]\n"
"v_exp_f32 v6, v6\n"
"v_exp_f32 v7, v7\n"
"v_mfma_f32_16x16x32_bf16 v[12:15], v[108:111], a[228:231], v[12:15]\n"
"v_cvt_pk_bf16_f32 v38, v4, v5\n"
"v_cvt_pk_bf16_f32 v39, v6, v7\n"
"s_nop 7\n"
"v_exp_f32 v8, v8\n"
"v_exp_f32 v9, v9\n"
"v_exp_f32 v10, v10\n"
"v_exp_f32 v11, v11\n"
"s_nop 0\n"
"v_cvt_pk_bf16_f32 v46, v8, v9\n"
"v_cvt_pk_bf16_f32 v47, v10, v11\n"
"s_waitcnt lgkmcnt(2)\n"
"v_mfma_f32_16x16x32_bf16 v[0:3], v[96:99], a[160:163], v[136:139]\n"
"v_exp_f32 v12, v12\n"
"v_mfma_f32_16x16x32_bf16 v[0:3], v[100:103], a[164:167], v[0:3]\n"
"s_waitcnt lgkmcnt(0)\n"
"v_mfma_f32_16x16x32_bf16 v[0:3], v[112:115], a[168:171], v[0:3]\n"
"buffer_load_dwordx4 v184, s[40:43], s52 offen offset:1024 lds\n"
"v_mfma_f32_16x16x32_bf16 v[0:3], v[116:119], a[172:175], v[0:3]\n"
"v_exp_f32 v13, v13\n"
"v_mfma_f32_16x16x32_bf16 a[0:3], v[120:123], v[16:19], 0\n"
"ds_read_b128 v[104:107], v180 offset:20480\n"
"v_cvt_pk_bf16_f32 v54, v12, v13\n"
"v_mfma_f32_16x16x32_bf16 a[0:3], v[124:127], v[20:23], a[0:3]\n"
"ds_read_b64_tr_b16 v[128:129], v182 offset:0\n"
"v_mfma_f32_16x16x32_bf16 a[32:35], v[120:123], v[24:27], 0\n"
"v_exp_f32 v14, v14\n"
"v_mfma_f32_16x16x32_bf16 a[32:35], v[124:127], v[28:31], a[32:35]\n"
"ds_read_b64_tr_b16 v[130:131], v182 offset:4096\n"
"v_mfma_f32_16x16x32_bf16 v[4:7], v[96:99], a[176:179], v[140:143]\n"
"v_exp_f32 v15, v15\n"
"v_mfma_f32_16x16x32_bf16 v[4:7], v[100:103], a[180:183], v[4:7]\n"
"ds_read_b64_tr_b16 v[132:133], v182 offset:8192\n"
"v_cvt_pk_bf16_f32 v55, v14, v15\n"
"v_mfma_f32_16x16x32_bf16 v[4:7], v[112:115], a[184:187], v[4:7]\n"
"buffer_load_dwordx4 v183, s[40:43], s52 offen offset:2048 lds\n"
"v_mfma_f32_16x16x32_bf16 v[4:7], v[116:119], a[188:191], v[4:7]\n"
"ds_read_b64_tr_b16 v[134:135], v182 offset:12288\n"
"v_mfma_f32_16x16x32_bf16 a[64:67], v[120:123], v[32:35], 0\n"
"v_exp_f32 v0, v0\n"
"v_mfma_f32_16x16x32_bf16 a[64:67], v[124:127], v[36:39], a[64:67]\n"
"v_exp_f32 v1, v1\n"
"v_mfma_f32_16x16x32_bf16 a[96:99], v[120:123], v[40:43], 0\n"
"ds_read_b128 v[108:111], v180 offset:20736\n"
"v_cvt_pk_bf16_f32 v56, v0, v1\n"
"v_mfma_f32_16x16x32_bf16 a[96:99], v[124:127], v[44:47], a[96:99]\n"
"v_exp_f32 v2, v2\n"
"v_mfma_f32_16x16x32_bf16 v[8:11], v[96:99], a[192:195], v[144:147]\n"
"v_exp_f32 v3, v3\n"
"v_mfma_f32_16x16x32_bf16 v[8:11], v[100:103], a[196:199], v[8:11]\n"
"v_exp_f32 v4, v4\n"
"v_mfma_f32_16x16x32_bf16 v[8:11], v[112:115], a[200:203], v[8:11]\n"
"v_exp_f32 v5, v5\n"
"v_mfma_f32_16x16x32_bf16 v[8:11], v[116:119], a[204:207], v[8:11]\n"
"buffer_store_dwordx4 v[216:219], v185, s[48:51], s71 offen offset:128\n"
"v_cvt_pk_bf16_f32 v57, v2, v3\n"
"v_mfma_f32_16x16x32_bf16 a[128:131], v[120:123], v[48:51], 0\n"
"v_exp_f32 v6, v6\n"
"v_mfma_f32_16x16x32_bf16 a[128:131], v[124:127], v[52:55], a[128:131]\n"
"v_cvt_pk_bf16_f32 v64, v4, v5\n"
"s_waitcnt lgkmcnt(1)\n"
"v_mfma_f32_16x16x32_bf16 a[4:7], v[128:131], v[16:19], 0\n"
"ds_read_b64_tr_b16 v[120:121], v181 offset:256\n"
"v_mfma_f32_16x16x32_bf16 a[4:7], v[132:135], v[20:23], a[4:7]\n"
"v_exp_f32 v7, v7\n"
"v_mfma_f32_16x16x32_bf16 v[12:15], v[96:99], a[208:211], v[148:151]\n"
"v_exp_f32 v8, v8\n"
"v_mfma_f32_16x16x32_bf16 v[12:15], v[100:103], a[212:215], v[12:15]\n"
"ds_read_b64_tr_b16 v[122:123], v181 offset:4352\n"
"v_cvt_pk_bf16_f32 v65, v6, v7\n"
"v_mfma_f32_16x16x32_bf16 v[12:15], v[112:115], a[216:219], v[12:15]\n"
"v_exp_f32 v9, v9\n"
"v_mfma_f32_16x16x32_bf16 v[12:15], v[116:119], a[220:223], v[12:15]\n"
"buffer_load_dwordx4 v184, s[40:43], s52 offen offset:3072 lds\n"
"v_cvt_pk_bf16_f32 v72, v8, v9\n"
"v_mfma_f32_16x16x32_bf16 a[36:39], v[128:131], v[24:27], 0\n"
"ds_read_b64_tr_b16 v[124:125], v181 offset:8448\n"
"v_mfma_f32_16x16x32_bf16 a[36:39], v[132:135], v[28:31], a[36:39]\n"
"v_exp_f32 v10, v10\n"
"v_mfma_f32_16x16x32_bf16 a[68:71], v[128:131], v[32:35], 0\n"
"v_exp_f32 v11, v11\n"
"v_mfma_f32_16x16x32_bf16 a[68:71], v[132:135], v[36:39], a[68:71]\n"
"ds_read_b64_tr_b16 v[126:127], v181 offset:12544\n"
"v_cvt_pk_bf16_f32 v73, v10, v11\n"
"v_mfma_f32_16x16x32_bf16 v[0:3], v[112:115], a[232:235], v[152:155]\n"
"ds_read_b128 v[112:115], v180 offset:20992\n"
"v_mfma_f32_16x16x32_bf16 v[0:3], v[116:119], a[236:239], v[0:3]\n"
"ds_read_b128 v[116:119], v180 offset:21248\n"
"v_mfma_f32_16x16x32_bf16 v[0:3], v[96:99], a[224:227], v[0:3]\n"
"v_exp_f32 v12, v12\n"
"v_mfma_f32_16x16x32_bf16 v[0:3], v[100:103], a[228:231], v[0:3]\n"
"v_exp_f32 v13, v13\n"
"v_mfma_f32_16x16x32_bf16 a[100:103], v[128:131], v[40:43], 0\n"
"buffer_store_dwordx4 v[220:223], v185, s[48:51], s71 offen offset:192\n"
"v_cvt_pk_bf16_f32 v80, v12, v13\n"
"v_mfma_f32_16x16x32_bf16 a[100:103], v[132:135], v[44:47], a[100:103]\n"
"v_exp_f32 v14, v14\n"
"v_mfma_f32_16x16x32_bf16 a[132:135], v[128:131], v[48:51], 0\n"
"v_exp_f32 v15, v15\n"
"v_mfma_f32_16x16x32_bf16 a[132:135], v[132:135], v[52:55], a[132:135]\n"
"v_cvt_pk_bf16_f32 v81, v14, v15\n"
"v_mfma_f32_16x16x32_bf16 v[156:159], v[176:179], v[16:19], 0\n"
"ds_read_b64_tr_b16 v[128:129], v182 offset:256\n"
"v_mfma_f32_16x16x32_bf16 v[156:159], v[176:179], v[20:23], v[156:159]\n"
"s_waitcnt lgkmcnt(7)\n"
"v_mfma_f32_16x16x32_bf16 v[4:7], v[104:107], a[160:163], v[136:139]\n"
"v_exp_f32 v0, v0\n"
"v_mfma_f32_16x16x32_bf16 v[4:7], v[108:111], a[164:167], v[4:7]\n"
"s_waitcnt lgkmcnt(1)\n"
"v_mfma_f32_16x16x32_bf16 v[4:7], v[112:115], a[168:171], v[4:7]\n"
"ds_read_b64_tr_b16 v[130:131], v182 offset:4352\n"
"v_mfma_f32_16x16x32_bf16 v[4:7], v[116:119], a[172:175], v[4:7]\n"
"s_add_u32 m0, s53, 0x0\n"
"v_mfma_f32_16x16x32_bf16 a[8:11], v[120:123], v[16:19], 0\n"
"ds_read_b128 v[96:99], v180 offset:24576\n"
"v_mfma_f32_16x16x32_bf16 a[8:11], v[124:127], v[20:23], a[8:11]\n"
"ds_read_b64_tr_b16 v[132:133], v182 offset:8448\n"
"v_mfma_f32_16x16x32_bf16 a[40:43], v[120:123], v[24:27], 0\n"
"v_exp_f32 v1, v1\n"
"v_mfma_f32_16x16x32_bf16 a[40:43], v[124:127], v[28:31], a[40:43]\n"
"ds_read_b64_tr_b16 v[134:135], v182 offset:12544\n"
"v_cvt_pk_bf16_f32 v88, v0, v1\n"
"v_mfma_f32_16x16x32_bf16 v[8:11], v[104:107], a[176:179], v[140:143]\n"
"v_exp_f32 v2, v2\n"
"v_mfma_f32_16x16x32_bf16 v[8:11], v[108:111], a[180:183], v[8:11]\n"
"v_exp_f32 v3, v3\n"
"v_mfma_f32_16x16x32_bf16 v[8:11], v[112:115], a[184:187], v[8:11]\n"
"v_exp_f32 v4, v4\n"
"v_mfma_f32_16x16x32_bf16 v[8:11], v[116:119], a[188:191], v[8:11]\n"
"buffer_load_dwordx4 v183, s[36:39], s52 offen offset:0 lds\n"
"v_cvt_pk_bf16_f32 v89, v2, v3\n"
"v_mfma_f32_16x16x32_bf16 a[72:75], v[120:123], v[32:35], 0\n"
"v_exp_f32 v5, v5\n"
"v_mfma_f32_16x16x32_bf16 a[72:75], v[124:127], v[36:39], a[72:75]\n"
"v_exp_f32 v6, v6\n"
"v_mfma_f32_16x16x32_bf16 a[104:107], v[120:123], v[40:43], 0\n"
"ds_read_b128 v[100:103], v180 offset:24832\n"
"v_cvt_pk_bf16_f32 v58, v4, v5\n"
"v_mfma_f32_16x16x32_bf16 a[104:107], v[124:127], v[44:47], a[104:107]\n"
"v_exp_f32 v7, v7\n"
"v_mfma_f32_16x16x32_bf16 v[12:15], v[104:107], a[192:195], v[144:147]\n"
"v_exp_f32 v8, v8\n"
"v_mfma_f32_16x16x32_bf16 v[12:15], v[108:111], a[196:199], v[12:15]\n"
"v_exp_f32 v9, v9\n"
"v_mfma_f32_16x16x32_bf16 v[12:15], v[112:115], a[200:203], v[12:15]\n"
"v_cvt_pk_bf16_f32 v59, v6, v7\n"
"v_cvt_pk_bf16_f32 v66, v8, v9\n"
"v_mfma_f32_16x16x32_bf16 v[12:15], v[116:119], a[204:207], v[12:15]\n"
"buffer_store_dwordx4 v[224:227], v185, s[48:51], s72 offen offset:0\n"
"v_mfma_f32_16x16x32_bf16 a[136:139], v[120:123], v[48:51], 0\n"
"v_exp_f32 v10, v10\n"
"v_mfma_f32_16x16x32_bf16 a[136:139], v[124:127], v[52:55], a[136:139]\n"
"s_waitcnt lgkmcnt(1)\n"
"v_mfma_f32_16x16x32_bf16 a[12:15], v[128:131], v[16:19], 0\n"
"ds_read_b64_tr_b16 v[120:121], v181 offset:512\n"
"v_mfma_f32_16x16x32_bf16 a[12:15], v[132:135], v[20:23], a[12:15]\n"
"v_exp_f32 v11, v11\n"
"v_mfma_f32_16x16x32_bf16 v[0:3], v[104:107], a[208:211], v[148:151]\n"
"v_exp_f32 v12, v12\n"
"v_mfma_f32_16x16x32_bf16 v[0:3], v[108:111], a[212:215], v[0:3]\n"
"ds_read_b64_tr_b16 v[122:123], v181 offset:4608\n"
"v_cvt_pk_bf16_f32 v67, v10, v11\n"
"v_mfma_f32_16x16x32_bf16 v[0:3], v[112:115], a[216:219], v[0:3]\n"
"v_exp_f32 v13, v13\n"
"v_mfma_f32_16x16x32_bf16 v[0:3], v[116:119], a[220:223], v[0:3]\n"
"v_exp_f32 v14, v14\n"
"v_mfma_f32_16x16x32_bf16 a[44:47], v[128:131], v[24:27], 0\n"
"ds_read_b64_tr_b16 v[124:125], v181 offset:8704\n"
"v_cvt_pk_bf16_f32 v74, v12, v13\n"
"v_mfma_f32_16x16x32_bf16 a[44:47], v[132:135], v[28:31], a[44:47]\n"
"buffer_store_dwordx4 v[228:231], v185, s[48:51], s72 offen offset:64\n"
"v_mfma_f32_16x16x32_bf16 a[76:79], v[128:131], v[32:35], 0\n"
"v_exp_f32 v15, v15\n"
"v_mfma_f32_16x16x32_bf16 a[76:79], v[132:135], v[36:39], a[76:79]\n"
"ds_read_b64_tr_b16 v[126:127], v181 offset:12800\n"
"v_cvt_pk_bf16_f32 v75, v14, v15\n"
"v_mfma_f32_16x16x32_bf16 v[4:7], v[112:115], a[232:235], v[152:155]\n"
"ds_read_b128 v[112:115], v180 offset:25088\n"
"v_mfma_f32_16x16x32_bf16 v[4:7], v[116:119], a[236:239], v[4:7]\n"
"ds_read_b128 v[116:119], v180 offset:25344\n"
"v_mfma_f32_16x16x32_bf16 v[4:7], v[104:107], a[224:227], v[4:7]\n"
"v_exp_f32 v0, v0\n"
"v_mfma_f32_16x16x32_bf16 v[4:7], v[108:111], a[228:231], v[4:7]\n"
"v_exp_f32 v1, v1\n"
"v_mfma_f32_16x16x32_bf16 a[108:111], v[128:131], v[40:43], 0\n"
"buffer_load_dwordx4 v184, s[36:39], s52 offen offset:1024 lds\n"
"v_cvt_pk_bf16_f32 v82, v0, v1\n"
"v_mfma_f32_16x16x32_bf16 a[108:111], v[132:135], v[44:47], a[108:111]\n"
"v_exp_f32 v2, v2\n"
"v_mfma_f32_16x16x32_bf16 a[140:143], v[128:131], v[48:51], 0\n"
"v_exp_f32 v3, v3\n"
"v_mfma_f32_16x16x32_bf16 a[140:143], v[132:135], v[52:55], a[140:143]\n"
"v_cvt_pk_bf16_f32 v83, v2, v3\n"
"v_mfma_f32_16x16x32_bf16 v[160:163], v[176:179], v[24:27], 0\n"
"ds_read_b64_tr_b16 v[128:129], v182 offset:512\n"
"v_mfma_f32_16x16x32_bf16 v[160:163], v[176:179], v[28:31], v[160:163]\n"
"s_waitcnt lgkmcnt(7)\n"
"v_mfma_f32_16x16x32_bf16 v[8:11], v[96:99], a[160:163], v[136:139]\n"
"v_exp_f32 v4, v4\n"
"v_mfma_f32_16x16x32_bf16 v[8:11], v[100:103], a[164:167], v[8:11]\n"
"s_waitcnt lgkmcnt(1)\n"
"v_mfma_f32_16x16x32_bf16 v[8:11], v[112:115], a[168:171], v[8:11]\n"
"ds_read_b64_tr_b16 v[130:131], v182 offset:4608\n"
"v_mfma_f32_16x16x32_bf16 v[8:11], v[116:119], a[172:175], v[8:11]\n"
"buffer_store_dwordx4 v[232:235], v185, s[48:51], s72 offen offset:128\n"
"v_mfma_f32_16x16x32_bf16 a[16:19], v[120:123], v[16:19], 0\n"
"ds_read_b128 v[104:107], v180 offset:28672\n"
"v_mfma_f32_16x16x32_bf16 a[16:19], v[124:127], v[20:23], a[16:19]\n"
"ds_read_b64_tr_b16 v[132:133], v182 offset:8704\n"
"v_mfma_f32_16x16x32_bf16 a[48:51], v[120:123], v[24:27], 0\n"
"v_exp_f32 v5, v5\n"
"v_mfma_f32_16x16x32_bf16 a[48:51], v[124:127], v[28:31], a[48:51]\n"
"ds_read_b64_tr_b16 v[134:135], v182 offset:12800\n"
"v_cvt_pk_bf16_f32 v90, v4, v5\n"
"v_mfma_f32_16x16x32_bf16 v[12:15], v[96:99], a[176:179], v[140:143]\n"
"v_exp_f32 v6, v6\n"
"v_mfma_f32_16x16x32_bf16 v[12:15], v[100:103], a[180:183], v[12:15]\n"
"v_exp_f32 v7, v7\n"
"v_mfma_f32_16x16x32_bf16 v[12:15], v[112:115], a[184:187], v[12:15]\n"
"v_exp_f32 v8, v8\n"
"v_mfma_f32_16x16x32_bf16 v[12:15], v[116:119], a[188:191], v[12:15]\n"
"buffer_load_dwordx4 v183, s[36:39], s52 offen offset:2048 lds\n"
"v_cvt_pk_bf16_f32 v91, v6, v7\n"
"v_mfma_f32_16x16x32_bf16 a[80:83], v[120:123], v[32:35], 0\n"
"v_exp_f32 v9, v9\n"
"v_mfma_f32_16x16x32_bf16 a[80:83], v[124:127], v[36:39], a[80:83]\n"
"v_exp_f32 v10, v10\n"
"v_mfma_f32_16x16x32_bf16 a[112:115], v[120:123], v[40:43], 0\n"
"ds_read_b128 v[108:111], v180 offset:28928\n"
"v_cvt_pk_bf16_f32 v60, v8, v9\n"
"v_mfma_f32_16x16x32_bf16 a[112:115], v[124:127], v[44:47], a[112:115]\n"
"v_exp_f32 v11, v11\n"
"v_mfma_f32_16x16x32_bf16 v[0:3], v[96:99], a[192:195], v[144:147]\n"
"v_exp_f32 v12, v12\n"
"v_mfma_f32_16x16x32_bf16 v[0:3], v[100:103], a[196:199], v[0:3]\n"
"v_exp_f32 v13, v13\n"
"v_mfma_f32_16x16x32_bf16 v[0:3], v[112:115], a[200:203], v[0:3]\n"
"v_cvt_pk_bf16_f32 v61, v10, v11\n"
"v_cvt_pk_bf16_f32 v68, v12, v13\n"
"v_mfma_f32_16x16x32_bf16 v[0:3], v[116:119], a[204:207], v[0:3]\n"
"v_exp_f32 v14, v14\n"
"v_mfma_f32_16x16x32_bf16 a[144:147], v[120:123], v[48:51], 0\n"
"buffer_store_dwordx4 v[236:239], v185, s[48:51], s72 offen offset:192\n"
"v_mfma_f32_16x16x32_bf16 a[144:147], v[124:127], v[52:55], a[144:147]\n"
"s_waitcnt lgkmcnt(1)\n"
"v_mfma_f32_16x16x32_bf16 a[20:23], v[128:131], v[16:19], 0\n"
"ds_read_b64_tr_b16 v[120:121], v181 offset:768\n"
"v_mfma_f32_16x16x32_bf16 a[20:23], v[132:135], v[20:23], a[20:23]\n"
"v_exp_f32 v15, v15\n"
"v_mfma_f32_16x16x32_bf16 v[172:175], v[176:179], v[48:51], 0\n"
"v_exp_f32 v0, v0\n"
"v_mfma_f32_16x16x32_bf16 v[172:175], v[176:179], v[52:55], v[172:175]\n"
"ds_read_b64_tr_b16 v[122:123], v181 offset:4864\n"
"v_cvt_pk_bf16_f32 v69, v14, v15\n"
"v_mfma_f32_16x16x32_bf16 v[4:7], v[96:99], a[208:211], v[148:151]\n"
"v_exp_f32 v1, v1\n"
"v_mfma_f32_16x16x32_bf16 v[4:7], v[100:103], a[212:215], v[4:7]\n"
"v_exp_f32 v2, v2\n"
"v_mfma_f32_16x16x32_bf16 v[4:7], v[112:115], a[216:219], v[4:7]\n"
"ds_read_b64_tr_b16 v[124:125], v181 offset:8960\n"
"v_cvt_pk_bf16_f32 v76, v0, v1\n"
"v_mfma_f32_16x16x32_bf16 v[4:7], v[116:119], a[220:223], v[4:7]\n"
"buffer_load_dwordx4 v184, s[36:39], s52 offen offset:3072 lds\n"
"v_mfma_f32_16x16x32_bf16 a[52:55], v[128:131], v[24:27], 0\n"
"v_exp_f32 v3, v3\n"
"v_mfma_f32_16x16x32_bf16 a[52:55], v[132:135], v[28:31], a[52:55]\n"
"ds_read_b64_tr_b16 v[126:127], v181 offset:13056\n"
"v_cvt_pk_bf16_f32 v77, v2, v3\n"
"v_mfma_f32_16x16x32_bf16 a[84:87], v[128:131], v[32:35], 0\n"
"v_mfma_f32_16x16x32_bf16 a[84:87], v[132:135], v[36:39], a[84:87]\n"
"v_mfma_f32_16x16x32_bf16 v[8:11], v[112:115], a[232:235], v[152:155]\n"
"ds_read_b128 v[112:115], v180 offset:29184\n"
"v_mfma_f32_16x16x32_bf16 v[8:11], v[116:119], a[236:239], v[8:11]\n"
"ds_read_b128 v[116:119], v180 offset:29440\n"
"v_mfma_f32_16x16x32_bf16 v[8:11], v[96:99], a[224:227], v[8:11]\n"
"v_exp_f32 v4, v4\n"
"v_mfma_f32_16x16x32_bf16 v[8:11], v[100:103], a[228:231], v[8:11]\n"
"buffer_store_dwordx4 v[240:243], v185, s[48:51], s73 offen offset:0\n"
"v_mfma_f32_16x16x32_bf16 a[116:119], v[128:131], v[40:43], 0\n"
"v_exp_f32 v5, v5\n"
"v_mfma_f32_16x16x32_bf16 a[116:119], v[132:135], v[44:47], a[116:119]\n"
"v_exp_f32 v6, v6\n"
"v_mfma_f32_16x16x32_bf16 a[148:151], v[128:131], v[48:51], 0\n"
"v_exp_f32 v7, v7\n"
"v_mfma_f32_16x16x32_bf16 a[148:151], v[132:135], v[52:55], a[148:151]\n"
"v_cvt_pk_bf16_f32 v84, v4, v5\n"
"v_cvt_pk_bf16_f32 v85, v6, v7\n"
"v_mfma_f32_16x16x32_bf16 v[164:167], v[176:179], v[32:35], 0\n"
"ds_read_b64_tr_b16 v[128:129], v182 offset:768\n"
"v_mfma_f32_16x16x32_bf16 v[164:167], v[176:179], v[36:39], v[164:167]\n"
"s_waitcnt lgkmcnt(7)\n"
"v_mfma_f32_16x16x32_bf16 v[12:15], v[104:107], a[160:163], v[136:139]\n"
"v_exp_f32 v8, v8\n"
"v_mfma_f32_16x16x32_bf16 v[12:15], v[108:111], a[164:167], v[12:15]\n"
"s_waitcnt lgkmcnt(1)\n"
"v_mfma_f32_16x16x32_bf16 v[12:15], v[112:115], a[168:171], v[12:15]\n"
"ds_read_b64_tr_b16 v[130:131], v182 offset:4864\n"
"v_mfma_f32_16x16x32_bf16 v[12:15], v[116:119], a[172:175], v[12:15]\n"
"s_add_u32 s52, s52, 0x4000\n"
"v_mfma_f32_16x16x32_bf16 a[24:27], v[120:123], v[16:19], 0\n"
"ds_read_b64_tr_b16 v[132:133], v182 offset:8960\n"
"v_mfma_f32_16x16x32_bf16 a[24:27], v[124:127], v[20:23], a[24:27]\n"
"v_exp_f32 v9, v9\n"
"v_mfma_f32_16x16x32_bf16 a[56:59], v[120:123], v[24:27], 0\n"
"v_exp_f32 v10, v10\n"
"v_mfma_f32_16x16x32_bf16 a[56:59], v[124:127], v[28:31], a[56:59]\n"
"ds_read_b64_tr_b16 v[134:135], v182 offset:13056\n"
"v_cvt_pk_bf16_f32 v92, v8, v9\n"
"v_mfma_f32_16x16x32_bf16 v[0:3], v[104:107], a[176:179], v[140:143]\n"
"v_exp_f32 v11, v11\n"
"v_mfma_f32_16x16x32_bf16 v[0:3], v[108:111], a[180:183], v[0:3]\n"
"v_exp_f32 v12, v12\n"
"v_mfma_f32_16x16x32_bf16 v[0:3], v[112:115], a[184:187], v[0:3]\n"
"buffer_store_dwordx4 v[244:247], v185, s[48:51], s73 offen offset:64\n"
"v_cvt_pk_bf16_f32 v93, v10, v11\n"
"v_mfma_f32_16x16x32_bf16 v[0:3], v[116:119], a[188:191], v[0:3]\n"
"v_exp_f32 v13, v13\n"
"v_mfma_f32_16x16x32_bf16 a[88:91], v[120:123], v[32:35], 0\n"
"v_exp_f32 v14, v14\n"
"v_mfma_f32_16x16x32_bf16 a[88:91], v[124:127], v[36:39], a[88:91]\n"
"v_exp_f32 v15, v15\n"
"v_mfma_f32_16x16x32_bf16 a[120:123], v[120:123], v[40:43], 0\n"
"v_cvt_pk_bf16_f32 v62, v12, v13\n"
"v_cvt_pk_bf16_f32 v63, v14, v15\n"
"v_mfma_f32_16x16x32_bf16 a[120:123], v[124:127], v[44:47], a[120:123]\n"
"v_mfma_f32_16x16x32_bf16 v[4:7], v[104:107], a[192:195], v[144:147]\n"
"v_exp_f32 v0, v0\n"
"v_mfma_f32_16x16x32_bf16 v[4:7], v[108:111], a[196:199], v[4:7]\n"
"v_exp_f32 v1, v1\n"
"v_mfma_f32_16x16x32_bf16 v[4:7], v[112:115], a[200:203], v[4:7]\n"
"v_exp_f32 v2, v2\n"
"v_mfma_f32_16x16x32_bf16 v[4:7], v[116:119], a[204:207], v[4:7]\n"
"v_exp_f32 v3, v3\n"
"v_mfma_f32_16x16x32_bf16 a[152:155], v[120:123], v[48:51], 0\n"
"v_cvt_pk_bf16_f32 v70, v0, v1\n"
"v_cvt_pk_bf16_f32 v71, v2, v3\n"
"v_mfma_f32_16x16x32_bf16 a[152:155], v[124:127], v[52:55], a[152:155]\n"
"s_waitcnt vmcnt(34) lgkmcnt(0)\n"
"v_xor_b32 v180, 0x8000, v180\n"
"s_barrier\n"
"v_mfma_f32_16x16x32_bf16 a[28:31], v[128:131], v[16:19], 0\n"
"ds_read_b128 v[96:99], v180 offset:0\n"
"v_mfma_f32_16x16x32_bf16 a[28:31], v[132:135], v[20:23], a[28:31]\n"
"ds_read_b128 v[100:103], v180 offset:256\n"
"v_mfma_f32_16x16x32_bf16 v[8:11], v[104:107], a[208:211], v[148:151]\n"
"ds_read_b64_tr_b16 v[120:121], v181 offset:16384\n"
"v_mfma_f32_16x16x32_bf16 v[8:11], v[108:111], a[212:215], v[8:11]\n"
"ds_read_b64_tr_b16 v[122:123], v181 offset:20480\n"
"v_mfma_f32_16x16x32_bf16 v[8:11], v[112:115], a[216:219], v[8:11]\n"
"ds_read_b64_tr_b16 v[124:125], v181 offset:24576\n"
"v_mfma_f32_16x16x32_bf16 v[8:11], v[116:119], a[220:223], v[8:11]\n"
"ds_read_b64_tr_b16 v[126:127], v181 offset:28672\n"
"v_mfma_f32_16x16x32_bf16 a[60:63], v[128:131], v[24:27], 0\n"
"s_add_u32 m0, s54, 0x0\n"
"v_mfma_f32_16x16x32_bf16 a[60:63], v[132:135], v[28:31], a[60:63]\n"
"buffer_load_dwordx4 v183, s[40:43], s52 offen offset:0 lds\n"
"v_mfma_f32_16x16x32_bf16 a[92:95], v[128:131], v[32:35], 0\n"
"v_exp_f32 v4, v4\n"
"v_mfma_f32_16x16x32_bf16 a[92:95], v[132:135], v[36:39], a[92:95]\n"
"buffer_store_dwordx4 v[248:251], v185, s[48:51], s73 offen offset:128\n"
"v_mfma_f32_16x16x32_bf16 v[12:15], v[112:115], a[232:235], v[152:155]\n"
"ds_read_b128 v[112:115], v180 offset:512\n"
"v_mfma_f32_16x16x32_bf16 v[12:15], v[116:119], a[236:239], v[12:15]\n"
"ds_read_b128 v[116:119], v180 offset:768\n"
"v_mfma_f32_16x16x32_bf16 v[12:15], v[104:107], a[224:227], v[12:15]\n"
"v_exp_f32 v5, v5\n"
"v_mfma_f32_16x16x32_bf16 v[12:15], v[108:111], a[228:231], v[12:15]\n"
"buffer_store_dwordx4 v[252:255], v185, s[48:51], s73 offen offset:192\n"
"v_cvt_pk_bf16_f32 v78, v4, v5\n"
"v_mfma_f32_16x16x32_bf16 a[124:127], v[128:131], v[40:43], 0\n"
"v_exp_f32 v6, v6\n"
"v_mfma_f32_16x16x32_bf16 a[124:127], v[132:135], v[44:47], a[124:127]\n"
"v_exp_f32 v7, v7\n"
"v_mfma_f32_16x16x32_bf16 a[156:159], v[128:131], v[48:51], 0\n"
"v_exp_f32 v8, v8\n"
"v_mfma_f32_16x16x32_bf16 a[156:159], v[132:135], v[52:55], a[156:159]\n"
"v_exp_f32 v9, v9\n"
"v_mfma_f32_16x16x32_bf16 v[168:171], v[176:179], v[40:43], 0\n"
"v_cvt_pk_bf16_f32 v79, v6, v7\n"
"v_cvt_pk_bf16_f32 v86, v8, v9\n"
"v_mfma_f32_16x16x32_bf16 v[168:171], v[176:179], v[44:47], v[168:171]\n"
"v_exp_f32 v10, v10\n"
"v_exp_f32 v11, v11\n"
"s_nop 0\n"
"v_cvt_pk_bf16_f32 v87, v10, v11\n"
"s_waitcnt lgkmcnt(2)\n"
"s_mov_b32 s55, 23\n"
"s_mov_b32 s59, s53\n"
"s_xor_b32 s58, s53, 0x8000\n"
"s_add_u32 s48, s48, 155648\n"
"s_addc_u32 s49, s49, 0\n"
"s_branch L_loop_a_%=\n"
"L_final_a_%=:\n"
"s_waitcnt lgkmcnt(0)\n"
"ds_read_b64_tr_b16 v[16:17], v182 offset:16384\n"
"ds_read_b64_tr_b16 v[18:19], v182 offset:20480\n"
"ds_read_b64_tr_b16 v[20:21], v182 offset:24576\n"
"ds_read_b64_tr_b16 v[22:23], v182 offset:28672\n"
"s_nop 3\n"
"v_exp_f32 v12, v12\n"
"v_exp_f32 v13, v13\n"
"v_exp_f32 v14, v14\n"
"v_exp_f32 v15, v15\n"
"s_nop 0\n"
"v_cvt_pk_bf16_f32 v94, v12, v13\n"
"v_cvt_pk_bf16_f32 v95, v14, v15\n"
"s_nop 1\n"
"ds_read_b64_tr_b16 v[24:25], v181 offset:16640\n"
"ds_read_b64_tr_b16 v[26:27], v181 offset:20736\n"
"ds_read_b64_tr_b16 v[28:29], v181 offset:24832\n"
"ds_read_b64_tr_b16 v[30:31], v181 offset:28928\n"
"ds_read_b64_tr_b16 v[32:33], v182 offset:16640\n"
"ds_read_b64_tr_b16 v[34:35], v182 offset:20736\n"
"ds_read_b64_tr_b16 v[36:37], v182 offset:24832\n"
"ds_read_b64_tr_b16 v[38:39], v182 offset:28928\n"
"ds_read_b64_tr_b16 v[40:41], v181 offset:16896\n"
"ds_read_b64_tr_b16 v[42:43], v181 offset:20992\n"
"ds_read_b64_tr_b16 v[44:45], v181 offset:25088\n"
"ds_read_b64_tr_b16 v[46:47], v181 offset:29184\n"
"ds_read_b64_tr_b16 v[48:49], v182 offset:16896\n"
"ds_read_b64_tr_b16 v[50:51], v182 offset:20992\n"
"ds_read_b64_tr_b16 v[52:53], v182 offset:25088\n"
"ds_read_b64_tr_b16 v[54:55], v182 offset:29184\n"
"ds_read_b64_tr_b16 v[96:97], v181 offset:17152\n"
"ds_read_b64_tr_b16 v[98:99], v181 offset:21248\n"
"ds_read_b64_tr_b16 v[100:101], v181 offset:25344\n"
"ds_read_b64_tr_b16 v[102:103], v181 offset:29440\n"
"ds_read_b64_tr_b16 v[104:105], v182 offset:17152\n"
"ds_read_b64_tr_b16 v[106:107], v182 offset:21248\n"
"ds_read_b64_tr_b16 v[108:109], v182 offset:25344\n"
"ds_read_b64_tr_b16 v[110:111], v182 offset:29440\n"
"v_mbcnt_lo_u32_b32 v0, -1, 0\n"
"v_mbcnt_hi_u32_b32 v0, -1, v0\n"
"v_and_b32 v1, 15, v0\n"
"v_lshrrev_b32 v2, 4, v0\n"
"v_and_b32 v3, 1, v2\n"
"v_lshrrev_b32 v4, 1, v2\n"
"v_lshl_add_u32 v3, v3, 1, v4\n"
"v_mov_b32 v7, 1.0\n"
"s_lshl_b32 s62, s64, 13\n"
"s_add_u32 s62, s62, 0x20000\n"
"v_add_u32 v8, v1, v3\n"
"v_and_b32 v8, 3, v8\n"
"v_lshlrev_b32 v8, 4, v8\n"
"v_lshl_add_u32 v8, v1, 8, v8\n"
"v_bfe_u32 v9, v1, 2, 1\n"
"v_lshl_add_u32 v186, v9, 6, v8\n"
"v_add_u32 v186, s62, v186\n"
"v_xor_b32 v187, 64, v186\n"
"v_lshlrev_b32 v188, 4, v0\n"
"v_add_u32 v188, s62, v188\n"
"v_sub_u32 v8, v1, v2\n"
"v_and_b32 v8, 3, v8\n"
"v_and_b32 v9, 12, v1\n"
"v_or_b32 v8, v8, v9\n"
"v_lshlrev_b32 v8, 4, v8\n"
"v_lshl_add_u32 v190, v2, 8, v8\n"
"v_xor_b32 v191, 64, v190\n"
"s_waitcnt lgkmcnt(0)\n"
"v_mfma_f32_16x16x32_bf16 v[156:159], v[176:179], v[56:59], v[156:159]\n"
"v_mfma_f32_16x16x32_bf16 v[156:159], v[176:179], v[60:63], v[156:159]\n"
"v_mfma_f32_16x16x32_bf16 a[0:3], v[120:123], v[56:59], a[0:3]\n"
"v_mfma_f32_16x16x32_bf16 a[0:3], v[124:127], v[60:63], a[0:3]\n"
"v_mfma_f32_16x16x32_bf16 a[4:7], v[16:19], v[56:59], a[4:7]\n"
"v_mfma_f32_16x16x32_bf16 a[4:7], v[20:23], v[60:63], a[4:7]\n"
"v_mfma_f32_16x16x32_bf16 a[8:11], v[24:27], v[56:59], a[8:11]\n"
"v_mfma_f32_16x16x32_bf16 a[8:11], v[28:31], v[60:63], a[8:11]\n"
"v_mfma_f32_16x16x32_bf16 a[12:15], v[32:35], v[56:59], a[12:15]\n"
"v_mfma_f32_16x16x32_bf16 a[12:15], v[36:39], v[60:63], a[12:15]\n"
"v_mfma_f32_16x16x4_f32 v[136:139], v7, v156, 0\n"
"v_cmp_ngt_f32 vcc, s65, v156\n"
"s_or_b64 s[60:61], s[60:61], vcc\n"
"v_mfma_f32_16x16x32_bf16 a[16:19], v[40:43], v[56:59], a[16:19]\n"
"v_mfma_f32_16x16x32_bf16 a[16:19], v[44:47], v[60:63], a[16:19]\n"
"v_mfma_f32_16x16x32_bf16 a[20:23], v[48:51], v[56:59], a[20:23]\n"
"v_mfma_f32_16x16x32_bf16 a[20:23], v[52:55], v[60:63], a[20:23]\n"
"v_mfma_f32_16x16x32_bf16 a[24:27], v[96:99], v[56:59], a[24:27]\n"
"v_mfma_f32_16x16x32_bf16 a[24:27], v[100:103], v[60:63], a[24:27]\n"
"v_mfma_f32_16x16x32_bf16 a[28:31], v[104:107], v[56:59], a[28:31]\n"
"v_mfma_f32_16x16x32_bf16 a[28:31], v[108:111], v[60:63], a[28:31]\n"
"s_nop 3\n"
"v_rcp_f32 v136, v136\n"
"v_accvgpr_read_b32 v192, a0\n"
"v_accvgpr_read_b32 v193, a1\n"
"v_accvgpr_read_b32 v194, a2\n"
"v_accvgpr_read_b32 v195, a3\n"
"v_accvgpr_read_b32 v196, a4\n"
"v_accvgpr_read_b32 v197, a5\n"
"v_accvgpr_read_b32 v198, a6\n"
"v_accvgpr_read_b32 v199, a7\n"
"v_pk_mul_f32 v[192:193], v[192:193], v[136:137] op_sel_hi:[1,0]\n"
"v_pk_mul_f32 v[194:195], v[194:195], v[136:137] op_sel_hi:[1,0]\n"
"v_pk_mul_f32 v[196:197], v[196:197], v[136:137] op_sel_hi:[1,0]\n"
"v_pk_mul_f32 v[198:199], v[198:199], v[136:137] op_sel_hi:[1,0]\n"
"v_cvt_pk_bf16_f32 v208, v192, v193\n"
"v_cvt_pk_bf16_f32 v209, v194, v195\n"
"v_cvt_pk_bf16_f32 v210, v196, v197\n"
"v_cvt_pk_bf16_f32 v211, v198, v199\n"
"v_accvgpr_read_b32 v200, a8\n"
"v_accvgpr_read_b32 v201, a9\n"
"v_accvgpr_read_b32 v202, a10\n"
"v_permlane16_swap_b32 v208, v210\n"
"v_permlane16_swap_b32 v209, v211\n"
"v_accvgpr_read_b32 v203, a11\n"
"v_accvgpr_read_b32 v204, a12\n"
"v_accvgpr_read_b32 v205, a13\n"
"v_accvgpr_read_b32 v206, a14\n"
"v_accvgpr_read_b32 v207, a15\n"
"v_pk_mul_f32 v[200:201], v[200:201], v[136:137] op_sel_hi:[1,0]\n"
"v_pk_mul_f32 v[202:203], v[202:203], v[136:137] op_sel_hi:[1,0]\n"
"v_pk_mul_f32 v[204:205], v[204:205], v[136:137] op_sel_hi:[1,0]\n"
"v_pk_mul_f32 v[206:207], v[206:207], v[136:137] op_sel_hi:[1,0]\n"
"v_cvt_pk_bf16_f32 v212, v200, v201\n"
"v_cvt_pk_bf16_f32 v213, v202, v203\n"
"v_cvt_pk_bf16_f32 v214, v204, v205\n"
"v_cvt_pk_bf16_f32 v215, v206, v207\n"
"ds_write_b128 v186, v[208:211] offset:0\n"
"v_accvgpr_read_b32 v192, a16\n"
"v_accvgpr_read_b32 v193, a17\n"
"v_accvgpr_read_b32 v194, a18\n"
"v_permlane16_swap_b32 v212, v214\n"
"v_permlane16_swap_b32 v213, v215\n"
"v_accvgpr_read_b32 v195, a19\n"
"v_accvgpr_read_b32 v196, a20\n"
"v_accvgpr_read_b32 v197, a21\n"
"v_accvgpr_read_b32 v198, a22\n"
"v_accvgpr_read_b32 v199, a23\n"
"v_pk_mul_f32 v[192:193], v[192:193], v[136:137] op_sel_hi:[1,0]\n"
"v_pk_mul_f32 v[194:195], v[194:195], v[136:137] op_sel_hi:[1,0]\n"
"v_pk_mul_f32 v[196:197], v[196:197], v[136:137] op_sel_hi:[1,0]\n"
"v_pk_mul_f32 v[198:199], v[198:199], v[136:137] op_sel_hi:[1,0]\n"
"v_cvt_pk_bf16_f32 v216, v192, v193\n"
"v_cvt_pk_bf16_f32 v217, v194, v195\n"
"v_cvt_pk_bf16_f32 v218, v196, v197\n"
"v_cvt_pk_bf16_f32 v219, v198, v199\n"
"ds_write_b128 v187, v[212:215] offset:0\n"
"v_accvgpr_read_b32 v200, a24\n"
"v_accvgpr_read_b32 v201, a25\n"
"v_accvgpr_read_b32 v202, a26\n"
"v_permlane16_swap_b32 v216, v218\n"
"v_permlane16_swap_b32 v217, v219\n"
"v_accvgpr_read_b32 v203, a27\n"
"v_accvgpr_read_b32 v204, a28\n"
"v_accvgpr_read_b32 v205, a29\n"
"v_accvgpr_read_b32 v206, a30\n"
"v_accvgpr_read_b32 v207, a31\n"
"v_pk_mul_f32 v[200:201], v[200:201], v[136:137] op_sel_hi:[1,0]\n"
"v_pk_mul_f32 v[202:203], v[202:203], v[136:137] op_sel_hi:[1,0]\n"
"v_pk_mul_f32 v[204:205], v[204:205], v[136:137] op_sel_hi:[1,0]\n"
"v_pk_mul_f32 v[206:207], v[206:207], v[136:137] op_sel_hi:[1,0]\n"
"v_cvt_pk_bf16_f32 v220, v200, v201\n"
"v_cvt_pk_bf16_f32 v221, v202, v203\n"
"v_cvt_pk_bf16_f32 v222, v204, v205\n"
"v_cvt_pk_bf16_f32 v223, v206, v207\n"
"ds_write_b128 v186, v[216:219] offset:128\n"
"s_nop 1\n"
"v_permlane16_swap_b32 v220, v222\n"
"v_permlane16_swap_b32 v221, v223\n"
"ds_write_b128 v187, v[220:223] offset:128\n"
"ds_read_b128 v[240:243], v188 offset:0\n"
"ds_read_b128 v[244:247], v188 offset:1024\n"
"ds_read_b128 v[248:251], v188 offset:2048\n"
"ds_read_b128 v[252:255], v188 offset:3072\n"
"v_mfma_f32_16x16x32_bf16 v[160:163], v[176:179], v[64:67], v[160:163]\n"
"v_mfma_f32_16x16x32_bf16 v[160:163], v[176:179], v[68:71], v[160:163]\n"
"v_mfma_f32_16x16x32_bf16 a[32:35], v[120:123], v[64:67], a[32:35]\n"
"v_mfma_f32_16x16x32_bf16 a[32:35], v[124:127], v[68:71], a[32:35]\n"
"v_mfma_f32_16x16x32_bf16 a[36:39], v[16:19], v[64:67], a[36:39]\n"
"v_mfma_f32_16x16x32_bf16 a[36:39], v[20:23], v[68:71], a[36:39]\n"
"v_mfma_f32_16x16x32_bf16 a[40:43], v[24:27], v[64:67], a[40:43]\n"
"v_mfma_f32_16x16x32_bf16 a[40:43], v[28:31], v[68:71], a[40:43]\n"
"v_mfma_f32_16x16x32_bf16 a[44:47], v[32:35], v[64:67], a[44:47]\n"
"v_mfma_f32_16x16x32_bf16 a[44:47], v[36:39], v[68:71], a[44:47]\n"
"v_mfma_f32_16x16x4_f32 v[140:143], v7, v160, 0\n"
"v_cmp_ngt_f32 vcc, s65, v160\n"
"s_or_b64 s[60:61], s[60:61], vcc\n"
"v_mfma_f32_16x16x32_bf16 a[48:51], v[40:43], v[64:67], a[48:51]\n"
"v_mfma_f32_16x16x32_bf16 a[48:51], v[44:47], v[68:71], a[48:51]\n"
"v_mfma_f32_16x16x32_bf16 a[52:55], v[48:51], v[64:67], a[52:55]\n"
"v_mfma_f32_16x16x32_bf16 a[52:55], v[52:55], v[68:71], a[52:55]\n"
"v_mfma_f32_16x16x32_bf16 a[56:59], v[96:99], v[64:67], a[56:59]\n"
"v_mfma_f32_16x16x32_bf16 a[56:59], v[100:103], v[68:71], a[56:59]\n"
"v_mfma_f32_16x16x32_bf16 a[60:63], v[104:107], v[64:67], a[60:63]\n"
"v_mfma_f32_16x16x32_bf16 a[60:63], v[108:111], v[68:71], a[60:63]\n"
"s_nop 3\n"
"v_rcp_f32 v140, v140\n"
"v_accvgpr_read_b32 v192, a32\n"
"v_accvgpr_read_b32 v193, a33\n"
"v_accvgpr_read_b32 v194, a34\n"
"v_accvgpr_read_b32 v195, a35\n"
"v_accvgpr_read_b32 v196, a36\n"
"v_accvgpr_read_b32 v197, a37\n"
"v_accvgpr_read_b32 v198, a38\n"
"v_accvgpr_read_b32 v199, a39\n"
"v_pk_mul_f32 v[192:193], v[192:193], v[140:141] op_sel_hi:[1,0]\n"
"v_pk_mul_f32 v[194:195], v[194:195], v[140:141] op_sel_hi:[1,0]\n"
"v_pk_mul_f32 v[196:197], v[196:197], v[140:141] op_sel_hi:[1,0]\n"
"v_pk_mul_f32 v[198:199], v[198:199], v[140:141] op_sel_hi:[1,0]\n"
"v_cvt_pk_bf16_f32 v224, v192, v193\n"
"v_cvt_pk_bf16_f32 v225, v194, v195\n"
"v_cvt_pk_bf16_f32 v226, v196, v197\n"
"v_cvt_pk_bf16_f32 v227, v198, v199\n"
"v_accvgpr_read_b32 v200, a40\n"
"v_accvgpr_read_b32 v201, a41\n"
"v_accvgpr_read_b32 v202, a42\n"
"v_permlane16_swap_b32 v224, v226\n"
"v_permlane16_swap_b32 v225, v227\n"
"v_accvgpr_read_b32 v203, a43\n"
"v_accvgpr_read_b32 v204, a44\n"
"v_accvgpr_read_b32 v205, a45\n"
"v_accvgpr_read_b32 v206, a46\n"
"v_accvgpr_read_b32 v207, a47\n"
"v_pk_mul_f32 v[200:201], v[200:201], v[140:141] op_sel_hi:[1,0]\n"
"v_pk_mul_f32 v[202:203], v[202:203], v[140:141] op_sel_hi:[1,0]\n"
"v_pk_mul_f32 v[204:205], v[204:205], v[140:141] op_sel_hi:[1,0]\n"
"v_pk_mul_f32 v[206:207], v[206:207], v[140:141] op_sel_hi:[1,0]\n"
"v_cvt_pk_bf16_f32 v228, v200, v201\n"
"v_cvt_pk_bf16_f32 v229, v202, v203\n"
"v_cvt_pk_bf16_f32 v230, v204, v205\n"
"v_cvt_pk_bf16_f32 v231, v206, v207\n"
"ds_write_b128 v186, v[224:227] offset:4096\n"
"s_waitcnt lgkmcnt(1)\n"
"buffer_store_dwordx4 v[240:243], v190, s[48:51], s70 offen offset:0 sc1\n"
"v_accvgpr_read_b32 v192, a48\n"
"v_accvgpr_read_b32 v193, a49\n"
"v_accvgpr_read_b32 v194, a50\n"
"v_permlane16_swap_b32 v228, v230\n"
"v_permlane16_swap_b32 v229, v231\n"
"v_accvgpr_read_b32 v195, a51\n"
"v_accvgpr_read_b32 v196, a52\n"
"v_accvgpr_read_b32 v197, a53\n"
"v_accvgpr_read_b32 v198, a54\n"
"v_accvgpr_read_b32 v199, a55\n"
"v_pk_mul_f32 v[192:193], v[192:193], v[140:141] op_sel_hi:[1,0]\n"
"v_pk_mul_f32 v[194:195], v[194:195], v[140:141] op_sel_hi:[1,0]\n"
"v_pk_mul_f32 v[196:197], v[196:197], v[140:141] op_sel_hi:[1,0]\n"
"v_pk_mul_f32 v[198:199], v[198:199], v[140:141] op_sel_hi:[1,0]\n"
"v_cvt_pk_bf16_f32 v232, v192, v193\n"
"v_cvt_pk_bf16_f32 v233, v194, v195\n"
"v_cvt_pk_bf16_f32 v234, v196, v197\n"
"v_cvt_pk_bf16_f32 v235, v198, v199\n"
"ds_write_b128 v187, v[228:231] offset:4096\n"
"buffer_store_dwordx4 v[244:247], v191, s[48:51], s70 offen offset:1024 sc1\n"
"v_accvgpr_read_b32 v200, a56\n"
"v_accvgpr_read_b32 v201, a57\n"
"v_accvgpr_read_b32 v202, a58\n"
"v_permlane16_swap_b32 v232, v234\n"
"v_permlane16_swap_b32 v233, v235\n"
"v_accvgpr_read_b32 v203, a59\n"
"v_accvgpr_read_b32 v204, a60\n"
"v_accvgpr_read_b32 v205, a61\n"
"v_accvgpr_read_b32 v206, a62\n"
"v_accvgpr_read_b32 v207, a63\n"
"v_pk_mul_f32 v[200:201], v[200:201], v[140:141] op_sel_hi:[1,0]\n"
"v_pk_mul_f32 v[202:203], v[202:203], v[140:141] op_sel_hi:[1,0]\n"
"v_pk_mul_f32 v[204:205], v[204:205], v[140:141] op_sel_hi:[1,0]\n"
"v_pk_mul_f32 v[206:207], v[206:207], v[140:141] op_sel_hi:[1,0]\n"
"v_cvt_pk_bf16_f32 v236, v200, v201\n"
"v_cvt_pk_bf16_f32 v237, v202, v203\n"
"v_cvt_pk_bf16_f32 v238, v204, v205\n"
"v_cvt_pk_bf16_f32 v239, v206, v207\n"
"ds_write_b128 v186, v[232:235] offset:4224\n"
"buffer_store_dwordx4 v[248:251], v190, s[48:51], s70 offen offset:2048 sc1\n"
"s_nop 1\n"
"v_permlane16_swap_b32 v236, v238\n"
"v_permlane16_swap_b32 v237, v239\n"
"ds_write_b128 v187, v[236:239] offset:4224\n"
"buffer_store_dwordx4 v[252:255], v191, s[48:51], s70 offen offset:3072 sc1\n"
"ds_read_b128 a[240:243], v188 offset:4096\n"
"ds_read_b128 a[244:247], v188 offset:5120\n"
"ds_read_b128 a[248:251], v188 offset:6144\n"
"ds_read_b128 a[252:255], v188 offset:7168\n"
"v_mfma_f32_16x16x32_bf16 v[164:167], v[176:179], v[72:75], v[164:167]\n"
"v_mfma_f32_16x16x32_bf16 v[164:167], v[176:179], v[76:79], v[164:167]\n"
"v_mfma_f32_16x16x32_bf16 a[64:67], v[120:123], v[72:75], a[64:67]\n"
"v_mfma_f32_16x16x32_bf16 a[64:67], v[124:127], v[76:79], a[64:67]\n"
"v_mfma_f32_16x16x32_bf16 a[68:71], v[16:19], v[72:75], a[68:71]\n"
"v_mfma_f32_16x16x32_bf16 a[68:71], v[20:23], v[76:79], a[68:71]\n"
"v_mfma_f32_16x16x32_bf16 a[72:75], v[24:27], v[72:75], a[72:75]\n"
"v_mfma_f32_16x16x32_bf16 a[72:75], v[28:31], v[76:79], a[72:75]\n"
"v_mfma_f32_16x16x32_bf16 a[76:79], v[32:35], v[72:75], a[76:79]\n"
"v_mfma_f32_16x16x32_bf16 a[76:79], v[36:39], v[76:79], a[76:79]\n"
"v_mfma_f32_16x16x4_f32 v[144:147], v7, v164, 0\n"
"v_cmp_ngt_f32 vcc, s65, v164\n"
"s_or_b64 s[60:61], s[60:61], vcc\n"
"v_mfma_f32_16x16x32_bf16 a[80:83], v[40:43], v[72:75], a[80:83]\n"
"v_mfma_f32_16x16x32_bf16 a[80:83], v[44:47], v[76:79], a[80:83]\n"
"v_mfma_f32_16x16x32_bf16 a[84:87], v[48:51], v[72:75], a[84:87]\n"
"v_mfma_f32_16x16x32_bf16 a[84:87], v[52:55], v[76:79], a[84:87]\n"
"v_mfma_f32_16x16x32_bf16 a[88:91], v[96:99], v[72:75], a[88:91]\n"
"v_mfma_f32_16x16x32_bf16 a[88:91], v[100:103], v[76:79], a[88:91]\n"
"v_mfma_f32_16x16x32_bf16 a[92:95], v[104:107], v[72:75], a[92:95]\n"
"v_mfma_f32_16x16x32_bf16 a[92:95], v[108:111], v[76:79], a[92:95]\n"
"s_nop 3\n"
"v_rcp_f32 v144, v144\n"
"v_accvgpr_read_b32 v192, a64\n"
"v_accvgpr_read_b32 v193, a65\n"
"v_accvgpr_read_b32 v194, a66\n"
"v_accvgpr_read_b32 v195, a67\n"
"v_accvgpr_read_b32 v196, a68\n"
"v_accvgpr_read_b32 v197, a69\n"
"v_accvgpr_read_b32 v198, a70\n"
"v_accvgpr_read_b32 v199, a71\n"
"v_pk_mul_f32 v[192:193], v[192:193], v[144:145] op_sel_hi:[1,0]\n"
"v_pk_mul_f32 v[194:195], v[194:195], v[144:145] op_sel_hi:[1,0]\n"
"v_pk_mul_f32 v[196:197], v[196:197], v[144:145] op_sel_hi:[1,0]\n"
"v_pk_mul_f32 v[198:199], v[198:199], v[144:145] op_sel_hi:[1,0]\n"
"v_cvt_pk_bf16_f32 v208, v192, v193\n"
"v_cvt_pk_bf16_f32 v209, v194, v195\n"
"v_cvt_pk_bf16_f32 v210, v196, v197\n"
"v_cvt_pk_bf16_f32 v211, v198, v199\n"
"v_accvgpr_read_b32 v200, a72\n"
"v_accvgpr_read_b32 v201, a73\n"
"v_accvgpr_read_b32 v202, a74\n"
"v_permlane16_swap_b32 v208, v210\n"
"v_permlane16_swap_b32 v209, v211\n"
"v_accvgpr_read_b32 v203, a75\n"
"v_accvgpr_read_b32 v204, a76\n"
"v_accvgpr_read_b32 v205, a77\n"
"v_accvgpr_read_b32 v206, a78\n"
"v_accvgpr_read_b32 v207, a79\n"
"v_pk_mul_f32 v[200:201], v[200:201], v[144:145] op_sel_hi:[1,0]\n"
"v_pk_mul_f32 v[202:203], v[202:203], v[144:145] op_sel_hi:[1,0]\n"
"v_pk_mul_f32 v[204:205], v[204:205], v[144:145] op_sel_hi:[1,0]\n"
"v_pk_mul_f32 v[206:207], v[206:207], v[144:145] op_sel_hi:[1,0]\n"
"v_cvt_pk_bf16_f32 v212, v200, v201\n"
"v_cvt_pk_bf16_f32 v213, v202, v203\n"
"v_cvt_pk_bf16_f32 v214, v204, v205\n"
"v_cvt_pk_bf16_f32 v215, v206, v207\n"
"ds_write_b128 v186, v[208:211] offset:0\n"
"s_waitcnt lgkmcnt(1)\n"
"buffer_store_dwordx4 a[240:243], v190, s[48:51], s71 offen offset:0 sc1\n"
"v_accvgpr_read_b32 v192, a80\n"
"v_accvgpr_read_b32 v193, a81\n"
"v_accvgpr_read_b32 v194, a82\n"
"v_permlane16_swap_b32 v212, v214\n"
"v_permlane16_swap_b32 v213, v215\n"
"v_accvgpr_read_b32 v195, a83\n"
"v_accvgpr_read_b32 v196, a84\n"
"v_accvgpr_read_b32 v197, a85\n"
"v_accvgpr_read_b32 v198, a86\n"
"v_accvgpr_read_b32 v199, a87\n"
"v_pk_mul_f32 v[192:193], v[192:193], v[144:145] op_sel_hi:[1,0]\n"
"v_pk_mul_f32 v[194:195], v[194:195], v[144:145] op_sel_hi:[1,0]\n"
"v_pk_mul_f32 v[196:197], v[196:197], v[144:145] op_sel_hi:[1,0]\n"
"v_pk_mul_f32 v[198:199], v[198:199], v[144:145] op_sel_hi:[1,0]\n"
"v_cvt_pk_bf16_f32 v216, v192, v193\n"
"v_cvt_pk_bf16_f32 v217, v194, v195\n"
"v_cvt_pk_bf16_f32 v218, v196, v197\n"
"v_cvt_pk_bf16_f32 v219, v198, v199\n"
"ds_write_b128 v187, v[212:215] offset:0\n"
"buffer_store_dwordx4 a[244:247], v191, s[48:51], s71 offen offset:1024 sc1\n"
"v_accvgpr_read_b32 v200, a88\n"
"v_accvgpr_read_b32 v201, a89\n"
"v_accvgpr_read_b32 v202, a90\n"
"v_permlane16_swap_b32 v216, v218\n"
"v_permlane16_swap_b32 v217, v219\n"
"v_accvgpr_read_b32 v203, a91\n"
"v_accvgpr_read_b32 v204, a92\n"
"v_accvgpr_read_b32 v205, a93\n"
"v_accvgpr_read_b32 v206, a94\n"
"v_accvgpr_read_b32 v207, a95\n"
"v_pk_mul_f32 v[200:201], v[200:201], v[144:145] op_sel_hi:[1,0]\n"
"v_pk_mul_f32 v[202:203], v[202:203], v[144:145] op_sel_hi:[1,0]\n"
"v_pk_mul_f32 v[204:205], v[204:205], v[144:145] op_sel_hi:[1,0]\n"
"v_pk_mul_f32 v[206:207], v[206:207], v[144:145] op_sel_hi:[1,0]\n"
"v_cvt_pk_bf16_f32 v220, v200, v201\n"
"v_cvt_pk_bf16_f32 v221, v202, v203\n"
"v_cvt_pk_bf16_f32 v222, v204, v205\n"
"v_cvt_pk_bf16_f32 v223, v206, v207\n"
"ds_write_b128 v186, v[216:219] offset:128\n"
"buffer_store_dwordx4 a[248:251], v190, s[48:51], s71 offen offset:2048 sc1\n"
"s_nop 1\n"
"v_permlane16_swap_b32 v220, v222\n"
"v_permlane16_swap_b32 v221, v223\n"
"ds_write_b128 v187, v[220:223] offset:128\n"
"buffer_store_dwordx4 a[252:255], v191, s[48:51], s71 offen offset:3072 sc1\n"
"ds_read_b128 v[240:243], v188 offset:0\n"
"ds_read_b128 v[244:247], v188 offset:1024\n"
"ds_read_b128 v[248:251], v188 offset:2048\n"
"ds_read_b128 v[252:255], v188 offset:3072\n"
"v_mfma_f32_16x16x32_bf16 v[168:171], v[176:179], v[80:83], v[168:171]\n"
"v_mfma_f32_16x16x32_bf16 v[168:171], v[176:179], v[84:87], v[168:171]\n"
"v_mfma_f32_16x16x32_bf16 a[96:99], v[120:123], v[80:83], a[96:99]\n"
"v_mfma_f32_16x16x32_bf16 a[96:99], v[124:127], v[84:87], a[96:99]\n"
"v_mfma_f32_16x16x32_bf16 a[100:103], v[16:19], v[80:83], a[100:103]\n"
"v_mfma_f32_16x16x32_bf16 a[100:103], v[20:23], v[84:87], a[100:103]\n"
"v_mfma_f32_16x16x32_bf16 a[104:107], v[24:27], v[80:83], a[104:107]\n"
"v_mfma_f32_16x16x32_bf16 a[104:107], v[28:31], v[84:87], a[104:107]\n"
"v_mfma_f32_16x16x32_bf16 a[108:111], v[32:35], v[80:83], a[108:111]\n"
"v_mfma_f32_16x16x32_bf16 a[108:111], v[36:39], v[84:87], a[108:111]\n"
"v_mfma_f32_16x16x4_f32 v[148:151], v7, v168, 0\n"
"v_cmp_ngt_f32 vcc, s65, v168\n"
"s_or_b64 s[60:61], s[60:61], vcc\n"
"v_mfma_f32_16x16x32_bf16 a[112:115], v[40:43], v[80:83], a[112:115]\n"
"v_mfma_f32_16x16x32_bf16 a[112:115], v[44:47], v[84:87], a[112:115]\n"
"v_mfma_f32_16x16x32_bf16 a[116:119], v[48:51], v[80:83], a[116:119]\n"
"v_mfma_f32_16x16x32_bf16 a[116:119], v[52:55], v[84:87], a[116:119]\n"
"v_mfma_f32_16x16x32_bf16 a[120:123], v[96:99], v[80:83], a[120:123]\n"
"v_mfma_f32_16x16x32_bf16 a[120:123], v[100:103], v[84:87], a[120:123]\n"
"v_mfma_f32_16x16x32_bf16 a[124:127], v[104:107], v[80:83], a[124:127]\n"
"v_mfma_f32_16x16x32_bf16 a[124:127], v[108:111], v[84:87], a[124:127]\n"
"s_nop 3\n"
"v_rcp_f32 v148, v148\n"
"v_accvgpr_read_b32 v192, a96\n"
"v_accvgpr_read_b32 v193, a97\n"
"v_accvgpr_read_b32 v194, a98\n"
"v_accvgpr_read_b32 v195, a99\n"
"v_accvgpr_read_b32 v196, a100\n"
"v_accvgpr_read_b32 v197, a101\n"
"v_accvgpr_read_b32 v198, a102\n"
"v_accvgpr_read_b32 v199, a103\n"
"v_pk_mul_f32 v[192:193], v[192:193], v[148:149] op_sel_hi:[1,0]\n"
"v_pk_mul_f32 v[194:195], v[194:195], v[148:149] op_sel_hi:[1,0]\n"
"v_pk_mul_f32 v[196:197], v[196:197], v[148:149] op_sel_hi:[1,0]\n"
"v_pk_mul_f32 v[198:199], v[198:199], v[148:149] op_sel_hi:[1,0]\n"
"v_cvt_pk_bf16_f32 v224, v192, v193\n"
"v_cvt_pk_bf16_f32 v225, v194, v195\n"
"v_cvt_pk_bf16_f32 v226, v196, v197\n"
"v_cvt_pk_bf16_f32 v227, v198, v199\n"
"v_accvgpr_read_b32 v200, a104\n"
"v_accvgpr_read_b32 v201, a105\n"
"v_accvgpr_read_b32 v202, a106\n"
"v_permlane16_swap_b32 v224, v226\n"
"v_permlane16_swap_b32 v225, v227\n"
"v_accvgpr_read_b32 v203, a107\n"
"v_accvgpr_read_b32 v204, a108\n"
"v_accvgpr_read_b32 v205, a109\n"
"v_accvgpr_read_b32 v206, a110\n"
"v_accvgpr_read_b32 v207, a111\n"
"v_pk_mul_f32 v[200:201], v[200:201], v[148:149] op_sel_hi:[1,0]\n"
"v_pk_mul_f32 v[202:203], v[202:203], v[148:149] op_sel_hi:[1,0]\n"
"v_pk_mul_f32 v[204:205], v[204:205], v[148:149] op_sel_hi:[1,0]\n"
"v_pk_mul_f32 v[206:207], v[206:207], v[148:149] op_sel_hi:[1,0]\n"
"v_cvt_pk_bf16_f32 v228, v200, v201\n"
"v_cvt_pk_bf16_f32 v229, v202, v203\n"
"v_cvt_pk_bf16_f32 v230, v204, v205\n"
"v_cvt_pk_bf16_f32 v231, v206, v207\n"
"ds_write_b128 v186, v[224:227] offset:4096\n"
"s_waitcnt lgkmcnt(1)\n"
"buffer_store_dwordx4 v[240:243], v190, s[48:51], s72 offen offset:0 sc1\n"
"v_accvgpr_read_b32 v192, a112\n"
"v_accvgpr_read_b32 v193, a113\n"
"v_accvgpr_read_b32 v194, a114\n"
"v_permlane16_swap_b32 v228, v230\n"
"v_permlane16_swap_b32 v229, v231\n"
"v_accvgpr_read_b32 v195, a115\n"
"v_accvgpr_read_b32 v196, a116\n"
"v_accvgpr_read_b32 v197, a117\n"
"v_accvgpr_read_b32 v198, a118\n"
"v_accvgpr_read_b32 v199, a119\n"
"v_pk_mul_f32 v[192:193], v[192:193], v[148:149] op_sel_hi:[1,0]\n"
"v_pk_mul_f32 v[194:195], v[194:195], v[148:149] op_sel_hi:[1,0]\n"
"v_pk_mul_f32 v[196:197], v[196:197], v[148:149] op_sel_hi:[1,0]\n"
"v_pk_mul_f32 v[198:199], v[198:199], v[148:149] op_sel_hi:[1,0]\n"
"v_cvt_pk_bf16_f32 v232, v192, v193\n"
"v_cvt_pk_bf16_f32 v233, v194, v195\n"
"v_cvt_pk_bf16_f32 v234, v196, v197\n"
"v_cvt_pk_bf16_f32 v235, v198, v199\n"
"ds_write_b128 v187, v[228:231] offset:4096\n"
"buffer_store_dwordx4 v[244:247], v191, s[48:51], s72 offen offset:1024 sc1\n"
"v_accvgpr_read_b32 v200, a120\n"
"v_accvgpr_read_b32 v201, a121\n"
"v_accvgpr_read_b32 v202, a122\n"
"v_permlane16_swap_b32 v232, v234\n"
"v_permlane16_swap_b32 v233, v235\n"
"v_accvgpr_read_b32 v203, a123\n"
"v_accvgpr_read_b32 v204, a124\n"
"v_accvgpr_read_b32 v205, a125\n"
"v_accvgpr_read_b32 v206, a126\n"
"v_accvgpr_read_b32 v207, a127\n"
"v_pk_mul_f32 v[200:201], v[200:201], v[148:149] op_sel_hi:[1,0]\n"
"v_pk_mul_f32 v[202:203], v[202:203], v[148:149] op_sel_hi:[1,0]\n"
"v_pk_mul_f32 v[204:205], v[204:205], v[148:149] op_sel_hi:[1,0]\n"
"v_pk_mul_f32 v[206:207], v[206:207], v[148:149] op_sel_hi:[1,0]\n"
"v_cvt_pk_bf16_f32 v236, v200, v201\n"
"v_cvt_pk_bf16_f32 v237, v202, v203\n"
"v_cvt_pk_bf16_f32 v238, v204, v205\n"
"v_cvt_pk_bf16_f32 v239, v206, v207\n"
"ds_write_b128 v186, v[232:235] offset:4224\n"
"buffer_store_dwordx4 v[248:251], v190, s[48:51], s72 offen offset:2048 sc1\n"
"s_nop 1\n"
"v_permlane16_swap_b32 v236, v238\n"
"v_permlane16_swap_b32 v237, v239\n"
"ds_write_b128 v187, v[236:239] offset:4224\n"
"buffer_store_dwordx4 v[252:255], v191, s[48:51], s72 offen offset:3072 sc1\n"
"ds_read_b128 a[240:243], v188 offset:4096\n"
"ds_read_b128 a[244:247], v188 offset:5120\n"
"ds_read_b128 a[248:251], v188 offset:6144\n"
"ds_read_b128 a[252:255], v188 offset:7168\n"
"v_mfma_f32_16x16x32_bf16 v[172:175], v[176:179], v[88:91], v[172:175]\n"
"v_mfma_f32_16x16x32_bf16 v[172:175], v[176:179], v[92:95], v[172:175]\n"
"v_mfma_f32_16x16x32_bf16 a[128:131], v[120:123], v[88:91], a[128:131]\n"
"v_mfma_f32_16x16x32_bf16 a[128:131], v[124:127], v[92:95], a[128:131]\n"
"v_mfma_f32_16x16x32_bf16 a[132:135], v[16:19], v[88:91], a[132:135]\n"
"v_mfma_f32_16x16x32_bf16 a[132:135], v[20:23], v[92:95], a[132:135]\n"
"v_mfma_f32_16x16x32_bf16 a[136:139], v[24:27], v[88:91], a[136:139]\n"
"v_mfma_f32_16x16x32_bf16 a[136:139], v[28:31], v[92:95], a[136:139]\n"
"v_mfma_f32_16x16x32_bf16 a[140:143], v[32:35], v[88:91], a[140:143]\n"
"v_mfma_f32_16x16x32_bf16 a[140:143], v[36:39], v[92:95], a[140:143]\n"
"v_mfma_f32_16x16x4_f32 v[152:155], v7, v172, 0\n"
"v_cmp_ngt_f32 vcc, s65, v172\n"
"s_or_b64 s[60:61], s[60:61], vcc\n"
"v_mfma_f32_16x16x32_bf16 a[144:147], v[40:43], v[88:91], a[144:147]\n"
"v_mfma_f32_16x16x32_bf16 a[144:147], v[44:47], v[92:95], a[144:147]\n"
"v_mfma_f32_16x16x32_bf16 a[148:151], v[48:51], v[88:91], a[148:151]\n"
"v_mfma_f32_16x16x32_bf16 a[148:151], v[52:55], v[92:95], a[148:151]\n"
"v_mfma_f32_16x16x32_bf16 a[152:155], v[96:99], v[88:91], a[152:155]\n"
"v_mfma_f32_16x16x32_bf16 a[152:155], v[100:103], v[92:95], a[152:155]\n"
"v_mfma_f32_16x16x32_bf16 a[156:159], v[104:107], v[88:91], a[156:159]\n"
"v_mfma_f32_16x16x32_bf16 a[156:159], v[108:111], v[92:95], a[156:159]\n"
"s_nop 3\n"
"v_rcp_f32 v152, v152\n"
"v_accvgpr_read_b32 v192, a128\n"
"v_accvgpr_read_b32 v193, a129\n"
"v_accvgpr_read_b32 v194, a130\n"
"v_accvgpr_read_b32 v195, a131\n"
"v_accvgpr_read_b32 v196, a132\n"
"v_accvgpr_read_b32 v197, a133\n"
"v_accvgpr_read_b32 v198, a134\n"
"v_accvgpr_read_b32 v199, a135\n"
"v_pk_mul_f32 v[192:193], v[192:193], v[152:153] op_sel_hi:[1,0]\n"
"v_pk_mul_f32 v[194:195], v[194:195], v[152:153] op_sel_hi:[1,0]\n"
"v_pk_mul_f32 v[196:197], v[196:197], v[152:153] op_sel_hi:[1,0]\n"
"v_pk_mul_f32 v[198:199], v[198:199], v[152:153] op_sel_hi:[1,0]\n"
"v_cvt_pk_bf16_f32 v208, v192, v193\n"
"v_cvt_pk_bf16_f32 v209, v194, v195\n"
"v_cvt_pk_bf16_f32 v210, v196, v197\n"
"v_cvt_pk_bf16_f32 v211, v198, v199\n"
"v_accvgpr_read_b32 v200, a136\n"
"v_accvgpr_read_b32 v201, a137\n"
"v_accvgpr_read_b32 v202, a138\n"
"v_permlane16_swap_b32 v208, v210\n"
"v_permlane16_swap_b32 v209, v211\n"
"v_accvgpr_read_b32 v203, a139\n"
"v_accvgpr_read_b32 v204, a140\n"
"v_accvgpr_read_b32 v205, a141\n"
"v_accvgpr_read_b32 v206, a142\n"
"v_accvgpr_read_b32 v207, a143\n"
"v_pk_mul_f32 v[200:201], v[200:201], v[152:153] op_sel_hi:[1,0]\n"
"v_pk_mul_f32 v[202:203], v[202:203], v[152:153] op_sel_hi:[1,0]\n"
"v_pk_mul_f32 v[204:205], v[204:205], v[152:153] op_sel_hi:[1,0]\n"
"v_pk_mul_f32 v[206:207], v[206:207], v[152:153] op_sel_hi:[1,0]\n"
"v_cvt_pk_bf16_f32 v212, v200, v201\n"
"v_cvt_pk_bf16_f32 v213, v202, v203\n"
"v_cvt_pk_bf16_f32 v214, v204, v205\n"
"v_cvt_pk_bf16_f32 v215, v206, v207\n"
"ds_write_b128 v186, v[208:211] offset:0\n"
"s_waitcnt lgkmcnt(1)\n"
"buffer_store_dwordx4 a[240:243], v190, s[48:51], s73 offen offset:0 sc1\n"
"v_accvgpr_read_b32 v192, a144\n"
"v_accvgpr_read_b32 v193, a145\n"
"v_accvgpr_read_b32 v194, a146\n"
"v_permlane16_swap_b32 v212, v214\n"
"v_permlane16_swap_b32 v213, v215\n"
"v_accvgpr_read_b32 v195, a147\n"
"v_accvgpr_read_b32 v196, a148\n"
"v_accvgpr_read_b32 v197, a149\n"
"v_accvgpr_read_b32 v198, a150\n"
"v_accvgpr_read_b32 v199, a151\n"
"v_pk_mul_f32 v[192:193], v[192:193], v[152:153] op_sel_hi:[1,0]\n"
"v_pk_mul_f32 v[194:195], v[194:195], v[152:153] op_sel_hi:[1,0]\n"
"v_pk_mul_f32 v[196:197], v[196:197], v[152:153] op_sel_hi:[1,0]\n"
"v_pk_mul_f32 v[198:199], v[198:199], v[152:153] op_sel_hi:[1,0]\n"
"v_cvt_pk_bf16_f32 v216, v192, v193\n"
"v_cvt_pk_bf16_f32 v217, v194, v195\n"
"v_cvt_pk_bf16_f32 v218, v196, v197\n"
"v_cvt_pk_bf16_f32 v219, v198, v199\n"
"ds_write_b128 v187, v[212:215] offset:0\n"
"buffer_store_dwordx4 a[244:247], v191, s[48:51], s73 offen offset:1024 sc1\n"
"v_accvgpr_read_b32 v200, a152\n"
"v_accvgpr_read_b32 v201, a153\n"
"v_accvgpr_read_b32 v202, a154\n"
"v_permlane16_swap_b32 v216, v218\n"
"v_permlane16_swap_b32 v217, v219\n"
"v_accvgpr_read_b32 v203, a155\n"
"v_accvgpr_read_b32 v204, a156\n"
"v_accvgpr_read_b32 v205, a157\n"
"v_accvgpr_read_b32 v206, a158\n"
"v_accvgpr_read_b32 v207, a159\n"
"v_pk_mul_f32 v[200:201], v[200:201], v[152:153] op_sel_hi:[1,0]\n"
"v_pk_mul_f32 v[202:203], v[202:203], v[152:153] op_sel_hi:[1,0]\n"
"v_pk_mul_f32 v[204:205], v[204:205], v[152:153] op_sel_hi:[1,0]\n"
"v_pk_mul_f32 v[206:207], v[206:207], v[152:153] op_sel_hi:[1,0]\n"
"v_cvt_pk_bf16_f32 v220, v200, v201\n"
"v_cvt_pk_bf16_f32 v221, v202, v203\n"
"v_cvt_pk_bf16_f32 v222, v204, v205\n"
"v_cvt_pk_bf16_f32 v223, v206, v207\n"
"ds_write_b128 v186, v[216:219] offset:128\n"
"buffer_store_dwordx4 a[248:251], v190, s[48:51], s73 offen offset:2048 sc1\n"
"s_nop 1\n"
"v_permlane16_swap_b32 v220, v222\n"
"v_permlane16_swap_b32 v221, v223\n"
"ds_write_b128 v187, v[220:223] offset:128\n"
"buffer_store_dwordx4 a[252:255], v191, s[48:51], s73 offen offset:3072 sc1\n"
"ds_read_b128 v[240:243], v188 offset:0\n"
"ds_read_b128 v[244:247], v188 offset:1024\n"
"ds_read_b128 v[248:251], v188 offset:2048\n"
"ds_read_b128 v[252:255], v188 offset:3072\n"
"s_waitcnt lgkmcnt(0)\n"
"buffer_store_dwordx4 v[240:243], v190, s[48:51], s74 offen offset:0 sc1\n"
"buffer_store_dwordx4 v[244:247], v191, s[48:51], s74 offen offset:1024 sc1\n"
"buffer_store_dwordx4 v[248:251], v190, s[48:51], s74 offen offset:2048 sc1\n"
"buffer_store_dwordx4 v[252:255], v191, s[48:51], s74 offen offset:3072 sc1\n"
"s_branch L_done_%=\n"
"L_w3_%=:\n"
"s_waitcnt vmcnt(8)\n"
"ds_read_b128 v[56:59], v186 offset:0\n"
"ds_read_b128 v[60:63], v186 offset:256\n"
"ds_read_b128 v[64:67], v186 offset:512\n"
"ds_read_b128 v[68:71], v186 offset:768\n"
"s_waitcnt lgkmcnt(0)\n"
"v_lshlrev_b32 v88, 16, v56\n"
"v_and_b32 v89, 0xffff0000, v56\n"
"v_pk_mul_f32 v[88:89], v[88:89], s[56:57]\n"
"v_cvt_pk_bf16_f32 v90, v88, v89\n"
"v_accvgpr_write_b32 a160, v90\n"
"v_lshlrev_b32 v92, 16, v57\n"
"v_and_b32 v93, 0xffff0000, v57\n"
"v_pk_mul_f32 v[92:93], v[92:93], s[56:57]\n"
"v_cvt_pk_bf16_f32 v94, v92, v93\n"
"v_accvgpr_write_b32 a161, v94\n"
"v_lshlrev_b32 v88, 16, v58\n"
"v_and_b32 v89, 0xffff0000, v58\n"
"v_pk_mul_f32 v[88:89], v[88:89], s[56:57]\n"
"v_cvt_pk_bf16_f32 v90, v88, v89\n"
"v_accvgpr_write_b32 a162, v90\n"
"v_lshlrev_b32 v92, 16, v59\n"
"v_and_b32 v93, 0xffff0000, v59\n"
"v_pk_mul_f32 v[92:93], v[92:93], s[56:57]\n"
"v_cvt_pk_bf16_f32 v94, v92, v93\n"
"v_accvgpr_write_b32 a163, v94\n"
"v_lshlrev_b32 v88, 16, v60\n"
"v_and_b32 v89, 0xffff0000, v60\n"
"v_pk_mul_f32 v[88:89], v[88:89], s[56:57]\n"
"v_cvt_pk_bf16_f32 v90, v88, v89\n"
"v_accvgpr_write_b32 a164, v90\n"
"v_lshlrev_b32 v92, 16, v61\n"
"v_and_b32 v93, 0xffff0000, v61\n"
"v_pk_mul_f32 v[92:93], v[92:93], s[56:57]\n"
"v_cvt_pk_bf16_f32 v94, v92, v93\n"
"v_accvgpr_write_b32 a165, v94\n"
"v_lshlrev_b32 v88, 16, v62\n"
"v_and_b32 v89, 0xffff0000, v62\n"
"v_pk_mul_f32 v[88:89], v[88:89], s[56:57]\n"
"v_cvt_pk_bf16_f32 v90, v88, v89\n"
"v_accvgpr_write_b32 a166, v90\n"
"v_lshlrev_b32 v92, 16, v63\n"
"v_and_b32 v93, 0xffff0000, v63\n"
"v_pk_mul_f32 v[92:93], v[92:93], s[56:57]\n"
"v_cvt_pk_bf16_f32 v94, v92, v93\n"
"v_accvgpr_write_b32 a167, v94\n"
"v_mfma_f32_16x16x32_bf16 v[0:3], v[192:195], a[160:163], 0\n"
"v_mfma_f32_16x16x32_bf16 v[4:7], v[208:211], a[160:163], 0\n"
"v_mfma_f32_16x16x32_bf16 v[8:11], v[224:227], a[160:163], 0\n"
"v_mfma_f32_16x16x32_bf16 v[12:15], v[240:243], a[160:163], 0\n"
"v_lshlrev_b32 v88, 16, v64\n"
"v_and_b32 v89, 0xffff0000, v64\n"
"v_pk_mul_f32 v[88:89], v[88:89], s[56:57]\n"
"v_cvt_pk_bf16_f32 v90, v88, v89\n"
"v_accvgpr_write_b32 a168, v90\n"
"v_lshlrev_b32 v92, 16, v65\n"
"v_and_b32 v93, 0xffff0000, v65\n"
"v_pk_mul_f32 v[92:93], v[92:93], s[56:57]\n"
"v_cvt_pk_bf16_f32 v94, v92, v93\n"
"v_accvgpr_write_b32 a169, v94\n"
"v_lshlrev_b32 v88, 16, v66\n"
"v_and_b32 v89, 0xffff0000, v66\n"
"v_pk_mul_f32 v[88:89], v[88:89], s[56:57]\n"
"v_cvt_pk_bf16_f32 v90, v88, v89\n"
"v_accvgpr_write_b32 a170, v90\n"
"v_lshlrev_b32 v92, 16, v67\n"
"v_and_b32 v93, 0xffff0000, v67\n"
"v_pk_mul_f32 v[92:93], v[92:93], s[56:57]\n"
"v_cvt_pk_bf16_f32 v94, v92, v93\n"
"v_accvgpr_write_b32 a171, v94\n"
"v_mfma_f32_16x16x32_bf16 v[0:3], v[196:199], a[164:167], v[0:3]\n"
"v_mfma_f32_16x16x32_bf16 v[4:7], v[212:215], a[164:167], v[4:7]\n"
"v_mfma_f32_16x16x32_bf16 v[8:11], v[228:231], a[164:167], v[8:11]\n"
"v_mfma_f32_16x16x32_bf16 v[12:15], v[244:247], a[164:167], v[12:15]\n"
"v_lshlrev_b32 v88, 16, v68\n"
"v_and_b32 v89, 0xffff0000, v68\n"
"v_pk_mul_f32 v[88:89], v[88:89], s[56:57]\n"
"v_cvt_pk_bf16_f32 v90, v88, v89\n"
"v_accvgpr_write_b32 a172, v90\n"
"v_lshlrev_b32 v92, 16, v69\n"
"v_and_b32 v93, 0xffff0000, v69\n"
"v_pk_mul_f32 v[92:93], v[92:93], s[56:57]\n"
"v_cvt_pk_bf16_f32 v94, v92, v93\n"
"v_accvgpr_write_b32 a173, v94\n"
"v_lshlrev_b32 v88, 16, v70\n"
"v_and_b32 v89, 0xffff0000, v70\n"
"v_pk_mul_f32 v[88:89], v[88:89], s[56:57]\n"
"v_cvt_pk_bf16_f32 v90, v88, v89\n"
"v_accvgpr_write_b32 a174, v90\n"
"v_lshlrev_b32 v92, 16, v71\n"
"v_and_b32 v93, 0xffff0000, v71\n"
"v_pk_mul_f32 v[92:93], v[92:93], s[56:57]\n"
"v_cvt_pk_bf16_f32 v94, v92, v93\n"
"v_accvgpr_write_b32 a175, v94\n"
"v_mfma_f32_16x16x32_bf16 v[0:3], v[200:203], a[168:171], v[0:3]\n"
"v_mfma_f32_16x16x32_bf16 v[4:7], v[216:219], a[168:171], v[4:7]\n"
"v_mfma_f32_16x16x32_bf16 v[8:11], v[232:235], a[168:171], v[8:11]\n"
"v_mfma_f32_16x16x32_bf16 v[12:15], v[248:251], a[168:171], v[12:15]\n"
"s_nop 1\n"
"v_mfma_f32_16x16x32_bf16 v[0:3], v[204:207], a[172:175], v[0:3]\n"
"v_mfma_f32_16x16x32_bf16 v[4:7], v[220:223], a[172:175], v[4:7]\n"
"v_mfma_f32_16x16x32_bf16 v[8:11], v[236:239], a[172:175], v[8:11]\n"
"v_mfma_f32_16x16x32_bf16 v[12:15], v[252:255], a[172:175], v[12:15]\n"
"s_nop 3\n"
"s_cmp_lg_u32 s66, 0\n"
"s_cbranch_scc1 L_gm0_b_%=\n"
"v_max3_f32 v136, v0, v1, v2\n"
"v_max3_f32 v137, v3, v4, v5\n"
"v_max3_f32 v136, v136, v6, v7\n"
"v_max3_f32 v137, v137, v8, v9\n"
"v_max3_f32 v136, v136, v10, v11\n"
"v_max3_f32 v137, v137, v12, v13\n"
"v_max3_f32 v136, v136, v14, v15\n"
"v_max_f32 v138, v136, v137\n"
"v_max_f32 v139, v136, v137\n"
"s_nop 1\n"
"v_permlane32_swap_b32 v138, v139\n"
"s_nop 1\n"
"v_max_f32 v136, v138, v139\n"
"v_max_f32 v137, v138, v139\n"
"s_nop 1\n"
"v_permlane16_swap_b32 v136, v137\n"
"s_nop 1\n"
"v_max_f32 v136, v136, v137\n"
"v_sub_f32 v136, 0, v136\n"
"L_gm0_b_%=:\n"
"v_mov_b32 v137, v136\n"
"v_mov_b32 v138, v136\n"
"v_mov_b32 v139, v136\n"
"v_pk_add_f32 v[0:1], v[0:1], v[136:137]\n"
"v_pk_add_f32 v[2:3], v[2:3], v[136:137]\n"
"v_pk_add_f32 v[4:5], v[4:5], v[136:137]\n"
"v_pk_add_f32 v[6:7], v[6:7], v[136:137]\n"
"v_pk_add_f32 v[8:9], v[8:9], v[136:137]\n"
"v_pk_add_f32 v[10:11], v[10:11], v[136:137]\n"
"v_pk_add_f32 v[12:13], v[12:13], v[136:137]\n"
"v_pk_add_f32 v[14:15], v[14:15], v[136:137]\n"
"v_exp_f32 v0, v0\n"
"v_exp_f32 v1, v1\n"
"v_exp_f32 v2, v2\n"
"v_exp_f32 v3, v3\n"
"v_exp_f32 v4, v4\n"
"v_exp_f32 v5, v5\n"
"v_exp_f32 v6, v6\n"
"v_exp_f32 v7, v7\n"
"v_cvt_pk_bf16_f32 v16, v0, v1\n"
"v_cvt_pk_bf16_f32 v17, v2, v3\n"
"v_exp_f32 v8, v8\n"
"v_exp_f32 v9, v9\n"
"v_exp_f32 v10, v10\n"
"v_exp_f32 v11, v11\n"
"v_cvt_pk_bf16_f32 v18, v4, v5\n"
"v_cvt_pk_bf16_f32 v19, v6, v7\n"
"v_exp_f32 v12, v12\n"
"v_exp_f32 v13, v13\n"
"v_exp_f32 v14, v14\n"
"v_exp_f32 v15, v15\n"
"v_cvt_pk_bf16_f32 v20, v8, v9\n"
"v_cvt_pk_bf16_f32 v21, v10, v11\n"
"s_nop 0\n"
"v_cvt_pk_bf16_f32 v22, v12, v13\n"
"v_cvt_pk_bf16_f32 v23, v14, v15\n"
"s_add_u32 m0, s62, 12288\n"
"s_nop 0\n"
"buffer_load_dwordx4 v183, s[44:47], s73 offen offset:0 nt lds\n"
"buffer_load_dwordx4 v184, s[44:47], s73 offen offset:1024 nt lds\n"
"buffer_load_dwordx4 v183, s[44:47], s73 offen offset:2048 nt lds\n"
"buffer_load_dwordx4 v184, s[44:47], s73 offen offset:3072 nt lds\n"
"s_waitcnt vmcnt(8)\n"
"ds_read_b128 v[72:75], v186 offset:4096\n"
"ds_read_b128 v[76:79], v186 offset:4352\n"
"ds_read_b128 v[80:83], v186 offset:4608\n"
"ds_read_b128 v[84:87], v186 offset:4864\n"
"s_waitcnt lgkmcnt(0)\n"
"v_lshlrev_b32 v88, 16, v72\n"
"v_and_b32 v89, 0xffff0000, v72\n"
"v_pk_mul_f32 v[88:89], v[88:89], s[56:57]\n"
"v_cvt_pk_bf16_f32 v90, v88, v89\n"
"v_accvgpr_write_b32 a176, v90\n"
"v_lshlrev_b32 v92, 16, v73\n"
"v_and_b32 v93, 0xffff0000, v73\n"
"v_pk_mul_f32 v[92:93], v[92:93], s[56:57]\n"
"v_cvt_pk_bf16_f32 v94, v92, v93\n"
"v_accvgpr_write_b32 a177, v94\n"
"v_lshlrev_b32 v88, 16, v74\n"
"v_and_b32 v89, 0xffff0000, v74\n"
"v_pk_mul_f32 v[88:89], v[88:89], s[56:57]\n"
"v_cvt_pk_bf16_f32 v90, v88, v89\n"
"v_accvgpr_write_b32 a178, v90\n"
"v_lshlrev_b32 v92, 16, v75\n"
"v_and_b32 v93, 0xffff0000, v75\n"
"v_pk_mul_f32 v[92:93], v[92:93], s[56:57]\n"
"v_cvt_pk_bf16_f32 v94, v92, v93\n"
"v_accvgpr_write_b32 a179, v94\n"
"v_lshlrev_b32 v88, 16, v76\n"
"v_and_b32 v89, 0xffff0000, v76\n"
"v_pk_mul_f32 v[88:89], v[88:89], s[56:57]\n"
"v_cvt_pk_bf16_f32 v90, v88, v89\n"
"v_accvgpr_write_b32 a180, v90\n"
"v_lshlrev_b32 v92, 16, v77\n"
"v_and_b32 v93, 0xffff0000, v77\n"
"v_pk_mul_f32 v[92:93], v[92:93], s[56:57]\n"
"v_cvt_pk_bf16_f32 v94, v92, v93\n"
"v_accvgpr_write_b32 a181, v94\n"
"v_lshlrev_b32 v88, 16, v78\n"
"v_and_b32 v89, 0xffff0000, v78\n"
"v_pk_mul_f32 v[88:89], v[88:89], s[56:57]\n"
"v_cvt_pk_bf16_f32 v90, v88, v89\n"
"v_accvgpr_write_b32 a182, v90\n"
"v_lshlrev_b32 v92, 16, v79\n"
"v_and_b32 v93, 0xffff0000, v79\n"
"v_pk_mul_f32 v[92:93], v[92:93], s[56:57]\n"
"v_cvt_pk_bf16_f32 v94, v92, v93\n"
"v_accvgpr_write_b32 a183, v94\n"
"v_mfma_f32_16x16x32_bf16 v[0:3], v[192:195], a[176:179], 0\n"
"v_mfma_f32_16x16x32_bf16 v[4:7], v[208:211], a[176:179], 0\n"
"v_mfma_f32_16x16x32_bf16 v[8:11], v[224:227], a[176:179], 0\n"
"v_mfma_f32_16x16x32_bf16 v[12:15], v[240:243], a[176:179], 0\n"
"v_lshlrev_b32 v88, 16, v80\n"
"v_and_b32 v89, 0xffff0000, v80\n"
"v_pk_mul_f32 v[88:89], v[88:89], s[56:57]\n"
"v_cvt_pk_bf16_f32 v90, v88, v89\n"
"v_accvgpr_write_b32 a184, v90\n"
"v_lshlrev_b32 v92, 16, v81\n"
"v_and_b32 v93, 0xffff0000, v81\n"
"v_pk_mul_f32 v[92:93], v[92:93], s[56:57]\n"
"v_cvt_pk_bf16_f32 v94, v92, v93\n"
"v_accvgpr_write_b32 a185, v94\n"
"v_lshlrev_b32 v88, 16, v82\n"
"v_and_b32 v89, 0xffff0000, v82\n"
"v_pk_mul_f32 v[88:89], v[88:89], s[56:57]\n"
"v_cvt_pk_bf16_f32 v90, v88, v89\n"
"v_accvgpr_write_b32 a186, v90\n"
"v_lshlrev_b32 v92, 16, v83\n"
"v_and_b32 v93, 0xffff0000, v83\n"
"v_pk_mul_f32 v[92:93], v[92:93], s[56:57]\n"
"v_cvt_pk_bf16_f32 v94, v92, v93\n"
"v_accvgpr_write_b32 a187, v94\n"
"v_mfma_f32_16x16x32_bf16 v[0:3], v[196:199], a[180:183], v[0:3]\n"
"v_mfma_f32_16x16x32_bf16 v[4:7], v[212:215], a[180:183], v[4:7]\n"
"v_mfma_f32_16x16x32_bf16 v[8:11], v[228:231], a[180:183], v[8:11]\n"
"v_mfma_f32_16x16x32_bf16 v[12:15], v[244:247], a[180:183], v[12:15]\n"
"v_lshlrev_b32 v88, 16, v84\n"
"v_and_b32 v89, 0xffff0000, v84\n"
"v_pk_mul_f32 v[88:89], v[88:89], s[56:57]\n"
"v_cvt_pk_bf16_f32 v90, v88, v89\n"
"v_accvgpr_write_b32 a188, v90\n"
"v_lshlrev_b32 v92, 16, v85\n"
"v_and_b32 v93, 0xffff0000, v85\n"
"v_pk_mul_f32 v[92:93], v[92:93], s[56:57]\n"
"v_cvt_pk_bf16_f32 v94, v92, v93\n"
"v_accvgpr_write_b32 a189, v94\n"
"v_lshlrev_b32 v88, 16, v86\n"
"v_and_b32 v89, 0xffff0000, v86\n"
"v_pk_mul_f32 v[88:89], v[88:89], s[56:57]\n"
"v_cvt_pk_bf16_f32 v90, v88, v89\n"
"v_accvgpr_write_b32 a190, v90\n"
"v_lshlrev_b32 v92, 16, v87\n"
"v_and_b32 v93, 0xffff0000, v87\n"
"v_pk_mul_f32 v[92:93], v[92:93], s[56:57]\n"
"v_cvt_pk_bf16_f32 v94, v92, v93\n"
"v_accvgpr_write_b32 a191, v94\n"
"v_mfma_f32_16x16x32_bf16 v[0:3], v[200:203], a[184:187], v[0:3]\n"
"v_mfma_f32_16x16x32_bf16 v[4:7], v[216:219], a[184:187], v[4:7]\n"
"v_mfma_f32_16x16x32_bf16 v[8:11], v[232:235], a[184:187], v[8:11]\n"
"v_mfma_f32_16x16x32_bf16 v[12:15], v[248:251], a[184:187], v[12:15]\n"
"s_nop 1\n"
"v_mfma_f32_16x16x32_bf16 v[0:3], v[204:207], a[188:191], v[0:3]\n"
"v_mfma_f32_16x16x32_bf16 v[4:7], v[220:223], a[188:191], v[4:7]\n"
"v_mfma_f32_16x16x32_bf16 v[8:11], v[236:239], a[188:191], v[8:11]\n"
"v_mfma_f32_16x16x32_bf16 v[12:15], v[252:255], a[188:191], v[12:15]\n"
"s_nop 3\n"
"s_cmp_lg_u32 s66, 0\n"
"s_cbranch_scc1 L_gm1_b_%=\n"
"v_max3_f32 v140, v0, v1, v2\n"
"v_max3_f32 v141, v3, v4, v5\n"
"v_max3_f32 v140, v140, v6, v7\n"
"v_max3_f32 v141, v141, v8, v9\n"
"v_max3_f32 v140, v140, v10, v11\n"
"v_max3_f32 v141, v141, v12, v13\n"
"v_max3_f32 v140, v140, v14, v15\n"
"v_max_f32 v142, v140, v141\n"
"v_max_f32 v143, v140, v141\n"
"s_nop 1\n"
"v_permlane32_swap_b32 v142, v143\n"
"s_nop 1\n"
"v_max_f32 v140, v142, v143\n"
"v_max_f32 v141, v142, v143\n"
"s_nop 1\n"
"v_permlane16_swap_b32 v140, v141\n"
"s_nop 1\n"
"v_max_f32 v140, v140, v141\n"
"v_sub_f32 v140, 0, v140\n"
"L_gm1_b_%=:\n"
"v_mov_b32 v141, v140\n"
"v_mov_b32 v142, v140\n"
"v_mov_b32 v143, v140\n"
"v_pk_add_f32 v[0:1], v[0:1], v[140:141]\n"
"v_pk_add_f32 v[2:3], v[2:3], v[140:141]\n"
"v_pk_add_f32 v[4:5], v[4:5], v[140:141]\n"
"v_pk_add_f32 v[6:7], v[6:7], v[140:141]\n"
"v_pk_add_f32 v[8:9], v[8:9], v[140:141]\n"
"v_pk_add_f32 v[10:11], v[10:11], v[140:141]\n"
"v_pk_add_f32 v[12:13], v[12:13], v[140:141]\n"
"v_pk_add_f32 v[14:15], v[14:15], v[140:141]\n"
"v_exp_f32 v0, v0\n"
"v_exp_f32 v1, v1\n"
"v_exp_f32 v2, v2\n"
"v_exp_f32 v3, v3\n"
"v_exp_f32 v4, v4\n"
"v_exp_f32 v5, v5\n"
"v_exp_f32 v6, v6\n"
"v_exp_f32 v7, v7\n"
"v_cvt_pk_bf16_f32 v24, v0, v1\n"
"v_cvt_pk_bf16_f32 v25, v2, v3\n"
"v_exp_f32 v8, v8\n"
"v_exp_f32 v9, v9\n"
"v_exp_f32 v10, v10\n"
"v_exp_f32 v11, v11\n"
"v_cvt_pk_bf16_f32 v26, v4, v5\n"
"v_cvt_pk_bf16_f32 v27, v6, v7\n"
"v_exp_f32 v12, v12\n"
"v_exp_f32 v13, v13\n"
"v_exp_f32 v14, v14\n"
"v_exp_f32 v15, v15\n"
"v_cvt_pk_bf16_f32 v28, v8, v9\n"
"v_cvt_pk_bf16_f32 v29, v10, v11\n"
"s_nop 0\n"
"v_cvt_pk_bf16_f32 v30, v12, v13\n"
"v_cvt_pk_bf16_f32 v31, v14, v15\n"
"s_add_u32 s63, s53, 0x4000\n"
"s_add_u32 m0, s53, 0x4000\n"
"s_nop 0\n"
"buffer_load_dwordx4 v183, s[36:39], s63 offen offset:0 lds\n"
"buffer_load_dwordx4 v184, s[36:39], s63 offen offset:1024 lds\n"
"buffer_load_dwordx4 v183, s[36:39], s63 offen offset:2048 lds\n"
"buffer_load_dwordx4 v184, s[36:39], s63 offen offset:3072 lds\n"
"s_waitcnt vmcnt(8)\n"
"ds_read_b128 v[56:59], v186 offset:8192\n"
"ds_read_b128 v[60:63], v186 offset:8448\n"
"ds_read_b128 v[64:67], v186 offset:8704\n"
"ds_read_b128 v[68:71], v186 offset:8960\n"
"s_waitcnt lgkmcnt(0)\n"
"v_lshlrev_b32 v88, 16, v56\n"
"v_and_b32 v89, 0xffff0000, v56\n"
"v_pk_mul_f32 v[88:89], v[88:89], s[56:57]\n"
"v_cvt_pk_bf16_f32 v90, v88, v89\n"
"v_accvgpr_write_b32 a192, v90\n"
"v_lshlrev_b32 v92, 16, v57\n"
"v_and_b32 v93, 0xffff0000, v57\n"
"v_pk_mul_f32 v[92:93], v[92:93], s[56:57]\n"
"v_cvt_pk_bf16_f32 v94, v92, v93\n"
"v_accvgpr_write_b32 a193, v94\n"
"v_lshlrev_b32 v88, 16, v58\n"
"v_and_b32 v89, 0xffff0000, v58\n"
"v_pk_mul_f32 v[88:89], v[88:89], s[56:57]\n"
"v_cvt_pk_bf16_f32 v90, v88, v89\n"
"v_accvgpr_write_b32 a194, v90\n"
"v_lshlrev_b32 v92, 16, v59\n"
"v_and_b32 v93, 0xffff0000, v59\n"
"v_pk_mul_f32 v[92:93], v[92:93], s[56:57]\n"
"v_cvt_pk_bf16_f32 v94, v92, v93\n"
"v_accvgpr_write_b32 a195, v94\n"
"v_lshlrev_b32 v88, 16, v60\n"
"v_and_b32 v89, 0xffff0000, v60\n"
"v_pk_mul_f32 v[88:89], v[88:89], s[56:57]\n"
"v_cvt_pk_bf16_f32 v90, v88, v89\n"
"v_accvgpr_write_b32 a196, v90\n"
"v_lshlrev_b32 v92, 16, v61\n"
"v_and_b32 v93, 0xffff0000, v61\n"
"v_pk_mul_f32 v[92:93], v[92:93], s[56:57]\n"
"v_cvt_pk_bf16_f32 v94, v92, v93\n"
"v_accvgpr_write_b32 a197, v94\n"
"v_lshlrev_b32 v88, 16, v62\n"
"v_and_b32 v89, 0xffff0000, v62\n"
"v_pk_mul_f32 v[88:89], v[88:89], s[56:57]\n"
"v_cvt_pk_bf16_f32 v90, v88, v89\n"
"v_accvgpr_write_b32 a198, v90\n"
"v_lshlrev_b32 v92, 16, v63\n"
"v_and_b32 v93, 0xffff0000, v63\n"
"v_pk_mul_f32 v[92:93], v[92:93], s[56:57]\n"
"v_cvt_pk_bf16_f32 v94, v92, v93\n"
"v_accvgpr_write_b32 a199, v94\n"
"v_mfma_f32_16x16x32_bf16 v[0:3], v[192:195], a[192:195], 0\n"
"v_mfma_f32_16x16x32_bf16 v[4:7], v[208:211], a[192:195], 0\n"
"v_mfma_f32_16x16x32_bf16 v[8:11], v[224:227], a[192:195], 0\n"
"v_mfma_f32_16x16x32_bf16 v[12:15], v[240:243], a[192:195], 0\n"
"v_lshlrev_b32 v88, 16, v64\n"
"v_and_b32 v89, 0xffff0000, v64\n"
"v_pk_mul_f32 v[88:89], v[88:89], s[56:57]\n"
"v_cvt_pk_bf16_f32 v90, v88, v89\n"
"v_accvgpr_write_b32 a200, v90\n"
"v_lshlrev_b32 v92, 16, v65\n"
"v_and_b32 v93, 0xffff0000, v65\n"
"v_pk_mul_f32 v[92:93], v[92:93], s[56:57]\n"
"v_cvt_pk_bf16_f32 v94, v92, v93\n"
"v_accvgpr_write_b32 a201, v94\n"
"v_lshlrev_b32 v88, 16, v66\n"
"v_and_b32 v89, 0xffff0000, v66\n"
"v_pk_mul_f32 v[88:89], v[88:89], s[56:57]\n"
"v_cvt_pk_bf16_f32 v90, v88, v89\n"
"v_accvgpr_write_b32 a202, v90\n"
"v_lshlrev_b32 v92, 16, v67\n"
"v_and_b32 v93, 0xffff0000, v67\n"
"v_pk_mul_f32 v[92:93], v[92:93], s[56:57]\n"
"v_cvt_pk_bf16_f32 v94, v92, v93\n"
"v_accvgpr_write_b32 a203, v94\n"
"v_mfma_f32_16x16x32_bf16 v[0:3], v[196:199], a[196:199], v[0:3]\n"
"v_mfma_f32_16x16x32_bf16 v[4:7], v[212:215], a[196:199], v[4:7]\n"
"v_mfma_f32_16x16x32_bf16 v[8:11], v[228:231], a[196:199], v[8:11]\n"
"v_mfma_f32_16x16x32_bf16 v[12:15], v[244:247], a[196:199], v[12:15]\n"
"v_lshlrev_b32 v88, 16, v68\n"
"v_and_b32 v89, 0xffff0000, v68\n"
"v_pk_mul_f32 v[88:89], v[88:89], s[56:57]\n"
"v_cvt_pk_bf16_f32 v90, v88, v89\n"
"v_accvgpr_write_b32 a204, v90\n"
"v_lshlrev_b32 v92, 16, v69\n"
"v_and_b32 v93, 0xffff0000, v69\n"
"v_pk_mul_f32 v[92:93], v[92:93], s[56:57]\n"
"v_cvt_pk_bf16_f32 v94, v92, v93\n"
"v_accvgpr_write_b32 a205, v94\n"
"v_lshlrev_b32 v88, 16, v70\n"
"v_and_b32 v89, 0xffff0000, v70\n"
"v_pk_mul_f32 v[88:89], v[88:89], s[56:57]\n"
"v_cvt_pk_bf16_f32 v90, v88, v89\n"
"v_accvgpr_write_b32 a206, v90\n"
"v_lshlrev_b32 v92, 16, v71\n"
"v_and_b32 v93, 0xffff0000, v71\n"
"v_pk_mul_f32 v[92:93], v[92:93], s[56:57]\n"
"v_cvt_pk_bf16_f32 v94, v92, v93\n"
"v_accvgpr_write_b32 a207, v94\n"
"v_mfma_f32_16x16x32_bf16 v[0:3], v[200:203], a[200:203], v[0:3]\n"
"v_mfma_f32_16x16x32_bf16 v[4:7], v[216:219], a[200:203], v[4:7]\n"
"v_mfma_f32_16x16x32_bf16 v[8:11], v[232:235], a[200:203], v[8:11]\n"
"v_mfma_f32_16x16x32_bf16 v[12:15], v[248:251], a[200:203], v[12:15]\n"
"s_nop 1\n"
"v_mfma_f32_16x16x32_bf16 v[0:3], v[204:207], a[204:207], v[0:3]\n"
"v_mfma_f32_16x16x32_bf16 v[4:7], v[220:223], a[204:207], v[4:7]\n"
"v_mfma_f32_16x16x32_bf16 v[8:11], v[236:239], a[204:207], v[8:11]\n"
"v_mfma_f32_16x16x32_bf16 v[12:15], v[252:255], a[204:207], v[12:15]\n"
"s_nop 3\n"
"s_cmp_lg_u32 s66, 0\n"
"s_cbranch_scc1 L_gm2_b_%=\n"
"v_max3_f32 v144, v0, v1, v2\n"
"v_max3_f32 v145, v3, v4, v5\n"
"v_max3_f32 v144, v144, v6, v7\n"
"v_max3_f32 v145, v145, v8, v9\n"
"v_max3_f32 v144, v144, v10, v11\n"
"v_max3_f32 v145, v145, v12, v13\n"
"v_max3_f32 v144, v144, v14, v15\n"
"v_max_f32 v146, v144, v145\n"
"v_max_f32 v147, v144, v145\n"
"s_nop 1\n"
"v_permlane32_swap_b32 v146, v147\n"
"s_nop 1\n"
"v_max_f32 v144, v146, v147\n"
"v_max_f32 v145, v146, v147\n"
"s_nop 1\n"
"v_permlane16_swap_b32 v144, v145\n"
"s_nop 1\n"
"v_max_f32 v144, v144, v145\n"
"v_sub_f32 v144, 0, v144\n"
"L_gm2_b_%=:\n"
"v_mov_b32 v145, v144\n"
"v_mov_b32 v146, v144\n"
"v_mov_b32 v147, v144\n"
"v_pk_add_f32 v[0:1], v[0:1], v[144:145]\n"
"v_pk_add_f32 v[2:3], v[2:3], v[144:145]\n"
"v_pk_add_f32 v[4:5], v[4:5], v[144:145]\n"
"v_pk_add_f32 v[6:7], v[6:7], v[144:145]\n"
"v_pk_add_f32 v[8:9], v[8:9], v[144:145]\n"
"v_pk_add_f32 v[10:11], v[10:11], v[144:145]\n"
"v_pk_add_f32 v[12:13], v[12:13], v[144:145]\n"
"v_pk_add_f32 v[14:15], v[14:15], v[144:145]\n"
"v_exp_f32 v0, v0\n"
"v_exp_f32 v1, v1\n"
"v_exp_f32 v2, v2\n"
"v_exp_f32 v3, v3\n"
"v_exp_f32 v4, v4\n"
"v_exp_f32 v5, v5\n"
"v_exp_f32 v6, v6\n"
"v_exp_f32 v7, v7\n"
"v_cvt_pk_bf16_f32 v32, v0, v1\n"
"v_cvt_pk_bf16_f32 v33, v2, v3\n"
"v_exp_f32 v8, v8\n"
"v_exp_f32 v9, v9\n"
"v_exp_f32 v10, v10\n"
"v_exp_f32 v11, v11\n"
"v_cvt_pk_bf16_f32 v34, v4, v5\n"
"v_cvt_pk_bf16_f32 v35, v6, v7\n"
"v_exp_f32 v12, v12\n"
"v_exp_f32 v13, v13\n"
"v_exp_f32 v14, v14\n"
"v_exp_f32 v15, v15\n"
"v_cvt_pk_bf16_f32 v36, v8, v9\n"
"v_cvt_pk_bf16_f32 v37, v10, v11\n"
"s_nop 0\n"
"v_cvt_pk_bf16_f32 v38, v12, v13\n"
"v_cvt_pk_bf16_f32 v39, v14, v15\n"
"s_add_u32 s63, s53, 0x4000\n"
"s_add_u32 m0, s54, 0x0\n"
"s_nop 0\n"
"buffer_load_dwordx4 v183, s[40:43], s63 offen offset:0 lds\n"
"buffer_load_dwordx4 v184, s[40:43], s63 offen offset:1024 lds\n"
"buffer_load_dwordx4 v183, s[40:43], s63 offen offset:2048 lds\n"
"buffer_load_dwordx4 v184, s[40:43], s63 offen offset:3072 lds\n"
"s_waitcnt vmcnt(8)\n"
"ds_read_b128 v[72:75], v186 offset:12288\n"
"ds_read_b128 v[76:79], v186 offset:12544\n"
"ds_read_b128 v[80:83], v186 offset:12800\n"
"ds_read_b128 v[84:87], v186 offset:13056\n"
"s_waitcnt lgkmcnt(0)\n"
"s_add_u32 s44, s44, 155648\n"
"s_addc_u32 s45, s45, 0\n"
"s_add_u32 s63, s53, 0x8000\n"
"s_add_u32 m0, s53, 0x8000\n"
"s_nop 0\n"
"buffer_load_dwordx4 v183, s[36:39], s63 offen offset:0 lds\n"
"buffer_load_dwordx4 v184, s[36:39], s63 offen offset:1024 lds\n"
"buffer_load_dwordx4 v183, s[36:39], s63 offen offset:2048 lds\n"
"buffer_load_dwordx4 v184, s[36:39], s63 offen offset:3072 lds\n"
"v_lshlrev_b32 v88, 16, v72\n"
"v_and_b32 v89, 0xffff0000, v72\n"
"v_pk_mul_f32 v[88:89], v[88:89], s[56:57]\n"
"v_cvt_pk_bf16_f32 v90, v88, v89\n"
"v_accvgpr_write_b32 a208, v90\n"
"v_lshlrev_b32 v92, 16, v73\n"
"v_and_b32 v93, 0xffff0000, v73\n"
"v_pk_mul_f32 v[92:93], v[92:93], s[56:57]\n"
"v_cvt_pk_bf16_f32 v94, v92, v93\n"
"v_accvgpr_write_b32 a209, v94\n"
"v_lshlrev_b32 v88, 16, v74\n"
"v_and_b32 v89, 0xffff0000, v74\n"
"v_pk_mul_f32 v[88:89], v[88:89], s[56:57]\n"
"v_cvt_pk_bf16_f32 v90, v88, v89\n"
"v_accvgpr_write_b32 a210, v90\n"
"v_lshlrev_b32 v92, 16, v75\n"
"v_and_b32 v93, 0xffff0000, v75\n"
"v_pk_mul_f32 v[92:93], v[92:93], s[56:57]\n"
"v_cvt_pk_bf16_f32 v94, v92, v93\n"
"v_accvgpr_write_b32 a211, v94\n"
"v_lshlrev_b32 v88, 16, v76\n"
"v_and_b32 v89, 0xffff0000, v76\n"
"v_pk_mul_f32 v[88:89], v[88:89], s[56:57]\n"
"v_cvt_pk_bf16_f32 v90, v88, v89\n"
"v_accvgpr_write_b32 a212, v90\n"
"v_lshlrev_b32 v92, 16, v77\n"
"v_and_b32 v93, 0xffff0000, v77\n"
"v_pk_mul_f32 v[92:93], v[92:93], s[56:57]\n"
"v_cvt_pk_bf16_f32 v94, v92, v93\n"
"v_accvgpr_write_b32 a213, v94\n"
"v_lshlrev_b32 v88, 16, v78\n"
"v_and_b32 v89, 0xffff0000, v78\n"
"v_pk_mul_f32 v[88:89], v[88:89], s[56:57]\n"
"v_cvt_pk_bf16_f32 v90, v88, v89\n"
"v_accvgpr_write_b32 a214, v90\n"
"v_lshlrev_b32 v92, 16, v79\n"
"v_and_b32 v93, 0xffff0000, v79\n"
"v_pk_mul_f32 v[92:93], v[92:93], s[56:57]\n"
"v_cvt_pk_bf16_f32 v94, v92, v93\n"
"v_accvgpr_write_b32 a215, v94\n"
"v_mfma_f32_16x16x32_bf16 v[0:3], v[192:195], a[208:211], 0\n"
"v_mfma_f32_16x16x32_bf16 v[4:7], v[208:211], a[208:211], 0\n"
"v_mfma_f32_16x16x32_bf16 v[8:11], v[224:227], a[208:211], 0\n"
"v_mfma_f32_16x16x32_bf16 v[12:15], v[240:243], a[208:211], 0\n"
"v_lshlrev_b32 v88, 16, v80\n"
"v_and_b32 v89, 0xffff0000, v80\n"
"v_pk_mul_f32 v[88:89], v[88:89], s[56:57]\n"
"v_cvt_pk_bf16_f32 v90, v88, v89\n"
"v_accvgpr_write_b32 a216, v90\n"
"v_lshlrev_b32 v92, 16, v81\n"
"v_and_b32 v93, 0xffff0000, v81\n"
"v_pk_mul_f32 v[92:93], v[92:93], s[56:57]\n"
"v_cvt_pk_bf16_f32 v94, v92, v93\n"
"v_accvgpr_write_b32 a217, v94\n"
"v_lshlrev_b32 v88, 16, v82\n"
"v_and_b32 v89, 0xffff0000, v82\n"
"v_pk_mul_f32 v[88:89], v[88:89], s[56:57]\n"
"v_cvt_pk_bf16_f32 v90, v88, v89\n"
"v_accvgpr_write_b32 a218, v90\n"
"v_lshlrev_b32 v92, 16, v83\n"
"v_and_b32 v93, 0xffff0000, v83\n"
"v_pk_mul_f32 v[92:93], v[92:93], s[56:57]\n"
"v_cvt_pk_bf16_f32 v94, v92, v93\n"
"v_accvgpr_write_b32 a219, v94\n"
"v_mfma_f32_16x16x32_bf16 v[0:3], v[196:199], a[212:215], v[0:3]\n"
"v_mfma_f32_16x16x32_bf16 v[4:7], v[212:215], a[212:215], v[4:7]\n"
"v_mfma_f32_16x16x32_bf16 v[8:11], v[228:231], a[212:215], v[8:11]\n"
"v_mfma_f32_16x16x32_bf16 v[12:15], v[244:247], a[212:215], v[12:15]\n"
"v_lshlrev_b32 v88, 16, v84\n"
"v_and_b32 v89, 0xffff0000, v84\n"
"v_pk_mul_f32 v[88:89], v[88:89], s[56:57]\n"
"v_cvt_pk_bf16_f32 v90, v88, v89\n"
"v_accvgpr_write_b32 a220, v90\n"
"v_lshlrev_b32 v92, 16, v85\n"
"v_and_b32 v93, 0xffff0000, v85\n"
"v_pk_mul_f32 v[92:93], v[92:93], s[56:57]\n"
"v_cvt_pk_bf16_f32 v94, v92, v93\n"
"v_accvgpr_write_b32 a221, v94\n"
"v_lshlrev_b32 v88, 16, v86\n"
"v_and_b32 v89, 0xffff0000, v86\n"
"v_pk_mul_f32 v[88:89], v[88:89], s[56:57]\n"
"v_cvt_pk_bf16_f32 v90, v88, v89\n"
"v_accvgpr_write_b32 a222, v90\n"
"v_lshlrev_b32 v92, 16, v87\n"
"v_and_b32 v93, 0xffff0000, v87\n"
"v_pk_mul_f32 v[92:93], v[92:93], s[56:57]\n"
"v_cvt_pk_bf16_f32 v94, v92, v93\n"
"v_accvgpr_write_b32 a223, v94\n"
"v_mfma_f32_16x16x32_bf16 v[0:3], v[200:203], a[216:219], v[0:3]\n"
"v_mfma_f32_16x16x32_bf16 v[4:7], v[216:219], a[216:219], v[4:7]\n"
"v_mfma_f32_16x16x32_bf16 v[8:11], v[232:235], a[216:219], v[8:11]\n"
"v_mfma_f32_16x16x32_bf16 v[12:15], v[248:251], a[216:219], v[12:15]\n"
"s_add_u32 s63, s53, 0x8000\n"
"s_add_u32 m0, s54, 0x4000\n"
"s_nop 0\n"
"buffer_load_dwordx4 v183, s[40:43], s63 offen offset:0 lds\n"
"buffer_load_dwordx4 v184, s[40:43], s63 offen offset:1024 lds\n"
"buffer_load_dwordx4 v183, s[40:43], s63 offen offset:2048 lds\n"
"buffer_load_dwordx4 v184, s[40:43], s63 offen offset:3072 lds\n"
"v_mfma_f32_16x16x32_bf16 v[0:3], v[204:207], a[220:223], v[0:3]\n"
"v_mfma_f32_16x16x32_bf16 v[4:7], v[220:223], a[220:223], v[4:7]\n"
"v_mfma_f32_16x16x32_bf16 v[8:11], v[236:239], a[220:223], v[8:11]\n"
"v_mfma_f32_16x16x32_bf16 v[12:15], v[252:255], a[220:223], v[12:15]\n"
"s_nop 3\n"
"s_cmp_lg_u32 s66, 0\n"
"s_cbranch_scc1 L_gm3_b_%=\n"
"v_max3_f32 v148, v0, v1, v2\n"
"v_max3_f32 v149, v3, v4, v5\n"
"v_max3_f32 v148, v148, v6, v7\n"
"v_max3_f32 v149, v149, v8, v9\n"
"v_max3_f32 v148, v148, v10, v11\n"
"v_max3_f32 v149, v149, v12, v13\n"
"v_max3_f32 v148, v148, v14, v15\n"
"v_max_f32 v150, v148, v149\n"
"v_max_f32 v151, v148, v149\n"
"s_nop 1\n"
"v_permlane32_swap_b32 v150, v151\n"
"s_nop 1\n"
"v_max_f32 v148, v150, v151\n"
"v_max_f32 v149, v150, v151\n"
"s_nop 1\n"
"v_permlane16_swap_b32 v148, v149\n"
"s_nop 1\n"
"v_max_f32 v148, v148, v149\n"
"v_sub_f32 v148, 0, v148\n"
"L_gm3_b_%=:\n"
"v_mov_b32 v149, v148\n"
"v_mov_b32 v150, v148\n"
"v_mov_b32 v151, v148\n"
"v_pk_add_f32 v[0:1], v[0:1], v[148:149]\n"
"v_pk_add_f32 v[2:3], v[2:3], v[148:149]\n"
"v_pk_add_f32 v[4:5], v[4:5], v[148:149]\n"
"v_pk_add_f32 v[6:7], v[6:7], v[148:149]\n"
"v_pk_add_f32 v[8:9], v[8:9], v[148:149]\n"
"v_pk_add_f32 v[10:11], v[10:11], v[148:149]\n"
"v_pk_add_f32 v[12:13], v[12:13], v[148:149]\n"
"v_pk_add_f32 v[14:15], v[14:15], v[148:149]\n"
"v_exp_f32 v0, v0\n"
"v_exp_f32 v1, v1\n"
"v_exp_f32 v2, v2\n"
"v_exp_f32 v3, v3\n"
"v_exp_f32 v4, v4\n"
"v_exp_f32 v5, v5\n"
"v_exp_f32 v6, v6\n"
"v_exp_f32 v7, v7\n"
"v_cvt_pk_bf16_f32 v40, v0, v1\n"
"v_cvt_pk_bf16_f32 v41, v2, v3\n"
"v_exp_f32 v8, v8\n"
"v_exp_f32 v9, v9\n"
"v_exp_f32 v10, v10\n"
"v_exp_f32 v11, v11\n"
"v_cvt_pk_bf16_f32 v42, v4, v5\n"
"v_cvt_pk_bf16_f32 v43, v6, v7\n"
"s_nop 0\n"
"v_cvt_pk_bf16_f32 v44, v8, v9\n"
"v_cvt_pk_bf16_f32 v45, v10, v11\n"
"s_waitcnt vmcnt(8) lgkmcnt(0)\n"
"s_barrier\n"
"ds_read_b128 v[96:99], v180 offset:16384\n"
"ds_read_b128 v[100:103], v180 offset:16640\n"
"ds_read_b64_tr_b16 v[120:121], v181 offset:0\n"
"ds_read_b64_tr_b16 v[122:123], v181 offset:4096\n"
"ds_read_b64_tr_b16 v[124:125], v181 offset:8192\n"
"ds_read_b64_tr_b16 v[126:127], v181 offset:12288\n"
"ds_read_b128 v[112:115], v180 offset:16896\n"
"ds_read_b128 v[116:119], v180 offset:17152\n"
"s_add_u32 s63, s53, 0xc000\n"
"s_add_u32 m0, s53, 0xc000\n"
"s_nop 0\n"
"buffer_load_dwordx4 v183, s[36:39], s63 offen offset:0 lds\n"
"buffer_load_dwordx4 v184, s[36:39], s63 offen offset:1024 lds\n"
"buffer_load_dwordx4 v183, s[36:39], s63 offen offset:2048 lds\n"
"buffer_load_dwordx4 v184, s[36:39], s63 offen offset:3072 lds\n"
"s_add_u32 s63, s53, 0xc000\n"
"s_add_u32 m0, s54, 0x8000\n"
"s_nop 0\n"
"buffer_load_dwordx4 v183, s[40:43], s63 offen offset:0 lds\n"
"buffer_load_dwordx4 v184, s[40:43], s63 offen offset:1024 lds\n"
"buffer_load_dwordx4 v183, s[40:43], s63 offen offset:2048 lds\n"
"buffer_load_dwordx4 v184, s[40:43], s63 offen offset:3072 lds\n"
"s_add_u32 m0, s54, 0xc000\n"
"s_nop 0\n"
"buffer_load_dwordx4 v183, s[40:43], s52 offen offset:0 lds\n"
"s_waitcnt lgkmcnt(2)\n"
"v_mfma_f32_16x16x32_bf16 v[0:3], v[96:99], a[160:163], v[136:139]\n"
"v_exp_f32 v12, v12\n"
"v_mfma_f32_16x16x32_bf16 v[0:3], v[100:103], a[164:167], v[0:3]\n"
"s_waitcnt lgkmcnt(0)\n"
"v_mfma_f32_16x16x32_bf16 v[0:3], v[112:115], a[168:171], v[0:3]\n"
"buffer_load_dwordx4 v184, s[40:43], s52 offen offset:1024 lds\n"
"v_mfma_f32_16x16x32_bf16 v[0:3], v[116:119], a[172:175], v[0:3]\n"
"v_exp_f32 v13, v13\n"
"v_mfma_f32_16x16x32_bf16 a[0:3], v[120:123], v[16:19], 0\n"
"ds_read_b128 v[104:107], v180 offset:20480\n"
"v_cvt_pk_bf16_f32 v46, v12, v13\n"
"v_mfma_f32_16x16x32_bf16 a[0:3], v[124:127], v[20:23], a[0:3]\n"
"ds_read_b64_tr_b16 v[128:129], v182 offset:0\n"
"v_mfma_f32_16x16x32_bf16 a[32:35], v[120:123], v[24:27], 0\n"
"ds_read_b64_tr_b16 v[130:131], v182 offset:4096\n"
"v_mfma_f32_16x16x32_bf16 a[32:35], v[124:127], v[28:31], a[32:35]\n"
"v_exp_f32 v14, v14\n"
"v_mfma_f32_16x16x32_bf16 v[4:7], v[96:99], a[176:179], v[140:143]\n"
"ds_read_b64_tr_b16 v[132:133], v182 offset:8192\n"
"v_mfma_f32_16x16x32_bf16 v[4:7], v[100:103], a[180:183], v[4:7]\n"
"v_exp_f32 v15, v15\n"
"v_mfma_f32_16x16x32_bf16 v[4:7], v[112:115], a[184:187], v[4:7]\n"
"ds_read_b64_tr_b16 v[134:135], v182 offset:12288\n"
"v_cvt_pk_bf16_f32 v47, v14, v15\n"
"v_mfma_f32_16x16x32_bf16 v[4:7], v[116:119], a[188:191], v[4:7]\n"
"v_exp_f32 v0, v0\n"
"v_mfma_f32_16x16x32_bf16 a[64:67], v[120:123], v[32:35], 0\n"
"ds_read_b128 v[108:111], v180 offset:20736\n"
"v_mfma_f32_16x16x32_bf16 a[64:67], v[124:127], v[36:39], a[64:67]\n"
"v_exp_f32 v1, v1\n"
"v_mfma_f32_16x16x32_bf16 a[96:99], v[120:123], v[40:43], 0\n"
"v_exp_f32 v2, v2\n"
"v_mfma_f32_16x16x32_bf16 a[96:99], v[124:127], v[44:47], a[96:99]\n"
"buffer_load_dwordx4 v183, s[40:43], s52 offen offset:2048 lds\n"
"v_cvt_pk_bf16_f32 v56, v0, v1\n"
"v_mfma_f32_16x16x32_bf16 v[8:11], v[96:99], a[192:195], v[144:147]\n"
"ds_read_b64_tr_b16 v[120:121], v181 offset:256\n"
"v_mfma_f32_16x16x32_bf16 v[8:11], v[100:103], a[196:199], v[8:11]\n"
"v_exp_f32 v3, v3\n"
"v_mfma_f32_16x16x32_bf16 v[8:11], v[112:115], a[200:203], v[8:11]\n"
"ds_read_b64_tr_b16 v[122:123], v181 offset:4352\n"
"v_cvt_pk_bf16_f32 v57, v2, v3\n"
"v_mfma_f32_16x16x32_bf16 v[8:11], v[116:119], a[204:207], v[8:11]\n"
"s_waitcnt lgkmcnt(3)\n"
"v_mfma_f32_16x16x32_bf16 a[4:7], v[128:131], v[16:19], 0\n"
"v_exp_f32 v4, v4\n"
"v_mfma_f32_16x16x32_bf16 a[4:7], v[132:135], v[20:23], a[4:7]\n"
"ds_read_b64_tr_b16 v[124:125], v181 offset:8448\n"
"v_mfma_f32_16x16x32_bf16 a[36:39], v[128:131], v[24:27], 0\n"
"v_exp_f32 v5, v5\n"
"v_mfma_f32_16x16x32_bf16 a[36:39], v[132:135], v[28:31], a[36:39]\n"
"v_exp_f32 v6, v6\n"
"v_mfma_f32_16x16x32_bf16 v[12:15], v[112:115], a[216:219], v[148:151]\n"
"ds_read_b128 v[112:115], v180 offset:20992\n"
"v_cvt_pk_bf16_f32 v64, v4, v5\n"
"v_mfma_f32_16x16x32_bf16 v[12:15], v[116:119], a[220:223], v[12:15]\n"
"ds_read_b128 v[116:119], v180 offset:21248\n"
"v_mfma_f32_16x16x32_bf16 v[12:15], v[96:99], a[208:211], v[12:15]\n"
"ds_read_b64_tr_b16 v[126:127], v181 offset:12544\n"
"v_mfma_f32_16x16x32_bf16 v[12:15], v[100:103], a[212:215], v[12:15]\n"
"v_exp_f32 v7, v7\n"
"v_mfma_f32_16x16x32_bf16 a[68:71], v[128:131], v[32:35], 0\n"
"buffer_load_dwordx4 v184, s[40:43], s52 offen offset:3072 lds\n"
"v_cvt_pk_bf16_f32 v65, v6, v7\n"
"v_mfma_f32_16x16x32_bf16 a[68:71], v[132:135], v[36:39], a[68:71]\n"
"v_exp_f32 v8, v8\n"
"v_mfma_f32_16x16x32_bf16 a[100:103], v[128:131], v[40:43], 0\n"
"v_exp_f32 v9, v9\n"
"v_mfma_f32_16x16x32_bf16 a[100:103], v[132:135], v[44:47], a[100:103]\n"
"v_exp_f32 v10, v10\n"
"v_mfma_f32_16x16x32_bf16 v[156:159], v[176:179], v[16:19], 0\n"
"ds_read_b64_tr_b16 v[128:129], v182 offset:256\n"
"v_cvt_pk_bf16_f32 v72, v8, v9\n"
"v_mfma_f32_16x16x32_bf16 v[156:159], v[176:179], v[20:23], v[156:159]\n"
"s_waitcnt lgkmcnt(7)\n"
"v_mfma_f32_16x16x32_bf16 v[0:3], v[104:107], a[160:163], v[136:139]\n"
"ds_read_b64_tr_b16 v[130:131], v182 offset:4352\n"
"v_mfma_f32_16x16x32_bf16 v[0:3], v[108:111], a[164:167], v[0:3]\n"
"s_waitcnt lgkmcnt(2)\n"
"v_mfma_f32_16x16x32_bf16 v[0:3], v[112:115], a[168:171], v[0:3]\n"
"v_exp_f32 v11, v11\n"
"v_mfma_f32_16x16x32_bf16 v[0:3], v[116:119], a[172:175], v[0:3]\n"
"ds_read_b64_tr_b16 v[132:133], v182 offset:8448\n"
"v_cvt_pk_bf16_f32 v73, v10, v11\n"
"v_mfma_f32_16x16x32_bf16 a[8:11], v[120:123], v[16:19], 0\n"
"ds_read_b128 v[96:99], v180 offset:24576\n"
"v_mfma_f32_16x16x32_bf16 a[8:11], v[124:127], v[20:23], a[8:11]\n"
"v_exp_f32 v12, v12\n"
"v_mfma_f32_16x16x32_bf16 a[40:43], v[120:123], v[24:27], 0\n"
"ds_read_b64_tr_b16 v[134:135], v182 offset:12544\n"
"v_mfma_f32_16x16x32_bf16 a[40:43], v[124:127], v[28:31], a[40:43]\n"
"s_add_u32 m0, s53, 0x0\n"
"v_mfma_f32_16x16x32_bf16 v[4:7], v[104:107], a[176:179], v[140:143]\n"
"v_exp_f32 v13, v13\n"
"v_mfma_f32_16x16x32_bf16 v[4:7], v[108:111], a[180:183], v[4:7]\n"
"v_exp_f32 v14, v14\n"
"v_mfma_f32_16x16x32_bf16 v[4:7], v[112:115], a[184:187], v[4:7]\n"
"v_exp_f32 v15, v15\n"
"v_mfma_f32_16x16x32_bf16 v[4:7], v[116:119], a[188:191], v[4:7]\n"
"v_cvt_pk_bf16_f32 v80, v12, v13\n"
"v_cvt_pk_bf16_f32 v81, v14, v15\n"
"v_mfma_f32_16x16x32_bf16 a[72:75], v[120:123], v[32:35], 0\n"
"ds_read_b128 v[100:103], v180 offset:24832\n"
"v_mfma_f32_16x16x32_bf16 a[72:75], v[124:127], v[36:39], a[72:75]\n"
"v_exp_f32 v0, v0\n"
"v_mfma_f32_16x16x32_bf16 a[104:107], v[120:123], v[40:43], 0\n"
"v_exp_f32 v1, v1\n"
"v_mfma_f32_16x16x32_bf16 a[104:107], v[124:127], v[44:47], a[104:107]\n"
"v_exp_f32 v2, v2\n"
"v_mfma_f32_16x16x32_bf16 v[8:11], v[104:107], a[192:195], v[144:147]\n"
"ds_read_b64_tr_b16 v[120:121], v181 offset:512\n"
"v_cvt_pk_bf16_f32 v58, v0, v1\n"
"v_mfma_f32_16x16x32_bf16 v[8:11], v[108:111], a[196:199], v[8:11]\n"
"v_exp_f32 v3, v3\n"
"v_mfma_f32_16x16x32_bf16 v[8:11], v[112:115], a[200:203], v[8:11]\n"
"ds_read_b64_tr_b16 v[122:123], v181 offset:4608\n"
"v_cvt_pk_bf16_f32 v59, v2, v3\n"
"v_mfma_f32_16x16x32_bf16 v[8:11], v[116:119], a[204:207], v[8:11]\n"
"s_waitcnt lgkmcnt(3)\n"
"v_mfma_f32_16x16x32_bf16 a[12:15], v[128:131], v[16:19], 0\n"
"buffer_load_dwordx4 v183, s[36:39], s52 offen offset:0 lds\n"
"v_mfma_f32_16x16x32_bf16 a[12:15], v[132:135], v[20:23], a[12:15]\n"
"ds_read_b64_tr_b16 v[124:125], v181 offset:8704\n"
"v_mfma_f32_16x16x32_bf16 a[44:47], v[128:131], v[24:27], 0\n"
"v_exp_f32 v4, v4\n"
"v_mfma_f32_16x16x32_bf16 a[44:47], v[132:135], v[28:31], a[44:47]\n"
"v_exp_f32 v5, v5\n"
"v_mfma_f32_16x16x32_bf16 v[12:15], v[112:115], a[216:219], v[148:151]\n"
"ds_read_b128 v[112:115], v180 offset:25088\n"
"v_cvt_pk_bf16_f32 v66, v4, v5\n"
"v_mfma_f32_16x16x32_bf16 v[12:15], v[116:119], a[220:223], v[12:15]\n"
"ds_read_b128 v[116:119], v180 offset:25344\n"
"v_mfma_f32_16x16x32_bf16 v[12:15], v[104:107], a[208:211], v[12:15]\n"
"ds_read_b64_tr_b16 v[126:127], v181 offset:12800\n"
"v_mfma_f32_16x16x32_bf16 v[12:15], v[108:111], a[212:215], v[12:15]\n"
"v_exp_f32 v6, v6\n"
"v_mfma_f32_16x16x32_bf16 a[76:79], v[128:131], v[32:35], 0\n"
"v_exp_f32 v7, v7\n"
"v_mfma_f32_16x16x32_bf16 a[76:79], v[132:135], v[36:39], a[76:79]\n"
"v_exp_f32 v8, v8\n"
"v_mfma_f32_16x16x32_bf16 a[108:111], v[128:131], v[40:43], 0\n"
"v_exp_f32 v9, v9\n"
"v_mfma_f32_16x16x32_bf16 a[108:111], v[132:135], v[44:47], a[108:111]\n"
"v_cvt_pk_bf16_f32 v67, v6, v7\n"
"v_cvt_pk_bf16_f32 v74, v8, v9\n"
"v_mfma_f32_16x16x32_bf16 v[160:163], v[176:179], v[24:27], 0\n"
"ds_read_b64_tr_b16 v[128:129], v182 offset:512\n"
"v_mfma_f32_16x16x32_bf16 v[160:163], v[176:179], v[28:31], v[160:163]\n"
"s_waitcnt lgkmcnt(7)\n"
"v_mfma_f32_16x16x32_bf16 v[0:3], v[96:99], a[160:163], v[136:139]\n"
"ds_read_b64_tr_b16 v[130:131], v182 offset:4608\n"
"v_mfma_f32_16x16x32_bf16 v[0:3], v[100:103], a[164:167], v[0:3]\n"
"s_waitcnt lgkmcnt(2)\n"
"v_mfma_f32_16x16x32_bf16 v[0:3], v[112:115], a[168:171], v[0:3]\n"
"buffer_load_dwordx4 v184, s[36:39], s52 offen offset:1024 lds\n"
"v_mfma_f32_16x16x32_bf16 v[0:3], v[116:119], a[172:175], v[0:3]\n"
"ds_read_b64_tr_b16 v[132:133], v182 offset:8704\n"
"v_mfma_f32_16x16x32_bf16 a[16:19], v[120:123], v[16:19], 0\n"
"ds_read_b128 v[104:107], v180 offset:28672\n"
"v_mfma_f32_16x16x32_bf16 a[16:19], v[124:127], v[20:23], a[16:19]\n"
"v_exp_f32 v10, v10\n"
"v_mfma_f32_16x16x32_bf16 a[48:51], v[120:123], v[24:27], 0\n"
"ds_read_b64_tr_b16 v[134:135], v182 offset:12800\n"
"v_mfma_f32_16x16x32_bf16 a[48:51], v[124:127], v[28:31], a[48:51]\n"
"v_exp_f32 v11, v11\n"
"v_mfma_f32_16x16x32_bf16 v[4:7], v[96:99], a[176:179], v[140:143]\n"
"v_exp_f32 v12, v12\n"
"v_mfma_f32_16x16x32_bf16 v[4:7], v[100:103], a[180:183], v[4:7]\n"
"v_exp_f32 v13, v13\n"
"v_mfma_f32_16x16x32_bf16 v[4:7], v[112:115], a[184:187], v[4:7]\n"
"v_cvt_pk_bf16_f32 v75, v10, v11\n"
"v_cvt_pk_bf16_f32 v82, v12, v13\n"
"v_mfma_f32_16x16x32_bf16 v[4:7], v[116:119], a[188:191], v[4:7]\n"
"v_exp_f32 v14, v14\n"
"v_mfma_f32_16x16x32_bf16 a[80:83], v[120:123], v[32:35], 0\n"
"ds_read_b128 v[108:111], v180 offset:28928\n"
"v_mfma_f32_16x16x32_bf16 a[80:83], v[124:127], v[36:39], a[80:83]\n"
"buffer_load_dwordx4 v183, s[36:39], s52 offen offset:2048 lds\n"
"v_mfma_f32_16x16x32_bf16 a[112:115], v[120:123], v[40:43], 0\n"
"v_exp_f32 v15, v15\n"
"v_mfma_f32_16x16x32_bf16 a[112:115], v[124:127], v[44:47], a[112:115]\n"
"v_exp_f32 v0, v0\n"
"v_mfma_f32_16x16x32_bf16 v[8:11], v[96:99], a[192:195], v[144:147]\n"
"ds_read_b64_tr_b16 v[120:121], v181 offset:768\n"
"v_cvt_pk_bf16_f32 v83, v14, v15\n"
"v_mfma_f32_16x16x32_bf16 v[8:11], v[100:103], a[196:199], v[8:11]\n"
"v_exp_f32 v1, v1\n"
"v_mfma_f32_16x16x32_bf16 v[8:11], v[112:115], a[200:203], v[8:11]\n"
"ds_read_b64_tr_b16 v[122:123], v181 offset:4864\n"
"v_cvt_pk_bf16_f32 v60, v0, v1\n"
"v_mfma_f32_16x16x32_bf16 v[8:11], v[116:119], a[204:207], v[8:11]\n"
"s_waitcnt lgkmcnt(3)\n"
"v_mfma_f32_16x16x32_bf16 a[20:23], v[128:131], v[16:19], 0\n"
"v_exp_f32 v2, v2\n"
"v_mfma_f32_16x16x32_bf16 a[20:23], v[132:135], v[20:23], a[20:23]\n"
"ds_read_b64_tr_b16 v[124:125], v181 offset:8960\n"
"v_mfma_f32_16x16x32_bf16 a[52:55], v[128:131], v[24:27], 0\n"
"v_exp_f32 v3, v3\n"
"v_mfma_f32_16x16x32_bf16 a[52:55], v[132:135], v[28:31], a[52:55]\n"
"v_exp_f32 v4, v4\n"
"v_mfma_f32_16x16x32_bf16 v[12:15], v[112:115], a[216:219], v[148:151]\n"
"ds_read_b128 v[112:115], v180 offset:29184\n"
"v_cvt_pk_bf16_f32 v61, v2, v3\n"
"v_mfma_f32_16x16x32_bf16 v[12:15], v[116:119], a[220:223], v[12:15]\n"
"ds_read_b128 v[116:119], v180 offset:29440\n"
"v_mfma_f32_16x16x32_bf16 v[12:15], v[96:99], a[208:211], v[12:15]\n"
"ds_read_b64_tr_b16 v[126:127], v181 offset:13056\n"
"v_mfma_f32_16x16x32_bf16 v[12:15], v[100:103], a[212:215], v[12:15]\n"
"buffer_load_dwordx4 v184, s[36:39], s52 offen offset:3072 lds\n"
"v_mfma_f32_16x16x32_bf16 a[84:87], v[128:131], v[32:35], 0\n"
"v_exp_f32 v5, v5\n"
"v_mfma_f32_16x16x32_bf16 a[84:87], v[132:135], v[36:39], a[84:87]\n"
"v_exp_f32 v6, v6\n"
"v_mfma_f32_16x16x32_bf16 a[116:119], v[128:131], v[40:43], 0\n"
"v_exp_f32 v7, v7\n"
"v_mfma_f32_16x16x32_bf16 a[116:119], v[132:135], v[44:47], a[116:119]\n"
"v_cvt_pk_bf16_f32 v68, v4, v5\n"
"v_cvt_pk_bf16_f32 v69, v6, v7\n"
"v_mfma_f32_16x16x32_bf16 v[164:167], v[176:179], v[32:35], 0\n"
"ds_read_b64_tr_b16 v[128:129], v182 offset:768\n"
"v_mfma_f32_16x16x32_bf16 v[164:167], v[176:179], v[36:39], v[164:167]\n"
"s_waitcnt lgkmcnt(7)\n"
"v_mfma_f32_16x16x32_bf16 v[0:3], v[104:107], a[160:163], v[136:139]\n"
"ds_read_b64_tr_b16 v[130:131], v182 offset:4864\n"
"v_mfma_f32_16x16x32_bf16 v[0:3], v[108:111], a[164:167], v[0:3]\n"
"s_waitcnt lgkmcnt(2)\n"
"v_mfma_f32_16x16x32_bf16 v[0:3], v[112:115], a[168:171], v[0:3]\n"
"v_exp_f32 v8, v8\n"
"v_mfma_f32_16x16x32_bf16 v[0:3], v[116:119], a[172:175], v[0:3]\n"
"ds_read_b64_tr_b16 v[132:133], v182 offset:8960\n"
"v_mfma_f32_16x16x32_bf16 a[24:27], v[120:123], v[16:19], 0\n"
"s_add_u32 s52, s52, 0x4000\n"
"v_mfma_f32_16x16x32_bf16 a[24:27], v[124:127], v[20:23], a[24:27]\n"
"v_exp_f32 v9, v9\n"
"v_mfma_f32_16x16x32_bf16 a[56:59], v[120:123], v[24:27], 0\n"
"ds_read_b64_tr_b16 v[134:135], v182 offset:13056\n"
"v_cvt_pk_bf16_f32 v76, v8, v9\n"
"v_mfma_f32_16x16x32_bf16 a[56:59], v[124:127], v[28:31], a[56:59]\n"
"v_exp_f32 v10, v10\n"
"v_mfma_f32_16x16x32_bf16 v[4:7], v[104:107], a[176:179], v[140:143]\n"
"v_exp_f32 v11, v11\n"
"v_mfma_f32_16x16x32_bf16 v[4:7], v[108:111], a[180:183], v[4:7]\n"
"v_exp_f32 v12, v12\n"
"v_mfma_f32_16x16x32_bf16 v[4:7], v[112:115], a[184:187], v[4:7]\n"
"v_exp_f32 v13, v13\n"
"v_mfma_f32_16x16x32_bf16 v[4:7], v[116:119], a[188:191], v[4:7]\n"
"v_cvt_pk_bf16_f32 v77, v10, v11\n"
"v_cvt_pk_bf16_f32 v84, v12, v13\n"
"v_mfma_f32_16x16x32_bf16 a[88:91], v[120:123], v[32:35], 0\n"
"v_exp_f32 v14, v14\n"
"v_mfma_f32_16x16x32_bf16 a[88:91], v[124:127], v[36:39], a[88:91]\n"
"v_exp_f32 v15, v15\n"
"v_mfma_f32_16x16x32_bf16 a[120:123], v[120:123], v[40:43], 0\n"
"v_exp_f32 v0, v0\n"
"v_mfma_f32_16x16x32_bf16 a[120:123], v[124:127], v[44:47], a[120:123]\n"
"v_exp_f32 v1, v1\n"
"v_mfma_f32_16x16x32_bf16 v[8:11], v[104:107], a[192:195], v[144:147]\n"
"v_cvt_pk_bf16_f32 v85, v14, v15\n"
"v_cvt_pk_bf16_f32 v62, v0, v1\n"
"v_mfma_f32_16x16x32_bf16 v[8:11], v[108:111], a[196:199], v[8:11]\n"
"v_exp_f32 v2, v2\n"
"v_mfma_f32_16x16x32_bf16 v[8:11], v[112:115], a[200:203], v[8:11]\n"
"v_exp_f32 v3, v3\n"
"v_mfma_f32_16x16x32_bf16 v[8:11], v[116:119], a[204:207], v[8:11]\n"
"s_waitcnt vmcnt(16) lgkmcnt(0)\n"
"v_xor_b32 v180, 0x8000, v180\n"
"s_barrier\n"
"v_mfma_f32_16x16x32_bf16 a[28:31], v[128:131], v[16:19], 0\n"
"ds_read_b128 v[96:99], v180 offset:0\n"
"v_cvt_pk_bf16_f32 v63, v2, v3\n"
"v_mfma_f32_16x16x32_bf16 a[28:31], v[132:135], v[20:23], a[28:31]\n"
"ds_read_b128 v[100:103], v180 offset:256\n"
"v_mfma_f32_16x16x32_bf16 a[60:63], v[128:131], v[24:27], 0\n"
"ds_read_b64_tr_b16 v[120:121], v181 offset:16384\n"
"ds_read_b64_tr_b16 v[124:125], v181 offset:24576\n"
"v_mfma_f32_16x16x32_bf16 a[60:63], v[132:135], v[28:31], a[60:63]\n"
"ds_read_b64_tr_b16 v[122:123], v181 offset:20480\n"
"ds_read_b64_tr_b16 v[126:127], v181 offset:28672\n"
"v_mfma_f32_16x16x32_bf16 v[12:15], v[112:115], a[216:219], v[148:151]\n"
"ds_read_b128 v[112:115], v180 offset:512\n"
"v_mfma_f32_16x16x32_bf16 v[12:15], v[116:119], a[220:223], v[12:15]\n"
"ds_read_b128 v[116:119], v180 offset:768\n"
"v_mfma_f32_16x16x32_bf16 v[12:15], v[104:107], a[208:211], v[12:15]\n"
"s_add_u32 m0, s54, 0x0\n"
"v_mfma_f32_16x16x32_bf16 v[12:15], v[108:111], a[212:215], v[12:15]\n"
"buffer_load_dwordx4 v183, s[40:43], s52 offen offset:0 lds\n"
"v_mfma_f32_16x16x32_bf16 a[92:95], v[128:131], v[32:35], 0\n"
"v_exp_f32 v4, v4\n"
"v_mfma_f32_16x16x32_bf16 a[92:95], v[132:135], v[36:39], a[92:95]\n"
"v_exp_f32 v5, v5\n"
"v_mfma_f32_16x16x32_bf16 a[124:127], v[128:131], v[40:43], 0\n"
"v_exp_f32 v6, v6\n"
"v_mfma_f32_16x16x32_bf16 a[124:127], v[132:135], v[44:47], a[124:127]\n"
"v_exp_f32 v7, v7\n"
"v_mfma_f32_16x16x32_bf16 v[168:171], v[176:179], v[40:43], 0\n"
"v_cvt_pk_bf16_f32 v70, v4, v5\n"
"v_cvt_pk_bf16_f32 v71, v6, v7\n"
"v_mfma_f32_16x16x32_bf16 v[168:171], v[176:179], v[44:47], v[168:171]\n"
"v_exp_f32 v8, v8\n"
"v_exp_f32 v9, v9\n"
"v_exp_f32 v10, v10\n"
"v_exp_f32 v11, v11\n"
"s_nop 0\n"
"v_cvt_pk_bf16_f32 v78, v8, v9\n"
"v_cvt_pk_bf16_f32 v79, v10, v11\n"
"s_waitcnt lgkmcnt(2)\n"
"s_mov_b32 s55, 34\n"
"s_mov_b32 s59, s53\n"
"s_xor_b32 s58, s53, 0x8000\n"
".p2align 6\n"
"L_loop_b_%=:\n"
"v_mfma_f32_16x16x32_bf16 v[0:3], v[96:99], a[160:163], v[136:139]\n"
"v_exp_f32 v12, v12\n"
"v_mfma_f32_16x16x32_bf16 v[0:3], v[100:103], a[164:167], v[0:3]\n"
"s_waitcnt lgkmcnt(0)\n"
"v_mfma_f32_16x16x32_bf16 v[0:3], v[112:115], a[168:171], v[0:3]\n"
"buffer_load_dwordx4 v184, s[40:43], s52 offen offset:1024 lds\n"
"v_mfma_f32_16x16x32_bf16 v[0:3], v[116:119], a[172:175], v[0:3]\n"
"v_exp_f32 v13, v13\n"
"v_mfma_f32_16x16x32_bf16 a[0:3], v[120:123], v[56:59], a[0:3]\n"
"ds_read_b128 v[104:107], v180 offset:4096\n"
"v_cvt_pk_bf16_f32 v86, v12, v13\n"
"v_mfma_f32_16x16x32_bf16 a[0:3], v[124:127], v[60:63], a[0:3]\n"
"ds_read_b64_tr_b16 v[128:129], v182 offset:16384\n"
"v_mfma_f32_16x16x32_bf16 a[32:35], v[120:123], v[64:67], a[32:35]\n"
"ds_read_b64_tr_b16 v[130:131], v182 offset:20480\n"
"v_mfma_f32_16x16x32_bf16 a[32:35], v[124:127], v[68:71], a[32:35]\n"
"v_exp_f32 v14, v14\n"
"v_mfma_f32_16x16x32_bf16 v[4:7], v[96:99], a[176:179], v[140:143]\n"
"ds_read_b64_tr_b16 v[132:133], v182 offset:24576\n"
"v_mfma_f32_16x16x32_bf16 v[4:7], v[100:103], a[180:183], v[4:7]\n"
"v_exp_f32 v15, v15\n"
"v_mfma_f32_16x16x32_bf16 v[4:7], v[112:115], a[184:187], v[4:7]\n"
"ds_read_b64_tr_b16 v[134:135], v182 offset:28672\n"
"v_cvt_pk_bf16_f32 v87, v14, v15\n"
"v_mfma_f32_16x16x32_bf16 v[4:7], v[116:119], a[188:191], v[4:7]\n"
"v_exp_f32 v0, v0\n"
"v_mfma_f32_16x16x32_bf16 a[64:67], v[120:123], v[72:75], a[64:67]\n"
"ds_read_b128 v[108:111], v180 offset:4352\n"
"v_mfma_f32_16x16x32_bf16 a[64:67], v[124:127], v[76:79], a[64:67]\n"
"v_exp_f32 v1, v1\n"
"v_mfma_f32_16x16x32_bf16 a[96:99], v[120:123], v[80:83], a[96:99]\n"
"v_exp_f32 v2, v2\n"
"v_mfma_f32_16x16x32_bf16 a[96:99], v[124:127], v[84:87], a[96:99]\n"
"buffer_load_dwordx4 v183, s[40:43], s52 offen offset:2048 lds\n"
"v_cvt_pk_bf16_f32 v16, v0, v1\n"
"v_mfma_f32_16x16x32_bf16 v[8:11], v[96:99], a[192:195], v[144:147]\n"
"ds_read_b64_tr_b16 v[120:121], v181 offset:16640\n"
"v_mfma_f32_16x16x32_bf16 v[8:11], v[100:103], a[196:199], v[8:11]\n"
"v_exp_f32 v3, v3\n"
"v_mfma_f32_16x16x32_bf16 v[8:11], v[112:115], a[200:203], v[8:11]\n"
"ds_read_b64_tr_b16 v[122:123], v181 offset:20736\n"
"v_cvt_pk_bf16_f32 v17, v2, v3\n"
"v_mfma_f32_16x16x32_bf16 v[8:11], v[116:119], a[204:207], v[8:11]\n"
"s_waitcnt lgkmcnt(3)\n"
"v_mfma_f32_16x16x32_bf16 a[4:7], v[128:131], v[56:59], a[4:7]\n"
"v_exp_f32 v4, v4\n"
"v_mfma_f32_16x16x32_bf16 a[4:7], v[132:135], v[60:63], a[4:7]\n"
"ds_read_b64_tr_b16 v[124:125], v181 offset:24832\n"
"v_mfma_f32_16x16x32_bf16 a[36:39], v[128:131], v[64:67], a[36:39]\n"
"v_exp_f32 v5, v5\n"
"v_mfma_f32_16x16x32_bf16 a[36:39], v[132:135], v[68:71], a[36:39]\n"
"v_exp_f32 v6, v6\n"
"v_mfma_f32_16x16x32_bf16 v[12:15], v[112:115], a[216:219], v[148:151]\n"
"ds_read_b128 v[112:115], v180 offset:4608\n"
"v_cvt_pk_bf16_f32 v24, v4, v5\n"
"v_mfma_f32_16x16x32_bf16 v[12:15], v[116:119], a[220:223], v[12:15]\n"
"ds_read_b128 v[116:119], v180 offset:4864\n"
"v_mfma_f32_16x16x32_bf16 v[12:15], v[96:99], a[208:211], v[12:15]\n"
"ds_read_b64_tr_b16 v[126:127], v181 offset:28928\n"
"v_mfma_f32_16x16x32_bf16 v[12:15], v[100:103], a[212:215], v[12:15]\n"
"v_exp_f32 v7, v7\n"
"v_mfma_f32_16x16x32_bf16 a[68:71], v[128:131], v[72:75], a[68:71]\n"
"buffer_load_dwordx4 v184, s[40:43], s52 offen offset:3072 lds\n"
"v_cvt_pk_bf16_f32 v25, v6, v7\n"
"v_mfma_f32_16x16x32_bf16 a[68:71], v[132:135], v[76:79], a[68:71]\n"
"v_exp_f32 v8, v8\n"
"v_mfma_f32_16x16x32_bf16 a[100:103], v[128:131], v[80:83], a[100:103]\n"
"v_exp_f32 v9, v9\n"
"v_mfma_f32_16x16x32_bf16 a[100:103], v[132:135], v[84:87], a[100:103]\n"
"v_exp_f32 v10, v10\n"
"v_mfma_f32_16x16x32_bf16 v[156:159], v[176:179], v[56:59], v[156:159]\n"
"ds_read_b64_tr_b16 v[128:129], v182 offset:16640\n"
"v_cvt_pk_bf16_f32 v32, v8, v9\n"
"v_mfma_f32_16x16x32_bf16 v[156:159], v[176:179], v[60:63], v[156:159]\n"
"s_waitcnt lgkmcnt(7)\n"
"v_mfma_f32_16x16x32_bf16 v[0:3], v[104:107], a[160:163], v[136:139]\n"
"ds_read_b64_tr_b16 v[130:131], v182 offset:20736\n"
"v_mfma_f32_16x16x32_bf16 v[0:3], v[108:111], a[164:167], v[0:3]\n"
"s_waitcnt lgkmcnt(2)\n"
"v_mfma_f32_16x16x32_bf16 v[0:3], v[112:115], a[168:171], v[0:3]\n"
"v_exp_f32 v11, v11\n"
"v_mfma_f32_16x16x32_bf16 v[0:3], v[116:119], a[172:175], v[0:3]\n"
"ds_read_b64_tr_b16 v[132:133], v182 offset:24832\n"
"v_cvt_pk_bf16_f32 v33, v10, v11\n"
"v_mfma_f32_16x16x32_bf16 a[8:11], v[120:123], v[56:59], a[8:11]\n"
"ds_read_b128 v[96:99], v180 offset:8192\n"
"v_mfma_f32_16x16x32_bf16 a[8:11], v[124:127], v[60:63], a[8:11]\n"
"v_exp_f32 v12, v12\n"
"v_mfma_f32_16x16x32_bf16 a[40:43], v[120:123], v[64:67], a[40:43]\n"
"ds_read_b64_tr_b16 v[134:135], v182 offset:28928\n"
"v_mfma_f32_16x16x32_bf16 a[40:43], v[124:127], v[68:71], a[40:43]\n"
"s_add_u32 m0, s59, 0x4000\n"
"v_mfma_f32_16x16x32_bf16 v[4:7], v[104:107], a[176:179], v[140:143]\n"
"v_exp_f32 v13, v13\n"
"v_mfma_f32_16x16x32_bf16 v[4:7], v[108:111], a[180:183], v[4:7]\n"
"v_exp_f32 v14, v14\n"
"v_mfma_f32_16x16x32_bf16 v[4:7], v[112:115], a[184:187], v[4:7]\n"
"v_exp_f32 v15, v15\n"
"v_mfma_f32_16x16x32_bf16 v[4:7], v[116:119], a[188:191], v[4:7]\n"
"v_cvt_pk_bf16_f32 v40, v12, v13\n"
"v_cvt_pk_bf16_f32 v41, v14, v15\n"
"v_mfma_f32_16x16x32_bf16 a[72:75], v[120:123], v[72:75], a[72:75]\n"
"ds_read_b128 v[100:103], v180 offset:8448\n"
"v_mfma_f32_16x16x32_bf16 a[72:75], v[124:127], v[76:79], a[72:75]\n"
"v_exp_f32 v0, v0\n"
"v_mfma_f32_16x16x32_bf16 a[104:107], v[120:123], v[80:83], a[104:107]\n"
"v_exp_f32 v1, v1\n"
"v_mfma_f32_16x16x32_bf16 a[104:107], v[124:127], v[84:87], a[104:107]\n"
"v_exp_f32 v2, v2\n"
"v_mfma_f32_16x16x32_bf16 v[8:11], v[104:107], a[192:195], v[144:147]\n"
"ds_read_b64_tr_b16 v[120:121], v181 offset:16896\n"
"v_cvt_pk_bf16_f32 v18, v0, v1\n"
"v_mfma_f32_16x16x32_bf16 v[8:11], v[108:111], a[196:199], v[8:11]\n"
"v_exp_f32 v3, v3\n"
"v_mfma_f32_16x16x32_bf16 v[8:11], v[112:115], a[200:203], v[8:11]\n"
"ds_read_b64_tr_b16 v[122:123], v181 offset:20992\n"
"v_cvt_pk_bf16_f32 v19, v2, v3\n"
"v_mfma_f32_16x16x32_bf16 v[8:11], v[116:119], a[204:207], v[8:11]\n"
"s_waitcnt lgkmcnt(3)\n"
"v_mfma_f32_16x16x32_bf16 a[12:15], v[128:131], v[56:59], a[12:15]\n"
"buffer_load_dwordx4 v183, s[36:39], s52 offen offset:0 lds\n"
"v_mfma_f32_16x16x32_bf16 a[12:15], v[132:135], v[60:63], a[12:15]\n"
"ds_read_b64_tr_b16 v[124:125], v181 offset:25088\n"
"v_mfma_f32_16x16x32_bf16 a[44:47], v[128:131], v[64:67], a[44:47]\n"
"v_exp_f32 v4, v4\n"
"v_mfma_f32_16x16x32_bf16 a[44:47], v[132:135], v[68:71], a[44:47]\n"
"v_exp_f32 v5, v5\n"
"v_mfma_f32_16x16x32_bf16 v[12:15], v[112:115], a[216:219], v[148:151]\n"
"ds_read_b128 v[112:115], v180 offset:8704\n"
"v_cvt_pk_bf16_f32 v26, v4, v5\n"
"v_mfma_f32_16x16x32_bf16 v[12:15], v[116:119], a[220:223], v[12:15]\n"
"ds_read_b128 v[116:119], v180 offset:8960\n"
"v_mfma_f32_16x16x32_bf16 v[12:15], v[104:107], a[208:211], v[12:15]\n"
"ds_read_b64_tr_b16 v[126:127], v181 offset:29184\n"
"v_mfma_f32_16x16x32_bf16 v[12:15], v[108:111], a[212:215], v[12:15]\n"
"v_exp_f32 v6, v6\n"
"v_mfma_f32_16x16x32_bf16 a[76:79], v[128:131], v[72:75], a[76:79]\n"
"v_exp_f32 v7, v7\n"
"v_mfma_f32_16x16x32_bf16 a[76:79], v[132:135], v[76:79], a[76:79]\n"
"v_exp_f32 v8, v8\n"
"v_mfma_f32_16x16x32_bf16 a[108:111], v[128:131], v[80:83], a[108:111]\n"
"v_exp_f32 v9, v9\n"
"v_mfma_f32_16x16x32_bf16 a[108:111], v[132:135], v[84:87], a[108:111]\n"
"v_cvt_pk_bf16_f32 v27, v6, v7\n"
"v_cvt_pk_bf16_f32 v34, v8, v9\n"
"v_mfma_f32_16x16x32_bf16 v[160:163], v[176:179], v[64:67], v[160:163]\n"
"ds_read_b64_tr_b16 v[128:129], v182 offset:16896\n"
"v_mfma_f32_16x16x32_bf16 v[160:163], v[176:179], v[68:71], v[160:163]\n"
"s_waitcnt lgkmcnt(7)\n"
"v_mfma_f32_16x16x32_bf16 v[0:3], v[96:99], a[160:163], v[136:139]\n"
"ds_read_b64_tr_b16 v[130:131], v182 offset:20992\n"
"v_mfma_f32_16x16x32_bf16 v[0:3], v[100:103], a[164:167], v[0:3]\n"
"s_waitcnt lgkmcnt(2)\n"
"v_mfma_f32_16x16x32_bf16 v[0:3], v[112:115], a[168:171], v[0:3]\n"
"buffer_load_dwordx4 v184, s[36:39], s52 offen offset:1024 lds\n"
"v_mfma_f32_16x16x32_bf16 v[0:3], v[116:119], a[172:175], v[0:3]\n"
"ds_read_b64_tr_b16 v[132:133], v182 offset:25088\n"
"v_mfma_f32_16x16x32_bf16 a[16:19], v[120:123], v[56:59], a[16:19]\n"
"ds_read_b128 v[104:107], v180 offset:12288\n"
"v_mfma_f32_16x16x32_bf16 a[16:19], v[124:127], v[60:63], a[16:19]\n"
"v_exp_f32 v10, v10\n"
"v_mfma_f32_16x16x32_bf16 a[48:51], v[120:123], v[64:67], a[48:51]\n"
"ds_read_b64_tr_b16 v[134:135], v182 offset:29184\n"
"v_mfma_f32_16x16x32_bf16 a[48:51], v[124:127], v[68:71], a[48:51]\n"
"v_exp_f32 v11, v11\n"
"v_mfma_f32_16x16x32_bf16 v[4:7], v[96:99], a[176:179], v[140:143]\n"
"v_exp_f32 v12, v12\n"
"v_mfma_f32_16x16x32_bf16 v[4:7], v[100:103], a[180:183], v[4:7]\n"
"v_exp_f32 v13, v13\n"
"v_mfma_f32_16x16x32_bf16 v[4:7], v[112:115], a[184:187], v[4:7]\n"
"v_cvt_pk_bf16_f32 v35, v10, v11\n"
"v_cvt_pk_bf16_f32 v42, v12, v13\n"
"v_mfma_f32_16x16x32_bf16 v[4:7], v[116:119], a[188:191], v[4:7]\n"
"v_exp_f32 v14, v14\n"
"v_mfma_f32_16x16x32_bf16 a[80:83], v[120:123], v[72:75], a[80:83]\n"
"ds_read_b128 v[108:111], v180 offset:12544\n"
"v_mfma_f32_16x16x32_bf16 a[80:83], v[124:127], v[76:79], a[80:83]\n"
"buffer_load_dwordx4 v183, s[36:39], s52 offen offset:2048 lds\n"
"v_mfma_f32_16x16x32_bf16 a[112:115], v[120:123], v[80:83], a[112:115]\n"
"v_exp_f32 v15, v15\n"
"v_mfma_f32_16x16x32_bf16 a[112:115], v[124:127], v[84:87], a[112:115]\n"
"v_exp_f32 v0, v0\n"
"v_mfma_f32_16x16x32_bf16 v[8:11], v[96:99], a[192:195], v[144:147]\n"
"ds_read_b64_tr_b16 v[120:121], v181 offset:17152\n"
"v_cvt_pk_bf16_f32 v43, v14, v15\n"
"v_mfma_f32_16x16x32_bf16 v[8:11], v[100:103], a[196:199], v[8:11]\n"
"v_exp_f32 v1, v1\n"
"v_mfma_f32_16x16x32_bf16 v[8:11], v[112:115], a[200:203], v[8:11]\n"
"ds_read_b64_tr_b16 v[122:123], v181 offset:21248\n"
"v_cvt_pk_bf16_f32 v20, v0, v1\n"
"v_mfma_f32_16x16x32_bf16 v[8:11], v[116:119], a[204:207], v[8:11]\n"
"s_waitcnt lgkmcnt(3)\n"
"v_mfma_f32_16x16x32_bf16 a[20:23], v[128:131], v[56:59], a[20:23]\n"
"v_exp_f32 v2, v2\n"
"v_mfma_f32_16x16x32_bf16 a[20:23], v[132:135], v[60:63], a[20:23]\n"
"ds_read_b64_tr_b16 v[124:125], v181 offset:25344\n"
"v_mfma_f32_16x16x32_bf16 a[52:55], v[128:131], v[64:67], a[52:55]\n"
"v_exp_f32 v3, v3\n"
"v_mfma_f32_16x16x32_bf16 a[52:55], v[132:135], v[68:71], a[52:55]\n"
"v_exp_f32 v4, v4\n"
"v_mfma_f32_16x16x32_bf16 v[12:15], v[112:115], a[216:219], v[148:151]\n"
"ds_read_b128 v[112:115], v180 offset:12800\n"
"v_cvt_pk_bf16_f32 v21, v2, v3\n"
"v_mfma_f32_16x16x32_bf16 v[12:15], v[116:119], a[220:223], v[12:15]\n"
"ds_read_b128 v[116:119], v180 offset:13056\n"
"v_mfma_f32_16x16x32_bf16 v[12:15], v[96:99], a[208:211], v[12:15]\n"
"ds_read_b64_tr_b16 v[126:127], v181 offset:29440\n"
"v_mfma_f32_16x16x32_bf16 v[12:15], v[100:103], a[212:215], v[12:15]\n"
"buffer_load_dwordx4 v184, s[36:39], s52 offen offset:3072 lds\n"
"v_mfma_f32_16x16x32_bf16 a[84:87], v[128:131], v[72:75], a[84:87]\n"
"v_exp_f32 v5, v5\n"
"v_mfma_f32_16x16x32_bf16 a[84:87], v[132:135], v[76:79], a[84:87]\n"
"v_exp_f32 v6, v6\n"
"v_mfma_f32_16x16x32_bf16 a[116:119], v[128:131], v[80:83], a[116:119]\n"
"v_exp_f32 v7, v7\n"
"v_mfma_f32_16x16x32_bf16 a[116:119], v[132:135], v[84:87], a[116:119]\n"
"v_cvt_pk_bf16_f32 v28, v4, v5\n"
"v_cvt_pk_bf16_f32 v29, v6, v7\n"
"v_mfma_f32_16x16x32_bf16 v[164:167], v[176:179], v[72:75], v[164:167]\n"
"ds_read_b64_tr_b16 v[128:129], v182 offset:17152\n"
"v_mfma_f32_16x16x32_bf16 v[164:167], v[176:179], v[76:79], v[164:167]\n"
"s_waitcnt lgkmcnt(7)\n"
"v_mfma_f32_16x16x32_bf16 v[0:3], v[104:107], a[160:163], v[136:139]\n"
"ds_read_b64_tr_b16 v[130:131], v182 offset:21248\n"
"v_mfma_f32_16x16x32_bf16 v[0:3], v[108:111], a[164:167], v[0:3]\n"
"s_waitcnt lgkmcnt(2)\n"
"v_mfma_f32_16x16x32_bf16 v[0:3], v[112:115], a[168:171], v[0:3]\n"
"v_exp_f32 v8, v8\n"
"v_mfma_f32_16x16x32_bf16 v[0:3], v[116:119], a[172:175], v[0:3]\n"
"ds_read_b64_tr_b16 v[132:133], v182 offset:25344\n"
"v_mfma_f32_16x16x32_bf16 a[24:27], v[120:123], v[56:59], a[24:27]\n"
"s_add_u32 s52, s52, 0x4000\n"
"v_mfma_f32_16x16x32_bf16 a[24:27], v[124:127], v[60:63], a[24:27]\n"
"v_exp_f32 v9, v9\n"
"v_mfma_f32_16x16x32_bf16 a[56:59], v[120:123], v[64:67], a[56:59]\n"
"ds_read_b64_tr_b16 v[134:135], v182 offset:29440\n"
"v_cvt_pk_bf16_f32 v36, v8, v9\n"
"v_mfma_f32_16x16x32_bf16 a[56:59], v[124:127], v[68:71], a[56:59]\n"
"v_exp_f32 v10, v10\n"
"v_mfma_f32_16x16x32_bf16 v[4:7], v[104:107], a[176:179], v[140:143]\n"
"v_exp_f32 v11, v11\n"
"v_mfma_f32_16x16x32_bf16 v[4:7], v[108:111], a[180:183], v[4:7]\n"
"v_exp_f32 v12, v12\n"
"v_mfma_f32_16x16x32_bf16 v[4:7], v[112:115], a[184:187], v[4:7]\n"
"v_exp_f32 v13, v13\n"
"v_mfma_f32_16x16x32_bf16 v[4:7], v[116:119], a[188:191], v[4:7]\n"
"v_cvt_pk_bf16_f32 v37, v10, v11\n"
"v_cvt_pk_bf16_f32 v44, v12, v13\n"
"v_mfma_f32_16x16x32_bf16 a[88:91], v[120:123], v[72:75], a[88:91]\n"
"v_exp_f32 v14, v14\n"
"v_mfma_f32_16x16x32_bf16 a[88:91], v[124:127], v[76:79], a[88:91]\n"
"v_exp_f32 v15, v15\n"
"v_mfma_f32_16x16x32_bf16 a[120:123], v[120:123], v[80:83], a[120:123]\n"
"v_exp_f32 v0, v0\n"
"v_mfma_f32_16x16x32_bf16 a[120:123], v[124:127], v[84:87], a[120:123]\n"
"v_exp_f32 v1, v1\n"
"v_mfma_f32_16x16x32_bf16 v[8:11], v[104:107], a[192:195], v[144:147]\n"
"v_cvt_pk_bf16_f32 v45, v14, v15\n"
"v_cvt_pk_bf16_f32 v22, v0, v1\n"
"v_mfma_f32_16x16x32_bf16 v[8:11], v[108:111], a[196:199], v[8:11]\n"
"v_exp_f32 v2, v2\n"
"v_mfma_f32_16x16x32_bf16 v[8:11], v[112:115], a[200:203], v[8:11]\n"
"v_exp_f32 v3, v3\n"
"v_mfma_f32_16x16x32_bf16 v[8:11], v[116:119], a[204:207], v[8:11]\n"
"s_waitcnt vmcnt(16) lgkmcnt(0)\n"
"v_xor_b32 v181, 0x8000, v181\n"
"v_xor_b32 v182, 0x8000, v182\n"
"s_barrier\n"
"v_mfma_f32_16x16x32_bf16 a[28:31], v[128:131], v[56:59], a[28:31]\n"
"ds_read_b128 v[96:99], v180 offset:16384\n"
"v_cvt_pk_bf16_f32 v23, v2, v3\n"
"v_mfma_f32_16x16x32_bf16 a[28:31], v[132:135], v[60:63], a[28:31]\n"
"ds_read_b128 v[100:103], v180 offset:16640\n"
"v_mfma_f32_16x16x32_bf16 a[60:63], v[128:131], v[64:67], a[60:63]\n"
"ds_read_b64_tr_b16 v[120:121], v181 offset:0\n"
"ds_read_b64_tr_b16 v[124:125], v181 offset:8192\n"
"v_mfma_f32_16x16x32_bf16 a[60:63], v[132:135], v[68:71], a[60:63]\n"
"ds_read_b64_tr_b16 v[122:123], v181 offset:4096\n"
"ds_read_b64_tr_b16 v[126:127], v181 offset:12288\n"
"v_mfma_f32_16x16x32_bf16 v[12:15], v[112:115], a[216:219], v[148:151]\n"
"ds_read_b128 v[112:115], v180 offset:16896\n"
"v_mfma_f32_16x16x32_bf16 v[12:15], v[116:119], a[220:223], v[12:15]\n"
"ds_read_b128 v[116:119], v180 offset:17152\n"
"v_mfma_f32_16x16x32_bf16 v[12:15], v[104:107], a[208:211], v[12:15]\n"
"s_add_u32 m0, s59, 0x14000\n"
"v_mfma_f32_16x16x32_bf16 v[12:15], v[108:111], a[212:215], v[12:15]\n"
"buffer_load_dwordx4 v183, s[40:43], s52 offen offset:0 lds\n"
"v_mfma_f32_16x16x32_bf16 a[92:95], v[128:131], v[72:75], a[92:95]\n"
"v_exp_f32 v4, v4\n"
"v_mfma_f32_16x16x32_bf16 a[92:95], v[132:135], v[76:79], a[92:95]\n"
"v_exp_f32 v5, v5\n"
"v_mfma_f32_16x16x32_bf16 a[124:127], v[128:131], v[80:83], a[124:127]\n"
"v_exp_f32 v6, v6\n"
"v_mfma_f32_16x16x32_bf16 a[124:127], v[132:135], v[84:87], a[124:127]\n"
"v_exp_f32 v7, v7\n"
"v_mfma_f32_16x16x32_bf16 v[168:171], v[176:179], v[80:83], v[168:171]\n"
"v_cvt_pk_bf16_f32 v30, v4, v5\n"
"v_cvt_pk_bf16_f32 v31, v6, v7\n"
"v_mfma_f32_16x16x32_bf16 v[168:171], v[176:179], v[84:87], v[168:171]\n"
"v_exp_f32 v8, v8\n"
"v_exp_f32 v9, v9\n"
"v_exp_f32 v10, v10\n"
"v_exp_f32 v11, v11\n"
"s_nop 0\n"
"v_cvt_pk_bf16_f32 v38, v8, v9\n"
"v_cvt_pk_bf16_f32 v39, v10, v11\n"
"s_waitcnt lgkmcnt(2)\n"
"v_mfma_f32_16x16x32_bf16 v[0:3], v[96:99], a[160:163], v[136:139]\n"
"v_exp_f32 v12, v12\n"
"v_mfma_f32_16x16x32_bf16 v[0:3], v[100:103], a[164:167], v[0:3]\n"
"s_waitcnt lgkmcnt(0)\n"
"v_mfma_f32_16x16x32_bf16 v[0:3], v[112:115], a[168:171], v[0:3]\n"
"buffer_load_dwordx4 v184, s[40:43], s52 offen offset:1024 lds\n"
"v_mfma_f32_16x16x32_bf16 v[0:3], v[116:119], a[172:175], v[0:3]\n"
"v_exp_f32 v13, v13\n"
"v_mfma_f32_16x16x32_bf16 a[0:3], v[120:123], v[16:19], a[0:3]\n"
"ds_read_b128 v[104:107], v180 offset:20480\n"
"v_cvt_pk_bf16_f32 v46, v12, v13\n"
"v_mfma_f32_16x16x32_bf16 a[0:3], v[124:127], v[20:23], a[0:3]\n"
"ds_read_b64_tr_b16 v[128:129], v182 offset:0\n"
"v_mfma_f32_16x16x32_bf16 a[32:35], v[120:123], v[24:27], a[32:35]\n"
"ds_read_b64_tr_b16 v[130:131], v182 offset:4096\n"
"v_mfma_f32_16x16x32_bf16 a[32:35], v[124:127], v[28:31], a[32:35]\n"
"v_exp_f32 v14, v14\n"
"v_mfma_f32_16x16x32_bf16 v[4:7], v[96:99], a[176:179], v[140:143]\n"
"ds_read_b64_tr_b16 v[132:133], v182 offset:8192\n"
"v_mfma_f32_16x16x32_bf16 v[4:7], v[100:103], a[180:183], v[4:7]\n"
"v_exp_f32 v15, v15\n"
"v_mfma_f32_16x16x32_bf16 v[4:7], v[112:115], a[184:187], v[4:7]\n"
"ds_read_b64_tr_b16 v[134:135], v182 offset:12288\n"
"v_cvt_pk_bf16_f32 v47, v14, v15\n"
"v_mfma_f32_16x16x32_bf16 v[4:7], v[116:119], a[188:191], v[4:7]\n"
"v_exp_f32 v0, v0\n"
"v_mfma_f32_16x16x32_bf16 a[64:67], v[120:123], v[32:35], a[64:67]\n"
"ds_read_b128 v[108:111], v180 offset:20736\n"
"v_mfma_f32_16x16x32_bf16 a[64:67], v[124:127], v[36:39], a[64:67]\n"
"v_exp_f32 v1, v1\n"
"v_mfma_f32_16x16x32_bf16 a[96:99], v[120:123], v[40:43], a[96:99]\n"
"v_exp_f32 v2, v2\n"
"v_mfma_f32_16x16x32_bf16 a[96:99], v[124:127], v[44:47], a[96:99]\n"
"buffer_load_dwordx4 v183, s[40:43], s52 offen offset:2048 lds\n"
"v_cvt_pk_bf16_f32 v56, v0, v1\n"
"v_mfma_f32_16x16x32_bf16 v[8:11], v[96:99], a[192:195], v[144:147]\n"
"ds_read_b64_tr_b16 v[120:121], v181 offset:256\n"
"v_mfma_f32_16x16x32_bf16 v[8:11], v[100:103], a[196:199], v[8:11]\n"
"v_exp_f32 v3, v3\n"
"v_mfma_f32_16x16x32_bf16 v[8:11], v[112:115], a[200:203], v[8:11]\n"
"ds_read_b64_tr_b16 v[122:123], v181 offset:4352\n"
"v_cvt_pk_bf16_f32 v57, v2, v3\n"
"v_mfma_f32_16x16x32_bf16 v[8:11], v[116:119], a[204:207], v[8:11]\n"
"s_waitcnt lgkmcnt(3)\n"
"v_mfma_f32_16x16x32_bf16 a[4:7], v[128:131], v[16:19], a[4:7]\n"
"v_exp_f32 v4, v4\n"
"v_mfma_f32_16x16x32_bf16 a[4:7], v[132:135], v[20:23], a[4:7]\n"
"ds_read_b64_tr_b16 v[124:125], v181 offset:8448\n"
"v_mfma_f32_16x16x32_bf16 a[36:39], v[128:131], v[24:27], a[36:39]\n"
"v_exp_f32 v5, v5\n"
"v_mfma_f32_16x16x32_bf16 a[36:39], v[132:135], v[28:31], a[36:39]\n"
"v_exp_f32 v6, v6\n"
"v_mfma_f32_16x16x32_bf16 v[12:15], v[112:115], a[216:219], v[148:151]\n"
"ds_read_b128 v[112:115], v180 offset:20992\n"
"v_cvt_pk_bf16_f32 v64, v4, v5\n"
"v_mfma_f32_16x16x32_bf16 v[12:15], v[116:119], a[220:223], v[12:15]\n"
"ds_read_b128 v[116:119], v180 offset:21248\n"
"v_mfma_f32_16x16x32_bf16 v[12:15], v[96:99], a[208:211], v[12:15]\n"
"ds_read_b64_tr_b16 v[126:127], v181 offset:12544\n"
"v_mfma_f32_16x16x32_bf16 v[12:15], v[100:103], a[212:215], v[12:15]\n"
"v_exp_f32 v7, v7\n"
"v_mfma_f32_16x16x32_bf16 a[68:71], v[128:131], v[32:35], a[68:71]\n"
"buffer_load_dwordx4 v184, s[40:43], s52 offen offset:3072 lds\n"
"v_cvt_pk_bf16_f32 v65, v6, v7\n"
"v_mfma_f32_16x16x32_bf16 a[68:71], v[132:135], v[36:39], a[68:71]\n"
"v_exp_f32 v8, v8\n"
"v_mfma_f32_16x16x32_bf16 a[100:103], v[128:131], v[40:43], a[100:103]\n"
"v_exp_f32 v9, v9\n"
"v_mfma_f32_16x16x32_bf16 a[100:103], v[132:135], v[44:47], a[100:103]\n"
"v_exp_f32 v10, v10\n"
"v_mfma_f32_16x16x32_bf16 v[156:159], v[176:179], v[16:19], v[156:159]\n"
"ds_read_b64_tr_b16 v[128:129], v182 offset:256\n"
"v_cvt_pk_bf16_f32 v72, v8, v9\n"
"v_mfma_f32_16x16x32_bf16 v[156:159], v[176:179], v[20:23], v[156:159]\n"
"s_waitcnt lgkmcnt(7)\n"
"v_mfma_f32_16x16x32_bf16 v[0:3], v[104:107], a[160:163], v[136:139]\n"
"ds_read_b64_tr_b16 v[130:131], v182 offset:4352\n"
"v_mfma_f32_16x16x32_bf16 v[0:3], v[108:111], a[164:167], v[0:3]\n"
"s_waitcnt lgkmcnt(2)\n"
"v_mfma_f32_16x16x32_bf16 v[0:3], v[112:115], a[168:171], v[0:3]\n"
"v_exp_f32 v11, v11\n"
"v_mfma_f32_16x16x32_bf16 v[0:3], v[116:119], a[172:175], v[0:3]\n"
"ds_read_b64_tr_b16 v[132:133], v182 offset:8448\n"
"v_cvt_pk_bf16_f32 v73, v10, v11\n"
"v_mfma_f32_16x16x32_bf16 a[8:11], v[120:123], v[16:19], a[8:11]\n"
"ds_read_b128 v[96:99], v180 offset:24576\n"
"v_mfma_f32_16x16x32_bf16 a[8:11], v[124:127], v[20:23], a[8:11]\n"
"v_exp_f32 v12, v12\n"
"v_mfma_f32_16x16x32_bf16 a[40:43], v[120:123], v[24:27], a[40:43]\n"
"ds_read_b64_tr_b16 v[134:135], v182 offset:12544\n"
"v_mfma_f32_16x16x32_bf16 a[40:43], v[124:127], v[28:31], a[40:43]\n"
"s_add_u32 m0, s58, 0x0\n"
"v_mfma_f32_16x16x32_bf16 v[4:7], v[104:107], a[176:179], v[140:143]\n"
"v_exp_f32 v13, v13\n"
"v_mfma_f32_16x16x32_bf16 v[4:7], v[108:111], a[180:183], v[4:7]\n"
"v_exp_f32 v14, v14\n"
"v_mfma_f32_16x16x32_bf16 v[4:7], v[112:115], a[184:187], v[4:7]\n"
"v_exp_f32 v15, v15\n"
"v_mfma_f32_16x16x32_bf16 v[4:7], v[116:119], a[188:191], v[4:7]\n"
"v_cvt_pk_bf16_f32 v80, v12, v13\n"
"v_cvt_pk_bf16_f32 v81, v14, v15\n"
"v_mfma_f32_16x16x32_bf16 a[72:75], v[120:123], v[32:35], a[72:75]\n"
"ds_read_b128 v[100:103], v180 offset:24832\n"
"v_mfma_f32_16x16x32_bf16 a[72:75], v[124:127], v[36:39], a[72:75]\n"
"v_exp_f32 v0, v0\n"
"v_mfma_f32_16x16x32_bf16 a[104:107], v[120:123], v[40:43], a[104:107]\n"
"v_exp_f32 v1, v1\n"
"v_mfma_f32_16x16x32_bf16 a[104:107], v[124:127], v[44:47], a[104:107]\n"
"v_exp_f32 v2, v2\n"
"v_mfma_f32_16x16x32_bf16 v[8:11], v[104:107], a[192:195], v[144:147]\n"
"ds_read_b64_tr_b16 v[120:121], v181 offset:512\n"
"v_cvt_pk_bf16_f32 v58, v0, v1\n"
"v_mfma_f32_16x16x32_bf16 v[8:11], v[108:111], a[196:199], v[8:11]\n"
"v_exp_f32 v3, v3\n"
"v_mfma_f32_16x16x32_bf16 v[8:11], v[112:115], a[200:203], v[8:11]\n"
"ds_read_b64_tr_b16 v[122:123], v181 offset:4608\n"
"v_cvt_pk_bf16_f32 v59, v2, v3\n"
"v_mfma_f32_16x16x32_bf16 v[8:11], v[116:119], a[204:207], v[8:11]\n"
"s_waitcnt lgkmcnt(3)\n"
"v_mfma_f32_16x16x32_bf16 a[12:15], v[128:131], v[16:19], a[12:15]\n"
"buffer_load_dwordx4 v183, s[36:39], s52 offen offset:0 lds\n"
"v_mfma_f32_16x16x32_bf16 a[12:15], v[132:135], v[20:23], a[12:15]\n"
"ds_read_b64_tr_b16 v[124:125], v181 offset:8704\n"
"v_mfma_f32_16x16x32_bf16 a[44:47], v[128:131], v[24:27], a[44:47]\n"
"v_exp_f32 v4, v4\n"
"v_mfma_f32_16x16x32_bf16 a[44:47], v[132:135], v[28:31], a[44:47]\n"
"v_exp_f32 v5, v5\n"
"v_mfma_f32_16x16x32_bf16 v[12:15], v[112:115], a[216:219], v[148:151]\n"
"ds_read_b128 v[112:115], v180 offset:25088\n"
"v_cvt_pk_bf16_f32 v66, v4, v5\n"
"v_mfma_f32_16x16x32_bf16 v[12:15], v[116:119], a[220:223], v[12:15]\n"
"ds_read_b128 v[116:119], v180 offset:25344\n"
"v_mfma_f32_16x16x32_bf16 v[12:15], v[104:107], a[208:211], v[12:15]\n"
"ds_read_b64_tr_b16 v[126:127], v181 offset:12800\n"
"v_mfma_f32_16x16x32_bf16 v[12:15], v[108:111], a[212:215], v[12:15]\n"
"v_exp_f32 v6, v6\n"
"v_mfma_f32_16x16x32_bf16 a[76:79], v[128:131], v[32:35], a[76:79]\n"
"v_exp_f32 v7, v7\n"
"v_mfma_f32_16x16x32_bf16 a[76:79], v[132:135], v[36:39], a[76:79]\n"
"v_exp_f32 v8, v8\n"
"v_mfma_f32_16x16x32_bf16 a[108:111], v[128:131], v[40:43], a[108:111]\n"
"v_exp_f32 v9, v9\n"
"v_mfma_f32_16x16x32_bf16 a[108:111], v[132:135], v[44:47], a[108:111]\n"
"v_cvt_pk_bf16_f32 v67, v6, v7\n"
"v_cvt_pk_bf16_f32 v74, v8, v9\n"
"v_mfma_f32_16x16x32_bf16 v[160:163], v[176:179], v[24:27], v[160:163]\n"
"ds_read_b64_tr_b16 v[128:129], v182 offset:512\n"
"v_mfma_f32_16x16x32_bf16 v[160:163], v[176:179], v[28:31], v[160:163]\n"
"s_waitcnt lgkmcnt(7)\n"
"v_mfma_f32_16x16x32_bf16 v[0:3], v[96:99], a[160:163], v[136:139]\n"
"ds_read_b64_tr_b16 v[130:131], v182 offset:4608\n"
"v_mfma_f32_16x16x32_bf16 v[0:3], v[100:103], a[164:167], v[0:3]\n"
"s_waitcnt lgkmcnt(2)\n"
"v_mfma_f32_16x16x32_bf16 v[0:3], v[112:115], a[168:171], v[0:3]\n"
"buffer_load_dwordx4 v184, s[36:39], s52 offen offset:1024 lds\n"
"v_mfma_f32_16x16x32_bf16 v[0:3], v[116:119], a[172:175], v[0:3]\n"
"ds_read_b64_tr_b16 v[132:133], v182 offset:8704\n"
"v_mfma_f32_16x16x32_bf16 a[16:19], v[120:123], v[16:19], a[16:19]\n"
"ds_read_b128 v[104:107], v180 offset:28672\n"
"v_mfma_f32_16x16x32_bf16 a[16:19], v[124:127], v[20:23], a[16:19]\n"
"v_exp_f32 v10, v10\n"
"v_mfma_f32_16x16x32_bf16 a[48:51], v[120:123], v[24:27], a[48:51]\n"
"ds_read_b64_tr_b16 v[134:135], v182 offset:12800\n"
"v_mfma_f32_16x16x32_bf16 a[48:51], v[124:127], v[28:31], a[48:51]\n"
"v_exp_f32 v11, v11\n"
"v_mfma_f32_16x16x32_bf16 v[4:7], v[96:99], a[176:179], v[140:143]\n"
"v_exp_f32 v12, v12\n"
"v_mfma_f32_16x16x32_bf16 v[4:7], v[100:103], a[180:183], v[4:7]\n"
"v_exp_f32 v13, v13\n"
"v_mfma_f32_16x16x32_bf16 v[4:7], v[112:115], a[184:187], v[4:7]\n"
"v_cvt_pk_bf16_f32 v75, v10, v11\n"
"v_cvt_pk_bf16_f32 v82, v12, v13\n"
"v_mfma_f32_16x16x32_bf16 v[4:7], v[116:119], a[188:191], v[4:7]\n"
"v_exp_f32 v14, v14\n"
"v_mfma_f32_16x16x32_bf16 a[80:83], v[120:123], v[32:35], a[80:83]\n"
"ds_read_b128 v[108:111], v180 offset:28928\n"
"v_mfma_f32_16x16x32_bf16 a[80:83], v[124:127], v[36:39], a[80:83]\n"
"buffer_load_dwordx4 v183, s[36:39], s52 offen offset:2048 lds\n"
"v_mfma_f32_16x16x32_bf16 a[112:115], v[120:123], v[40:43], a[112:115]\n"
"v_exp_f32 v15, v15\n"
"v_mfma_f32_16x16x32_bf16 a[112:115], v[124:127], v[44:47], a[112:115]\n"
"v_exp_f32 v0, v0\n"
"v_mfma_f32_16x16x32_bf16 v[8:11], v[96:99], a[192:195], v[144:147]\n"
"ds_read_b64_tr_b16 v[120:121], v181 offset:768\n"
"v_cvt_pk_bf16_f32 v83, v14, v15\n"
"v_mfma_f32_16x16x32_bf16 v[8:11], v[100:103], a[196:199], v[8:11]\n"
"v_exp_f32 v1, v1\n"
"v_mfma_f32_16x16x32_bf16 v[8:11], v[112:115], a[200:203], v[8:11]\n"
"ds_read_b64_tr_b16 v[122:123], v181 offset:4864\n"
"v_cvt_pk_bf16_f32 v60, v0, v1\n"
"v_mfma_f32_16x16x32_bf16 v[8:11], v[116:119], a[204:207], v[8:11]\n"
"s_waitcnt lgkmcnt(3)\n"
"v_mfma_f32_16x16x32_bf16 a[20:23], v[128:131], v[16:19], a[20:23]\n"
"v_exp_f32 v2, v2\n"
"v_mfma_f32_16x16x32_bf16 a[20:23], v[132:135], v[20:23], a[20:23]\n"
"ds_read_b64_tr_b16 v[124:125], v181 offset:8960\n"
"v_mfma_f32_16x16x32_bf16 a[52:55], v[128:131], v[24:27], a[52:55]\n"
"v_exp_f32 v3, v3\n"
"v_mfma_f32_16x16x32_bf16 a[52:55], v[132:135], v[28:31], a[52:55]\n"
"v_exp_f32 v4, v4\n"
"v_mfma_f32_16x16x32_bf16 v[12:15], v[112:115], a[216:219], v[148:151]\n"
"ds_read_b128 v[112:115], v180 offset:29184\n"
"v_cvt_pk_bf16_f32 v61, v2, v3\n"
"v_mfma_f32_16x16x32_bf16 v[12:15], v[116:119], a[220:223], v[12:15]\n"
"ds_read_b128 v[116:119], v180 offset:29440\n"
"v_mfma_f32_16x16x32_bf16 v[12:15], v[96:99], a[208:211], v[12:15]\n"
"ds_read_b64_tr_b16 v[126:127], v181 offset:13056\n"
"v_mfma_f32_16x16x32_bf16 v[12:15], v[100:103], a[212:215], v[12:15]\n"
"buffer_load_dwordx4 v184, s[36:39], s52 offen offset:3072 lds\n"
"v_mfma_f32_16x16x32_bf16 a[84:87], v[128:131], v[32:35], a[84:87]\n"
"v_exp_f32 v5, v5\n"
"v_mfma_f32_16x16x32_bf16 a[84:87], v[132:135], v[36:39], a[84:87]\n"
"v_exp_f32 v6, v6\n"
"v_mfma_f32_16x16x32_bf16 a[116:119], v[128:131], v[40:43], a[116:119]\n"
"v_exp_f32 v7, v7\n"
"v_mfma_f32_16x16x32_bf16 a[116:119], v[132:135], v[44:47], a[116:119]\n"
"v_cvt_pk_bf16_f32 v68, v4, v5\n"
"v_cvt_pk_bf16_f32 v69, v6, v7\n"
"v_mfma_f32_16x16x32_bf16 v[164:167], v[176:179], v[32:35], v[164:167]\n"
"ds_read_b64_tr_b16 v[128:129], v182 offset:768\n"
"v_mfma_f32_16x16x32_bf16 v[164:167], v[176:179], v[36:39], v[164:167]\n"
"s_waitcnt lgkmcnt(7)\n"
"v_mfma_f32_16x16x32_bf16 v[0:3], v[104:107], a[160:163], v[136:139]\n"
"ds_read_b64_tr_b16 v[130:131], v182 offset:4864\n"
"v_mfma_f32_16x16x32_bf16 v[0:3], v[108:111], a[164:167], v[0:3]\n"
"s_waitcnt lgkmcnt(2)\n"
"v_mfma_f32_16x16x32_bf16 v[0:3], v[112:115], a[168:171], v[0:3]\n"
"v_exp_f32 v8, v8\n"
"v_mfma_f32_16x16x32_bf16 v[0:3], v[116:119], a[172:175], v[0:3]\n"
"ds_read_b64_tr_b16 v[132:133], v182 offset:8960\n"
"v_mfma_f32_16x16x32_bf16 a[24:27], v[120:123], v[16:19], a[24:27]\n"
"s_add_u32 s52, s52, 0x4000\n"
"v_mfma_f32_16x16x32_bf16 a[24:27], v[124:127], v[20:23], a[24:27]\n"
"v_exp_f32 v9, v9\n"
"v_mfma_f32_16x16x32_bf16 a[56:59], v[120:123], v[24:27], a[56:59]\n"
"ds_read_b64_tr_b16 v[134:135], v182 offset:13056\n"
"v_cvt_pk_bf16_f32 v76, v8, v9\n"
"v_mfma_f32_16x16x32_bf16 a[56:59], v[124:127], v[28:31], a[56:59]\n"
"v_exp_f32 v10, v10\n"
"v_mfma_f32_16x16x32_bf16 v[4:7], v[104:107], a[176:179], v[140:143]\n"
"v_exp_f32 v11, v11\n"
"v_mfma_f32_16x16x32_bf16 v[4:7], v[108:111], a[180:183], v[4:7]\n"
"v_exp_f32 v12, v12\n"
"v_mfma_f32_16x16x32_bf16 v[4:7], v[112:115], a[184:187], v[4:7]\n"
"v_exp_f32 v13, v13\n"
"v_mfma_f32_16x16x32_bf16 v[4:7], v[116:119], a[188:191], v[4:7]\n"
"v_cvt_pk_bf16_f32 v77, v10, v11\n"
"v_cvt_pk_bf16_f32 v84, v12, v13\n"
"v_mfma_f32_16x16x32_bf16 a[88:91], v[120:123], v[32:35], a[88:91]\n"
"v_exp_f32 v14, v14\n"
"v_mfma_f32_16x16x32_bf16 a[88:91], v[124:127], v[36:39], a[88:91]\n"
"v_exp_f32 v15, v15\n"
"v_mfma_f32_16x16x32_bf16 a[120:123], v[120:123], v[40:43], a[120:123]\n"
"v_exp_f32 v0, v0\n"
"v_mfma_f32_16x16x32_bf16 a[120:123], v[124:127], v[44:47], a[120:123]\n"
"v_exp_f32 v1, v1\n"
"v_mfma_f32_16x16x32_bf16 v[8:11], v[104:107], a[192:195], v[144:147]\n"
"v_cvt_pk_bf16_f32 v85, v14, v15\n"
"v_cvt_pk_bf16_f32 v62, v0, v1\n"
"v_mfma_f32_16x16x32_bf16 v[8:11], v[108:111], a[196:199], v[8:11]\n"
"v_exp_f32 v2, v2\n"
"v_mfma_f32_16x16x32_bf16 v[8:11], v[112:115], a[200:203], v[8:11]\n"
"v_exp_f32 v3, v3\n"
"v_mfma_f32_16x16x32_bf16 v[8:11], v[116:119], a[204:207], v[8:11]\n"
"s_waitcnt vmcnt(16) lgkmcnt(0)\n"
"v_xor_b32 v180, 0x8000, v180\n"
"s_barrier\n"
"v_mfma_f32_16x16x32_bf16 a[28:31], v[128:131], v[16:19], a[28:31]\n"
"ds_read_b128 v[96:99], v180 offset:0\n"
"v_cvt_pk_bf16_f32 v63, v2, v3\n"
"v_mfma_f32_16x16x32_bf16 a[28:31], v[132:135], v[20:23], a[28:31]\n"
"ds_read_b128 v[100:103], v180 offset:256\n"
"v_mfma_f32_16x16x32_bf16 a[60:63], v[128:131], v[24:27], a[60:63]\n"
"ds_read_b64_tr_b16 v[120:121], v181 offset:16384\n"
"ds_read_b64_tr_b16 v[124:125], v181 offset:24576\n"
"v_mfma_f32_16x16x32_bf16 a[60:63], v[132:135], v[28:31], a[60:63]\n"
"ds_read_b64_tr_b16 v[122:123], v181 offset:20480\n"
"ds_read_b64_tr_b16 v[126:127], v181 offset:28672\n"
"v_mfma_f32_16x16x32_bf16 v[12:15], v[112:115], a[216:219], v[148:151]\n"
"ds_read_b128 v[112:115], v180 offset:512\n"
"v_mfma_f32_16x16x32_bf16 v[12:15], v[116:119], a[220:223], v[12:15]\n"
"ds_read_b128 v[116:119], v180 offset:768\n"
"v_mfma_f32_16x16x32_bf16 v[12:15], v[104:107], a[208:211], v[12:15]\n"
"s_add_u32 m0, s58, 0x10000\n"
"v_mfma_f32_16x16x32_bf16 v[12:15], v[108:111], a[212:215], v[12:15]\n"
"buffer_load_dwordx4 v183, s[40:43], s52 offen offset:0 lds\n"
"v_mfma_f32_16x16x32_bf16 a[92:95], v[128:131], v[32:35], a[92:95]\n"
"v_exp_f32 v4, v4\n"
"v_mfma_f32_16x16x32_bf16 a[92:95], v[132:135], v[36:39], a[92:95]\n"
"v_exp_f32 v5, v5\n"
"v_mfma_f32_16x16x32_bf16 a[124:127], v[128:131], v[40:43], a[124:127]\n"
"v_exp_f32 v6, v6\n"
"v_mfma_f32_16x16x32_bf16 a[124:127], v[132:135], v[44:47], a[124:127]\n"
"v_exp_f32 v7, v7\n"
"v_mfma_f32_16x16x32_bf16 v[168:171], v[176:179], v[40:43], v[168:171]\n"
"v_cvt_pk_bf16_f32 v70, v4, v5\n"
"v_cvt_pk_bf16_f32 v71, v6, v7\n"
"v_mfma_f32_16x16x32_bf16 v[168:171], v[176:179], v[44:47], v[168:171]\n"
"v_exp_f32 v8, v8\n"
"v_exp_f32 v9, v9\n"
"v_exp_f32 v10, v10\n"
"v_exp_f32 v11, v11\n"
"s_nop 0\n"
"v_cvt_pk_bf16_f32 v78, v8, v9\n"
"v_cvt_pk_bf16_f32 v79, v10, v11\n"
"s_waitcnt lgkmcnt(2)\n"
"s_xor_b32 s58, s58, 0x8000\n"
"s_xor_b32 s59, s59, 0x8000\n"
"s_add_u32 s55, s55, -1\n"
"s_and_b32 s62, s55, 15\n"
"s_cmp_lg_u32 s62, 0\n"
"s_cbranch_scc1 L_loop_b_%=\n"
"s_cmp_lg_u32 s55, 32\n"
"s_cbranch_scc1 L_seam_b_%=\n"
"v_mfma_f32_16x16x32_bf16 v[0:3], v[96:99], a[160:163], v[136:139]\n"
"v_exp_f32 v12, v12\n"
"v_mfma_f32_16x16x32_bf16 v[0:3], v[100:103], a[164:167], v[0:3]\n"
"s_waitcnt lgkmcnt(0)\n"
"v_mfma_f32_16x16x32_bf16 v[0:3], v[112:115], a[168:171], v[0:3]\n"
"buffer_load_dwordx4 v184, s[40:43], s52 offen offset:1024 lds\n"
"v_mfma_f32_16x16x32_bf16 v[0:3], v[116:119], a[172:175], v[0:3]\n"
"v_exp_f32 v13, v13\n"
"v_mfma_f32_16x16x32_bf16 a[0:3], v[120:123], v[56:59], a[0:3]\n"
"ds_read_b128 v[104:107], v180 offset:4096\n"
"v_cvt_pk_bf16_f32 v86, v12, v13\n"
"v_mfma_f32_16x16x32_bf16 a[0:3], v[124:127], v[60:63], a[0:3]\n"
"ds_read_b64_tr_b16 v[128:129], v182 offset:16384\n"
"v_mfma_f32_16x16x32_bf16 a[32:35], v[120:123], v[64:67], a[32:35]\n"
"ds_read_b64_tr_b16 v[130:131], v182 offset:20480\n"
"v_mfma_f32_16x16x32_bf16 a[32:35], v[124:127], v[68:71], a[32:35]\n"
"v_exp_f32 v14, v14\n"
"v_mfma_f32_16x16x32_bf16 v[4:7], v[96:99], a[176:179], v[140:143]\n"
"ds_read_b64_tr_b16 v[132:133], v182 offset:24576\n"
"v_mfma_f32_16x16x32_bf16 v[4:7], v[100:103], a[180:183], v[4:7]\n"
"buffer_load_dwordx4 v183, s[40:43], s52 offen offset:2048 lds\n"
"v_mfma_f32_16x16x32_bf16 v[4:7], v[112:115], a[184:187], v[4:7]\n"
"ds_read_b64_tr_b16 v[134:135], v182 offset:28672\n"
"v_mfma_f32_16x16x32_bf16 v[4:7], v[116:119], a[188:191], v[4:7]\n"
"v_exp_f32 v15, v15\n"
"v_mfma_f32_16x16x32_bf16 a[64:67], v[120:123], v[72:75], a[64:67]\n"
"ds_read_b128 v[108:111], v180 offset:4352\n"
"v_cvt_pk_bf16_f32 v87, v14, v15\n"
"v_mfma_f32_16x16x32_bf16 a[64:67], v[124:127], v[76:79], a[64:67]\n"
"v_exp_f32 v0, v0\n"
"v_mfma_f32_16x16x32_bf16 a[96:99], v[120:123], v[80:83], a[96:99]\n"
"v_exp_f32 v1, v1\n"
"v_mfma_f32_16x16x32_bf16 a[96:99], v[124:127], v[84:87], a[96:99]\n"
"v_exp_f32 v2, v2\n"
"v_mfma_f32_16x16x32_bf16 v[8:11], v[96:99], a[192:195], v[144:147]\n"
"ds_read_b64_tr_b16 v[120:121], v181 offset:16640\n"
"v_cvt_pk_bf16_f32 v16, v0, v1\n"
"v_mfma_f32_16x16x32_bf16 v[8:11], v[100:103], a[196:199], v[8:11]\n"
"buffer_load_dwordx4 v[192:195], v185, s[44:47], s70 offen offset:0 nt\n"
"v_mfma_f32_16x16x32_bf16 v[8:11], v[112:115], a[200:203], v[8:11]\n"
"ds_read_b64_tr_b16 v[122:123], v181 offset:20736\n"
"v_mfma_f32_16x16x32_bf16 v[8:11], v[116:119], a[204:207], v[8:11]\n"
"s_waitcnt lgkmcnt(3)\n"
"v_mfma_f32_16x16x32_bf16 a[4:7], v[128:131], v[56:59], a[4:7]\n"
"v_exp_f32 v3, v3\n"
"v_mfma_f32_16x16x32_bf16 a[4:7], v[132:135], v[60:63], a[4:7]\n"
"ds_read_b64_tr_b16 v[124:125], v181 offset:24832\n"
"v_cvt_pk_bf16_f32 v17, v2, v3\n"
"v_mfma_f32_16x16x32_bf16 a[36:39], v[128:131], v[64:67], a[36:39]\n"
"v_exp_f32 v4, v4\n"
"v_mfma_f32_16x16x32_bf16 a[36:39], v[132:135], v[68:71], a[36:39]\n"
"v_exp_f32 v5, v5\n"
"v_mfma_f32_16x16x32_bf16 v[12:15], v[112:115], a[216:219], v[148:151]\n"
"ds_read_b128 v[112:115], v180 offset:4608\n"
"v_cvt_pk_bf16_f32 v24, v4, v5\n"
"v_mfma_f32_16x16x32_bf16 v[12:15], v[116:119], a[220:223], v[12:15]\n"
"ds_read_b128 v[116:119], v180 offset:4864\n"
"v_mfma_f32_16x16x32_bf16 v[12:15], v[96:99], a[208:211], v[12:15]\n"
"ds_read_b64_tr_b16 v[126:127], v181 offset:28928\n"
"v_mfma_f32_16x16x32_bf16 v[12:15], v[100:103], a[212:215], v[12:15]\n"
"buffer_load_dwordx4 v184, s[40:43], s52 offen offset:3072 lds\n"
"v_mfma_f32_16x16x32_bf16 a[68:71], v[128:131], v[72:75], a[68:71]\n"
"v_exp_f32 v6, v6\n"
"v_mfma_f32_16x16x32_bf16 a[68:71], v[132:135], v[76:79], a[68:71]\n"
"v_exp_f32 v7, v7\n"
"v_mfma_f32_16x16x32_bf16 a[100:103], v[128:131], v[80:83], a[100:103]\n"
"v_exp_f32 v8, v8\n"
"v_mfma_f32_16x16x32_bf16 a[100:103], v[132:135], v[84:87], a[100:103]\n"
"v_exp_f32 v9, v9\n"
"v_mfma_f32_16x16x32_bf16 v[156:159], v[176:179], v[56:59], v[156:159]\n"
"ds_read_b64_tr_b16 v[128:129], v182 offset:16640\n"
"v_cvt_pk_bf16_f32 v25, v6, v7\n"
"v_mfma_f32_16x16x32_bf16 v[156:159], v[176:179], v[60:63], v[156:159]\n"
"v_cvt_pk_bf16_f32 v32, v8, v9\n"
"s_waitcnt lgkmcnt(7)\n"
"v_mfma_f32_16x16x32_bf16 v[0:3], v[104:107], a[160:163], v[136:139]\n"
"ds_read_b64_tr_b16 v[130:131], v182 offset:20736\n"
"v_mfma_f32_16x16x32_bf16 v[0:3], v[108:111], a[164:167], v[0:3]\n"
"s_waitcnt lgkmcnt(2)\n"
"v_mfma_f32_16x16x32_bf16 v[0:3], v[112:115], a[168:171], v[0:3]\n"
"buffer_load_dwordx4 v[196:199], v185, s[44:47], s70 offen offset:64 nt\n"
"v_mfma_f32_16x16x32_bf16 v[0:3], v[116:119], a[172:175], v[0:3]\n"
"ds_read_b64_tr_b16 v[132:133], v182 offset:24832\n"
"v_mfma_f32_16x16x32_bf16 a[8:11], v[120:123], v[56:59], a[8:11]\n"
"ds_read_b128 v[96:99], v180 offset:8192\n"
"v_mfma_f32_16x16x32_bf16 a[8:11], v[124:127], v[60:63], a[8:11]\n"
"v_exp_f32 v10, v10\n"
"v_mfma_f32_16x16x32_bf16 a[40:43], v[120:123], v[64:67], a[40:43]\n"
"ds_read_b64_tr_b16 v[134:135], v182 offset:28928\n"
"v_mfma_f32_16x16x32_bf16 a[40:43], v[124:127], v[68:71], a[40:43]\n"
"s_add_u32 m0, s53, 0x4000\n"
"v_mfma_f32_16x16x32_bf16 v[4:7], v[104:107], a[176:179], v[140:143]\n"
"v_exp_f32 v11, v11\n"
"v_mfma_f32_16x16x32_bf16 v[4:7], v[108:111], a[180:183], v[4:7]\n"
"v_exp_f32 v12, v12\n"
"v_mfma_f32_16x16x32_bf16 v[4:7], v[112:115], a[184:187], v[4:7]\n"
"v_exp_f32 v13, v13\n"
"v_mfma_f32_16x16x32_bf16 v[4:7], v[116:119], a[188:191], v[4:7]\n"
"v_cvt_pk_bf16_f32 v33, v10, v11\n"
"v_cvt_pk_bf16_f32 v40, v12, v13\n"
"v_mfma_f32_16x16x32_bf16 a[72:75], v[120:123], v[72:75], a[72:75]\n"
"ds_read_b128 v[100:103], v180 offset:8448\n"
"v_mfma_f32_16x16x32_bf16 a[72:75], v[124:127], v[76:79], a[72:75]\n"
"v_exp_f32 v14, v14\n"
"v_mfma_f32_16x16x32_bf16 a[104:107], v[120:123], v[80:83], a[104:107]\n"
"buffer_load_dwordx4 v183, s[36:39], s52 offen offset:0 lds\n"
"v_mfma_f32_16x16x32_bf16 a[104:107], v[124:127], v[84:87], a[104:107]\n"
"v_exp_f32 v15, v15\n"
"v_mfma_f32_16x16x32_bf16 v[8:11], v[104:107], a[192:195], v[144:147]\n"
"ds_read_b64_tr_b16 v[120:121], v181 offset:16896\n"
"v_cvt_pk_bf16_f32 v41, v14, v15\n"
"v_mfma_f32_16x16x32_bf16 v[8:11], v[108:111], a[196:199], v[8:11]\n"
"v_exp_f32 v0, v0\n"
"v_mfma_f32_16x16x32_bf16 v[8:11], v[112:115], a[200:203], v[8:11]\n"
"ds_read_b64_tr_b16 v[122:123], v181 offset:20992\n"
"v_mfma_f32_16x16x32_bf16 v[8:11], v[116:119], a[204:207], v[8:11]\n"
"s_waitcnt lgkmcnt(3)\n"
"v_mfma_f32_16x16x32_bf16 a[12:15], v[128:131], v[56:59], a[12:15]\n"
"v_exp_f32 v1, v1\n"
"v_mfma_f32_16x16x32_bf16 a[12:15], v[132:135], v[60:63], a[12:15]\n"
"ds_read_b64_tr_b16 v[124:125], v181 offset:25088\n"
"v_cvt_pk_bf16_f32 v18, v0, v1\n"
"v_mfma_f32_16x16x32_bf16 a[44:47], v[128:131], v[64:67], a[44:47]\n"
"buffer_load_dwordx4 v[200:203], v185, s[44:47], s70 offen offset:128 nt\n"
"v_mfma_f32_16x16x32_bf16 a[44:47], v[132:135], v[68:71], a[44:47]\n"
"v_exp_f32 v2, v2\n"
"v_mfma_f32_16x16x32_bf16 v[12:15], v[112:115], a[216:219], v[148:151]\n"
"ds_read_b128 v[112:115], v180 offset:8704\n"
"v_mfma_f32_16x16x32_bf16 v[12:15], v[116:119], a[220:223], v[12:15]\n"
"ds_read_b128 v[116:119], v180 offset:8960\n"
"v_mfma_f32_16x16x32_bf16 v[12:15], v[104:107], a[208:211], v[12:15]\n"
"ds_read_b64_tr_b16 v[126:127], v181 offset:29184\n"
"v_mfma_f32_16x16x32_bf16 v[12:15], v[108:111], a[212:215], v[12:15]\n"
"v_exp_f32 v3, v3\n"
"v_mfma_f32_16x16x32_bf16 a[76:79], v[128:131], v[72:75], a[76:79]\n"
"v_exp_f32 v4, v4\n"
"v_mfma_f32_16x16x32_bf16 a[76:79], v[132:135], v[76:79], a[76:79]\n"
"v_exp_f32 v5, v5\n"
"v_mfma_f32_16x16x32_bf16 a[108:111], v[128:131], v[80:83], a[108:111]\n"
"buffer_load_dwordx4 v184, s[36:39], s52 offen offset:1024 lds\n"
"v_cvt_pk_bf16_f32 v19, v2, v3\n"
"v_mfma_f32_16x16x32_bf16 a[108:111], v[132:135], v[84:87], a[108:111]\n"
"v_exp_f32 v6, v6\n"
"v_mfma_f32_16x16x32_bf16 v[160:163], v[176:179], v[64:67], v[160:163]\n"
"ds_read_b64_tr_b16 v[128:129], v182 offset:16896\n"
"v_cvt_pk_bf16_f32 v26, v4, v5\n"
"v_mfma_f32_16x16x32_bf16 v[160:163], v[176:179], v[68:71], v[160:163]\n"
"s_waitcnt lgkmcnt(7)\n"
"v_mfma_f32_16x16x32_bf16 v[0:3], v[96:99], a[160:163], v[136:139]\n"
"ds_read_b64_tr_b16 v[130:131], v182 offset:20992\n"
"v_mfma_f32_16x16x32_bf16 v[0:3], v[100:103], a[164:167], v[0:3]\n"
"s_waitcnt lgkmcnt(2)\n"
"v_mfma_f32_16x16x32_bf16 v[0:3], v[112:115], a[168:171], v[0:3]\n"
"v_exp_f32 v7, v7\n"
"v_mfma_f32_16x16x32_bf16 v[0:3], v[116:119], a[172:175], v[0:3]\n"
"ds_read_b64_tr_b16 v[132:133], v182 offset:25088\n"
"v_cvt_pk_bf16_f32 v27, v6, v7\n"
"v_mfma_f32_16x16x32_bf16 a[16:19], v[120:123], v[56:59], a[16:19]\n"
"ds_read_b128 v[104:107], v180 offset:12288\n"
"v_mfma_f32_16x16x32_bf16 a[16:19], v[124:127], v[60:63], a[16:19]\n"
"buffer_load_dwordx4 v[204:207], v185, s[44:47], s70 offen offset:192 nt\n"
"v_mfma_f32_16x16x32_bf16 a[48:51], v[120:123], v[64:67], a[48:51]\n"
"ds_read_b64_tr_b16 v[134:135], v182 offset:29184\n"
"v_mfma_f32_16x16x32_bf16 a[48:51], v[124:127], v[68:71], a[48:51]\n"
"v_exp_f32 v8, v8\n"
"v_mfma_f32_16x16x32_bf16 v[4:7], v[96:99], a[176:179], v[140:143]\n"
"v_exp_f32 v9, v9\n"
"v_mfma_f32_16x16x32_bf16 v[4:7], v[100:103], a[180:183], v[4:7]\n"
"v_exp_f32 v10, v10\n"
"v_mfma_f32_16x16x32_bf16 v[4:7], v[112:115], a[184:187], v[4:7]\n"
"v_exp_f32 v11, v11\n"
"v_mfma_f32_16x16x32_bf16 v[4:7], v[116:119], a[188:191], v[4:7]\n"
"v_cvt_pk_bf16_f32 v34, v8, v9\n"
"v_cvt_pk_bf16_f32 v35, v10, v11\n"
"v_mfma_f32_16x16x32_bf16 a[80:83], v[120:123], v[72:75], a[80:83]\n"
"ds_read_b128 v[108:111], v180 offset:12544\n"
"v_mfma_f32_16x16x32_bf16 a[80:83], v[124:127], v[76:79], a[80:83]\n"
"buffer_load_dwordx4 v183, s[36:39], s52 offen offset:2048 lds\n"
"v_mfma_f32_16x16x32_bf16 a[112:115], v[120:123], v[80:83], a[112:115]\n"
"v_exp_f32 v12, v12\n"
"v_mfma_f32_16x16x32_bf16 a[112:115], v[124:127], v[84:87], a[112:115]\n"
"v_exp_f32 v13, v13\n"
"v_mfma_f32_16x16x32_bf16 v[8:11], v[96:99], a[192:195], v[144:147]\n"
"ds_read_b64_tr_b16 v[120:121], v181 offset:17152\n"
"v_cvt_pk_bf16_f32 v42, v12, v13\n"
"v_mfma_f32_16x16x32_bf16 v[8:11], v[100:103], a[196:199], v[8:11]\n"
"v_exp_f32 v14, v14\n"
"v_mfma_f32_16x16x32_bf16 v[8:11], v[112:115], a[200:203], v[8:11]\n"
"ds_read_b64_tr_b16 v[122:123], v181 offset:21248\n"
"v_mfma_f32_16x16x32_bf16 v[8:11], v[116:119], a[204:207], v[8:11]\n"
"s_waitcnt lgkmcnt(3)\n"
"v_mfma_f32_16x16x32_bf16 a[20:23], v[128:131], v[56:59], a[20:23]\n"
"buffer_load_dwordx4 v184, s[36:39], s52 offen offset:3072 lds\n"
"v_mfma_f32_16x16x32_bf16 a[20:23], v[132:135], v[60:63], a[20:23]\n"
"ds_read_b64_tr_b16 v[124:125], v181 offset:25344\n"
"v_mfma_f32_16x16x32_bf16 a[52:55], v[128:131], v[64:67], a[52:55]\n"
"v_exp_f32 v15, v15\n"
"v_mfma_f32_16x16x32_bf16 a[52:55], v[132:135], v[68:71], a[52:55]\n"
"v_exp_f32 v0, v0\n"
"v_cvt_pk_bf16_f32 v43, v14, v15\n"
"v_mfma_f32_16x16x32_bf16 v[12:15], v[112:115], a[216:219], v[148:151]\n"
"ds_read_b128 v[112:115], v180 offset:12800\n"
"v_mfma_f32_16x16x32_bf16 v[12:15], v[116:119], a[220:223], v[12:15]\n"
"ds_read_b128 v[116:119], v180 offset:13056\n"
"v_mfma_f32_16x16x32_bf16 v[12:15], v[96:99], a[208:211], v[12:15]\n"
"ds_read_b64_tr_b16 v[126:127], v181 offset:29440\n"
"v_mfma_f32_16x16x32_bf16 v[12:15], v[100:103], a[212:215], v[12:15]\n"
"buffer_load_dwordx4 v[208:211], v185, s[44:47], s71 offen offset:0 nt\n"
"v_mfma_f32_16x16x32_bf16 a[84:87], v[128:131], v[72:75], a[84:87]\n"
"v_exp_f32 v1, v1\n"
"v_mfma_f32_16x16x32_bf16 a[84:87], v[132:135], v[76:79], a[84:87]\n"
"v_exp_f32 v2, v2\n"
"v_mfma_f32_16x16x32_bf16 a[116:119], v[128:131], v[80:83], a[116:119]\n"
"v_exp_f32 v3, v3\n"
"v_mfma_f32_16x16x32_bf16 a[116:119], v[132:135], v[84:87], a[116:119]\n"
"v_cvt_pk_bf16_f32 v20, v0, v1\n"
"v_cvt_pk_bf16_f32 v21, v2, v3\n"
"v_mfma_f32_16x16x32_bf16 v[164:167], v[176:179], v[72:75], v[164:167]\n"
"ds_read_b64_tr_b16 v[128:129], v182 offset:17152\n"
"v_mfma_f32_16x16x32_bf16 v[164:167], v[176:179], v[76:79], v[164:167]\n"
"s_waitcnt lgkmcnt(7)\n"
"v_mfma_f32_16x16x32_bf16 v[0:3], v[104:107], a[160:163], v[136:139]\n"
"ds_read_b64_tr_b16 v[130:131], v182 offset:21248\n"
"v_mfma_f32_16x16x32_bf16 v[0:3], v[108:111], a[164:167], v[0:3]\n"
"s_waitcnt lgkmcnt(2)\n"
"v_mfma_f32_16x16x32_bf16 v[0:3], v[112:115], a[168:171], v[0:3]\n"
"s_add_u32 s52, s52, 0x4000\n"
"v_mfma_f32_16x16x32_bf16 v[0:3], v[116:119], a[172:175], v[0:3]\n"
"ds_read_b64_tr_b16 v[132:133], v182 offset:25344\n"
"v_mfma_f32_16x16x32_bf16 a[24:27], v[120:123], v[56:59], a[24:27]\n"
"v_exp_f32 v4, v4\n"
"v_mfma_f32_16x16x32_bf16 a[24:27], v[124:127], v[60:63], a[24:27]\n"
"v_exp_f32 v5, v5\n"
"v_mfma_f32_16x16x32_bf16 a[56:59], v[120:123], v[64:67], a[56:59]\n"
"ds_read_b64_tr_b16 v[134:135], v182 offset:29440\n"
"v_cvt_pk_bf16_f32 v28, v4, v5\n"
"v_mfma_f32_16x16x32_bf16 a[56:59], v[124:127], v[68:71], a[56:59]\n"
"v_exp_f32 v6, v6\n"
"v_exp_f32 v7, v7\n"
"s_nop 0\n"
"v_cvt_pk_bf16_f32 v29, v6, v7\n"
"v_mfma_f32_16x16x32_bf16 v[4:7], v[104:107], a[176:179], v[140:143]\n"
"v_exp_f32 v8, v8\n"
"v_mfma_f32_16x16x32_bf16 v[4:7], v[108:111], a[180:183], v[4:7]\n"
"buffer_load_dwordx4 v[212:215], v185, s[44:47], s71 offen offset:64 nt\n"
"v_mfma_f32_16x16x32_bf16 v[4:7], v[112:115], a[184:187], v[4:7]\n"
"v_exp_f32 v9, v9\n"
"v_mfma_f32_16x16x32_bf16 v[4:7], v[116:119], a[188:191], v[4:7]\n"
"v_exp_f32 v10, v10\n"
"v_mfma_f32_16x16x32_bf16 a[88:91], v[120:123], v[72:75], a[88:91]\n"
"v_exp_f32 v11, v11\n"
"v_mfma_f32_16x16x32_bf16 a[88:91], v[124:127], v[76:79], a[88:91]\n"
"v_cvt_pk_bf16_f32 v36, v8, v9\n"
"v_cvt_pk_bf16_f32 v37, v10, v11\n"
"v_mfma_f32_16x16x32_bf16 a[120:123], v[120:123], v[80:83], a[120:123]\n"
"v_exp_f32 v12, v12\n"
"v_mfma_f32_16x16x32_bf16 a[120:123], v[124:127], v[84:87], a[120:123]\n"
"v_exp_f32 v13, v13\n"
"v_mfma_f32_16x16x32_bf16 v[8:11], v[104:107], a[192:195], v[144:147]\n"
"v_exp_f32 v14, v14\n"
"v_mfma_f32_16x16x32_bf16 v[8:11], v[108:111], a[196:199], v[8:11]\n"
"v_exp_f32 v15, v15\n"
"v_mfma_f32_16x16x32_bf16 v[8:11], v[112:115], a[200:203], v[8:11]\n"
"v_cvt_pk_bf16_f32 v44, v12, v13\n"
"v_cvt_pk_bf16_f32 v45, v14, v15\n"
"v_mfma_f32_16x16x32_bf16 v[8:11], v[116:119], a[204:207], v[8:11]\n"
"s_waitcnt vmcnt(22) lgkmcnt(0)\n"
"v_xor_b32 v181, 0x8000, v181\n"
"v_xor_b32 v182, 0x8000, v182\n"
"s_barrier\n"
"v_mfma_f32_16x16x32_bf16 a[28:31], v[128:131], v[56:59], a[28:31]\n"
"ds_read_b128 v[96:99], v180 offset:16384\n"
"v_mfma_f32_16x16x32_bf16 a[28:31], v[132:135], v[60:63], a[28:31]\n"
"ds_read_b128 v[100:103], v180 offset:16640\n"
"v_mfma_f32_16x16x32_bf16 a[60:63], v[128:131], v[64:67], a[60:63]\n"
"ds_read_b64_tr_b16 v[120:121], v181 offset:0\n"
"ds_read_b64_tr_b16 v[124:125], v181 offset:8192\n"
"v_mfma_f32_16x16x32_bf16 a[60:63], v[132:135], v[68:71], a[60:63]\n"
"ds_read_b64_tr_b16 v[122:123], v181 offset:4096\n"
"ds_read_b64_tr_b16 v[126:127], v181 offset:12288\n"
"v_mfma_f32_16x16x32_bf16 v[12:15], v[112:115], a[216:219], v[148:151]\n"
"ds_read_b128 v[112:115], v180 offset:16896\n"
"v_mfma_f32_16x16x32_bf16 v[12:15], v[116:119], a[220:223], v[12:15]\n"
"ds_read_b128 v[116:119], v180 offset:17152\n"
"v_mfma_f32_16x16x32_bf16 v[12:15], v[104:107], a[208:211], v[12:15]\n"
"s_add_u32 m0, s54, 0x4000\n"
"v_mfma_f32_16x16x32_bf16 v[12:15], v[108:111], a[212:215], v[12:15]\n"
"buffer_load_dwordx4 v183, s[40:43], s52 offen offset:0 lds\n"
"v_mfma_f32_16x16x32_bf16 a[92:95], v[128:131], v[72:75], a[92:95]\n"
"v_exp_f32 v0, v0\n"
"v_mfma_f32_16x16x32_bf16 a[92:95], v[132:135], v[76:79], a[92:95]\n"
"v_exp_f32 v1, v1\n"
"v_mfma_f32_16x16x32_bf16 a[124:127], v[128:131], v[80:83], a[124:127]\n"
"v_exp_f32 v2, v2\n"
"v_mfma_f32_16x16x32_bf16 a[124:127], v[132:135], v[84:87], a[124:127]\n"
"v_exp_f32 v3, v3\n"
"v_mfma_f32_16x16x32_bf16 v[168:171], v[176:179], v[80:83], v[168:171]\n"
"v_cvt_pk_bf16_f32 v22, v0, v1\n"
"v_cvt_pk_bf16_f32 v23, v2, v3\n"
"v_mfma_f32_16x16x32_bf16 v[168:171], v[176:179], v[84:87], v[168:171]\n"
"v_exp_f32 v4, v4\n"
"v_exp_f32 v5, v5\n"
"v_exp_f32 v6, v6\n"
"v_exp_f32 v7, v7\n"
"v_exp_f32 v8, v8\n"
"v_exp_f32 v9, v9\n"
"v_exp_f32 v10, v10\n"
"v_exp_f32 v11, v11\n"
"s_nop 0\n"
"v_cvt_pk_bf16_f32 v30, v4, v5\n"
"v_cvt_pk_bf16_f32 v31, v6, v7\n"
"v_cvt_pk_bf16_f32 v38, v8, v9\n"
"v_cvt_pk_bf16_f32 v39, v10, v11\n"
"s_waitcnt lgkmcnt(2)\n"
"v_mfma_f32_16x16x32_bf16 v[0:3], v[96:99], a[160:163], v[136:139]\n"
"v_exp_f32 v12, v12\n"
"v_mfma_f32_16x16x32_bf16 v[0:3], v[100:103], a[164:167], v[0:3]\n"
"s_waitcnt lgkmcnt(0)\n"
"v_mfma_f32_16x16x32_bf16 v[0:3], v[112:115], a[168:171], v[0:3]\n"
"buffer_load_dwordx4 v184, s[40:43], s52 offen offset:1024 lds\n"
"v_mfma_f32_16x16x32_bf16 v[0:3], v[116:119], a[172:175], v[0:3]\n"
"v_exp_f32 v13, v13\n"
"v_mfma_f32_16x16x32_bf16 a[0:3], v[120:123], v[16:19], a[0:3]\n"
"ds_read_b128 v[104:107], v180 offset:20480\n"
"v_cvt_pk_bf16_f32 v46, v12, v13\n"
"v_mfma_f32_16x16x32_bf16 a[0:3], v[124:127], v[20:23], a[0:3]\n"
"ds_read_b64_tr_b16 v[128:129], v182 offset:0\n"
"v_mfma_f32_16x16x32_bf16 a[32:35], v[120:123], v[24:27], a[32:35]\n"
"ds_read_b64_tr_b16 v[130:131], v182 offset:4096\n"
"v_mfma_f32_16x16x32_bf16 a[32:35], v[124:127], v[28:31], a[32:35]\n"
"v_exp_f32 v14, v14\n"
"v_mfma_f32_16x16x32_bf16 v[4:7], v[96:99], a[176:179], v[140:143]\n"
"ds_read_b64_tr_b16 v[132:133], v182 offset:8192\n"
"v_mfma_f32_16x16x32_bf16 v[4:7], v[100:103], a[180:183], v[4:7]\n"
"buffer_load_dwordx4 v183, s[40:43], s52 offen offset:2048 lds\n"
"v_mfma_f32_16x16x32_bf16 v[4:7], v[112:115], a[184:187], v[4:7]\n"
"ds_read_b64_tr_b16 v[134:135], v182 offset:12288\n"
"v_mfma_f32_16x16x32_bf16 v[4:7], v[116:119], a[188:191], v[4:7]\n"
"v_exp_f32 v15, v15\n"
"v_mfma_f32_16x16x32_bf16 a[64:67], v[120:123], v[32:35], a[64:67]\n"
"ds_read_b128 v[108:111], v180 offset:20736\n"
"v_cvt_pk_bf16_f32 v47, v14, v15\n"
"v_mfma_f32_16x16x32_bf16 a[64:67], v[124:127], v[36:39], a[64:67]\n"
"v_exp_f32 v0, v0\n"
"v_mfma_f32_16x16x32_bf16 a[96:99], v[120:123], v[40:43], a[96:99]\n"
"v_exp_f32 v1, v1\n"
"v_mfma_f32_16x16x32_bf16 a[96:99], v[124:127], v[44:47], a[96:99]\n"
"v_exp_f32 v2, v2\n"
"v_mfma_f32_16x16x32_bf16 v[8:11], v[96:99], a[192:195], v[144:147]\n"
"ds_read_b64_tr_b16 v[120:121], v181 offset:256\n"
"v_cvt_pk_bf16_f32 v56, v0, v1\n"
"v_mfma_f32_16x16x32_bf16 v[8:11], v[100:103], a[196:199], v[8:11]\n"
"buffer_load_dwordx4 v[216:219], v185, s[44:47], s71 offen offset:128 nt\n"
"v_mfma_f32_16x16x32_bf16 v[8:11], v[112:115], a[200:203], v[8:11]\n"
"ds_read_b64_tr_b16 v[122:123], v181 offset:4352\n"
"v_mfma_f32_16x16x32_bf16 v[8:11], v[116:119], a[204:207], v[8:11]\n"
"s_waitcnt lgkmcnt(3)\n"
"v_mfma_f32_16x16x32_bf16 a[4:7], v[128:131], v[16:19], a[4:7]\n"
"v_exp_f32 v3, v3\n"
"v_mfma_f32_16x16x32_bf16 a[4:7], v[132:135], v[20:23], a[4:7]\n"
"ds_read_b64_tr_b16 v[124:125], v181 offset:8448\n"
"v_cvt_pk_bf16_f32 v57, v2, v3\n"
"v_mfma_f32_16x16x32_bf16 a[36:39], v[128:131], v[24:27], a[36:39]\n"
"v_exp_f32 v4, v4\n"
"v_mfma_f32_16x16x32_bf16 a[36:39], v[132:135], v[28:31], a[36:39]\n"
"v_exp_f32 v5, v5\n"
"v_mfma_f32_16x16x32_bf16 v[12:15], v[112:115], a[216:219], v[148:151]\n"
"ds_read_b128 v[112:115], v180 offset:20992\n"
"v_cvt_pk_bf16_f32 v64, v4, v5\n"
"v_mfma_f32_16x16x32_bf16 v[12:15], v[116:119], a[220:223], v[12:15]\n"
"ds_read_b128 v[116:119], v180 offset:21248\n"
"v_mfma_f32_16x16x32_bf16 v[12:15], v[96:99], a[208:211], v[12:15]\n"
"ds_read_b64_tr_b16 v[126:127], v181 offset:12544\n"
"v_mfma_f32_16x16x32_bf16 v[12:15], v[100:103], a[212:215], v[12:15]\n"
"buffer_load_dwordx4 v184, s[40:43], s52 offen offset:3072 lds\n"
"v_mfma_f32_16x16x32_bf16 a[68:71], v[128:131], v[32:35], a[68:71]\n"
"v_exp_f32 v6, v6\n"
"v_mfma_f32_16x16x32_bf16 a[68:71], v[132:135], v[36:39], a[68:71]\n"
"v_exp_f32 v7, v7\n"
"v_mfma_f32_16x16x32_bf16 a[100:103], v[128:131], v[40:43], a[100:103]\n"
"v_exp_f32 v8, v8\n"
"v_mfma_f32_16x16x32_bf16 a[100:103], v[132:135], v[44:47], a[100:103]\n"
"v_exp_f32 v9, v9\n"
"v_mfma_f32_16x16x32_bf16 v[156:159], v[176:179], v[16:19], v[156:159]\n"
"ds_read_b64_tr_b16 v[128:129], v182 offset:256\n"
"v_cvt_pk_bf16_f32 v65, v6, v7\n"
"v_mfma_f32_16x16x32_bf16 v[156:159], v[176:179], v[20:23], v[156:159]\n"
"v_cvt_pk_bf16_f32 v72, v8, v9\n"
"s_waitcnt lgkmcnt(7)\n"
"v_mfma_f32_16x16x32_bf16 v[0:3], v[104:107], a[160:163], v[136:139]\n"
"ds_read_b64_tr_b16 v[130:131], v182 offset:4352\n"
"v_mfma_f32_16x16x32_bf16 v[0:3], v[108:111], a[164:167], v[0:3]\n"
"s_waitcnt lgkmcnt(2)\n"
"v_mfma_f32_16x16x32_bf16 v[0:3], v[112:115], a[168:171], v[0:3]\n"
"buffer_load_dwordx4 v[220:223], v185, s[44:47], s71 offen offset:192 nt\n"
"v_mfma_f32_16x16x32_bf16 v[0:3], v[116:119], a[172:175], v[0:3]\n"
"ds_read_b64_tr_b16 v[132:133], v182 offset:8448\n"
"v_mfma_f32_16x16x32_bf16 a[8:11], v[120:123], v[16:19], a[8:11]\n"
"ds_read_b128 v[96:99], v180 offset:24576\n"
"v_mfma_f32_16x16x32_bf16 a[8:11], v[124:127], v[20:23], a[8:11]\n"
"v_exp_f32 v10, v10\n"
"v_mfma_f32_16x16x32_bf16 a[40:43], v[120:123], v[24:27], a[40:43]\n"
"ds_read_b64_tr_b16 v[134:135], v182 offset:12544\n"
"v_mfma_f32_16x16x32_bf16 a[40:43], v[124:127], v[28:31], a[40:43]\n"
"s_add_u32 m0, s53, 0x8000\n"
"v_mfma_f32_16x16x32_bf16 v[4:7], v[104:107], a[176:179], v[140:143]\n"
"v_exp_f32 v11, v11\n"
"v_mfma_f32_16x16x32_bf16 v[4:7], v[108:111], a[180:183], v[4:7]\n"
"v_exp_f32 v12, v12\n"
"v_mfma_f32_16x16x32_bf16 v[4:7], v[112:115], a[184:187], v[4:7]\n"
"v_exp_f32 v13, v13\n"
"v_mfma_f32_16x16x32_bf16 v[4:7], v[116:119], a[188:191], v[4:7]\n"
"v_cvt_pk_bf16_f32 v73, v10, v11\n"
"v_cvt_pk_bf16_f32 v80, v12, v13\n"
"v_mfma_f32_16x16x32_bf16 a[72:75], v[120:123], v[32:35], a[72:75]\n"
"ds_read_b128 v[100:103], v180 offset:24832\n"
"v_mfma_f32_16x16x32_bf16 a[72:75], v[124:127], v[36:39], a[72:75]\n"
"v_exp_f32 v14, v14\n"
"v_mfma_f32_16x16x32_bf16 a[104:107], v[120:123], v[40:43], a[104:107]\n"
"buffer_load_dwordx4 v183, s[36:39], s52 offen offset:0 lds\n"
"v_mfma_f32_16x16x32_bf16 a[104:107], v[124:127], v[44:47], a[104:107]\n"
"v_exp_f32 v15, v15\n"
"v_mfma_f32_16x16x32_bf16 v[8:11], v[104:107], a[192:195], v[144:147]\n"
"ds_read_b64_tr_b16 v[120:121], v181 offset:512\n"
"v_cvt_pk_bf16_f32 v81, v14, v15\n"
"v_mfma_f32_16x16x32_bf16 v[8:11], v[108:111], a[196:199], v[8:11]\n"
"v_exp_f32 v0, v0\n"
"v_mfma_f32_16x16x32_bf16 v[8:11], v[112:115], a[200:203], v[8:11]\n"
"ds_read_b64_tr_b16 v[122:123], v181 offset:4608\n"
"v_mfma_f32_16x16x32_bf16 v[8:11], v[116:119], a[204:207], v[8:11]\n"
"s_waitcnt lgkmcnt(3)\n"
"v_mfma_f32_16x16x32_bf16 a[12:15], v[128:131], v[16:19], a[12:15]\n"
"v_exp_f32 v1, v1\n"
"v_mfma_f32_16x16x32_bf16 a[12:15], v[132:135], v[20:23], a[12:15]\n"
"ds_read_b64_tr_b16 v[124:125], v181 offset:8704\n"
"v_cvt_pk_bf16_f32 v58, v0, v1\n"
"v_mfma_f32_16x16x32_bf16 a[44:47], v[128:131], v[24:27], a[44:47]\n"
"buffer_load_dwordx4 v[224:227], v185, s[44:47], s72 offen offset:0 nt\n"
"v_mfma_f32_16x16x32_bf16 a[44:47], v[132:135], v[28:31], a[44:47]\n"
"v_exp_f32 v2, v2\n"
"v_mfma_f32_16x16x32_bf16 v[12:15], v[112:115], a[216:219], v[148:151]\n"
"ds_read_b128 v[112:115], v180 offset:25088\n"
"v_mfma_f32_16x16x32_bf16 v[12:15], v[116:119], a[220:223], v[12:15]\n"
"ds_read_b128 v[116:119], v180 offset:25344\n"
"v_mfma_f32_16x16x32_bf16 v[12:15], v[104:107], a[208:211], v[12:15]\n"
"ds_read_b64_tr_b16 v[126:127], v181 offset:12800\n"
"v_mfma_f32_16x16x32_bf16 v[12:15], v[108:111], a[212:215], v[12:15]\n"
"v_exp_f32 v3, v3\n"
"v_mfma_f32_16x16x32_bf16 a[76:79], v[128:131], v[32:35], a[76:79]\n"
"v_exp_f32 v4, v4\n"
"v_mfma_f32_16x16x32_bf16 a[76:79], v[132:135], v[36:39], a[76:79]\n"
"v_exp_f32 v5, v5\n"
"v_mfma_f32_16x16x32_bf16 a[108:111], v[128:131], v[40:43], a[108:111]\n"
"buffer_load_dwordx4 v184, s[36:39], s52 offen offset:1024 lds\n"
"v_cvt_pk_bf16_f32 v59, v2, v3\n"
"v_mfma_f32_16x16x32_bf16 a[108:111], v[132:135], v[44:47], a[108:111]\n"
"v_exp_f32 v6, v6\n"
"v_mfma_f32_16x16x32_bf16 v[160:163], v[176:179], v[24:27], v[160:163]\n"
"ds_read_b64_tr_b16 v[128:129], v182 offset:512\n"
"v_cvt_pk_bf16_f32 v66, v4, v5\n"
"v_mfma_f32_16x16x32_bf16 v[160:163], v[176:179], v[28:31], v[160:163]\n"
"s_waitcnt lgkmcnt(7)\n"
"v_mfma_f32_16x16x32_bf16 v[0:3], v[96:99], a[160:163], v[136:139]\n"
"ds_read_b64_tr_b16 v[130:131], v182 offset:4608\n"
"v_mfma_f32_16x16x32_bf16 v[0:3], v[100:103], a[164:167], v[0:3]\n"
"s_waitcnt lgkmcnt(2)\n"
"v_mfma_f32_16x16x32_bf16 v[0:3], v[112:115], a[168:171], v[0:3]\n"
"v_exp_f32 v7, v7\n"
"v_mfma_f32_16x16x32_bf16 v[0:3], v[116:119], a[172:175], v[0:3]\n"
"ds_read_b64_tr_b16 v[132:133], v182 offset:8704\n"
"v_cvt_pk_bf16_f32 v67, v6, v7\n"
"v_mfma_f32_16x16x32_bf16 a[16:19], v[120:123], v[16:19], a[16:19]\n"
"ds_read_b128 v[104:107], v180 offset:28672\n"
"v_mfma_f32_16x16x32_bf16 a[16:19], v[124:127], v[20:23], a[16:19]\n"
"buffer_load_dwordx4 v[228:231], v185, s[44:47], s72 offen offset:64 nt\n"
"v_mfma_f32_16x16x32_bf16 a[48:51], v[120:123], v[24:27], a[48:51]\n"
"ds_read_b64_tr_b16 v[134:135], v182 offset:12800\n"
"v_mfma_f32_16x16x32_bf16 a[48:51], v[124:127], v[28:31], a[48:51]\n"
"v_exp_f32 v8, v8\n"
"v_mfma_f32_16x16x32_bf16 v[4:7], v[96:99], a[176:179], v[140:143]\n"
"v_exp_f32 v9, v9\n"
"v_mfma_f32_16x16x32_bf16 v[4:7], v[100:103], a[180:183], v[4:7]\n"
"v_exp_f32 v10, v10\n"
"v_mfma_f32_16x16x32_bf16 v[4:7], v[112:115], a[184:187], v[4:7]\n"
"v_exp_f32 v11, v11\n"
"v_mfma_f32_16x16x32_bf16 v[4:7], v[116:119], a[188:191], v[4:7]\n"
"v_cvt_pk_bf16_f32 v74, v8, v9\n"
"v_cvt_pk_bf16_f32 v75, v10, v11\n"
"v_mfma_f32_16x16x32_bf16 a[80:83], v[120:123], v[32:35], a[80:83]\n"
"ds_read_b128 v[108:111], v180 offset:28928\n"
"v_mfma_f32_16x16x32_bf16 a[80:83], v[124:127], v[36:39], a[80:83]\n"
"buffer_load_dwordx4 v183, s[36:39], s52 offen offset:2048 lds\n"
"v_mfma_f32_16x16x32_bf16 a[112:115], v[120:123], v[40:43], a[112:115]\n"
"v_exp_f32 v12, v12\n"
"v_mfma_f32_16x16x32_bf16 a[112:115], v[124:127], v[44:47], a[112:115]\n"
"v_exp_f32 v13, v13\n"
"v_mfma_f32_16x16x32_bf16 v[8:11], v[96:99], a[192:195], v[144:147]\n"
"ds_read_b64_tr_b16 v[120:121], v181 offset:768\n"
"v_cvt_pk_bf16_f32 v82, v12, v13\n"
"v_mfma_f32_16x16x32_bf16 v[8:11], v[100:103], a[196:199], v[8:11]\n"
"v_exp_f32 v14, v14\n"
"v_mfma_f32_16x16x32_bf16 v[8:11], v[112:115], a[200:203], v[8:11]\n"
"ds_read_b64_tr_b16 v[122:123], v181 offset:4864\n"
"v_mfma_f32_16x16x32_bf16 v[8:11], v[116:119], a[204:207], v[8:11]\n"
"s_waitcnt lgkmcnt(3)\n"
"v_mfma_f32_16x16x32_bf16 a[20:23], v[128:131], v[16:19], a[20:23]\n"
"buffer_load_dwordx4 v184, s[36:39], s52 offen offset:3072 lds\n"
"v_mfma_f32_16x16x32_bf16 a[20:23], v[132:135], v[20:23], a[20:23]\n"
"ds_read_b64_tr_b16 v[124:125], v181 offset:8960\n"
"v_mfma_f32_16x16x32_bf16 a[52:55], v[128:131], v[24:27], a[52:55]\n"
"v_exp_f32 v15, v15\n"
"v_mfma_f32_16x16x32_bf16 a[52:55], v[132:135], v[28:31], a[52:55]\n"
"v_exp_f32 v0, v0\n"
"v_cvt_pk_bf16_f32 v83, v14, v15\n"
"v_mfma_f32_16x16x32_bf16 v[12:15], v[112:115], a[216:219], v[148:151]\n"
"ds_read_b128 v[112:115], v180 offset:29184\n"
"v_mfma_f32_16x16x32_bf16 v[12:15], v[116:119], a[220:223], v[12:15]\n"
"ds_read_b128 v[116:119], v180 offset:29440\n"
"v_mfma_f32_16x16x32_bf16 v[12:15], v[96:99], a[208:211], v[12:15]\n"
"ds_read_b64_tr_b16 v[126:127], v181 offset:13056\n"
"v_mfma_f32_16x16x32_bf16 v[12:15], v[100:103], a[212:215], v[12:15]\n"
"buffer_load_dwordx4 v[232:235], v185, s[44:47], s72 offen offset:128 nt\n"
"v_mfma_f32_16x16x32_bf16 a[84:87], v[128:131], v[32:35], a[84:87]\n"
"v_exp_f32 v1, v1\n"
"v_mfma_f32_16x16x32_bf16 a[84:87], v[132:135], v[36:39], a[84:87]\n"
"v_exp_f32 v2, v2\n"
"v_mfma_f32_16x16x32_bf16 a[116:119], v[128:131], v[40:43], a[116:119]\n"
"v_exp_f32 v3, v3\n"
"v_mfma_f32_16x16x32_bf16 a[116:119], v[132:135], v[44:47], a[116:119]\n"
"v_cvt_pk_bf16_f32 v60, v0, v1\n"
"v_cvt_pk_bf16_f32 v61, v2, v3\n"
"v_mfma_f32_16x16x32_bf16 v[164:167], v[176:179], v[32:35], v[164:167]\n"
"ds_read_b64_tr_b16 v[128:129], v182 offset:768\n"
"v_mfma_f32_16x16x32_bf16 v[164:167], v[176:179], v[36:39], v[164:167]\n"
"s_waitcnt lgkmcnt(7)\n"
"v_mfma_f32_16x16x32_bf16 v[0:3], v[104:107], a[160:163], v[136:139]\n"
"ds_read_b64_tr_b16 v[130:131], v182 offset:4864\n"
"v_mfma_f32_16x16x32_bf16 v[0:3], v[108:111], a[164:167], v[0:3]\n"
"s_waitcnt lgkmcnt(2)\n"
"v_mfma_f32_16x16x32_bf16 v[0:3], v[112:115], a[168:171], v[0:3]\n"
"s_add_u32 s52, s52, 0x4000\n"
"v_mfma_f32_16x16x32_bf16 v[0:3], v[116:119], a[172:175], v[0:3]\n"
"ds_read_b64_tr_b16 v[132:133], v182 offset:8960\n"
"v_mfma_f32_16x16x32_bf16 a[24:27], v[120:123], v[16:19], a[24:27]\n"
"v_exp_f32 v4, v4\n"
"v_mfma_f32_16x16x32_bf16 a[24:27], v[124:127], v[20:23], a[24:27]\n"
"v_exp_f32 v5, v5\n"
"v_mfma_f32_16x16x32_bf16 a[56:59], v[120:123], v[24:27], a[56:59]\n"
"ds_read_b64_tr_b16 v[134:135], v182 offset:13056\n"
"v_cvt_pk_bf16_f32 v68, v4, v5\n"
"v_mfma_f32_16x16x32_bf16 a[56:59], v[124:127], v[28:31], a[56:59]\n"
"v_exp_f32 v6, v6\n"
"v_exp_f32 v7, v7\n"
"s_nop 0\n"
"v_cvt_pk_bf16_f32 v69, v6, v7\n"
"v_mfma_f32_16x16x32_bf16 v[4:7], v[104:107], a[176:179], v[140:143]\n"
"v_exp_f32 v8, v8\n"
"v_mfma_f32_16x16x32_bf16 v[4:7], v[108:111], a[180:183], v[4:7]\n"
"buffer_load_dwordx4 v[236:239], v185, s[44:47], s72 offen offset:192 nt\n"
"v_mfma_f32_16x16x32_bf16 v[4:7], v[112:115], a[184:187], v[4:7]\n"
"v_exp_f32 v9, v9\n"
"v_mfma_f32_16x16x32_bf16 v[4:7], v[116:119], a[188:191], v[4:7]\n"
"v_exp_f32 v10, v10\n"
"v_mfma_f32_16x16x32_bf16 a[88:91], v[120:123], v[32:35], a[88:91]\n"
"v_exp_f32 v11, v11\n"
"v_mfma_f32_16x16x32_bf16 a[88:91], v[124:127], v[36:39], a[88:91]\n"
"v_cvt_pk_bf16_f32 v76, v8, v9\n"
"v_cvt_pk_bf16_f32 v77, v10, v11\n"
"v_mfma_f32_16x16x32_bf16 a[120:123], v[120:123], v[40:43], a[120:123]\n"
"v_exp_f32 v12, v12\n"
"v_mfma_f32_16x16x32_bf16 a[120:123], v[124:127], v[44:47], a[120:123]\n"
"v_exp_f32 v13, v13\n"
"v_mfma_f32_16x16x32_bf16 v[8:11], v[104:107], a[192:195], v[144:147]\n"
"v_exp_f32 v14, v14\n"
"v_mfma_f32_16x16x32_bf16 v[8:11], v[108:111], a[196:199], v[8:11]\n"
"v_exp_f32 v15, v15\n"
"v_mfma_f32_16x16x32_bf16 v[8:11], v[112:115], a[200:203], v[8:11]\n"
"v_cvt_pk_bf16_f32 v84, v12, v13\n"
"v_cvt_pk_bf16_f32 v85, v14, v15\n"
"v_mfma_f32_16x16x32_bf16 v[8:11], v[116:119], a[204:207], v[8:11]\n"
"s_waitcnt vmcnt(14) lgkmcnt(0)\n"
"v_xor_b32 v180, 0x8000, v180\n"
"s_barrier\n"
"v_mfma_f32_16x16x32_bf16 a[28:31], v[128:131], v[16:19], a[28:31]\n"
"ds_read_b128 v[96:99], v180 offset:0\n"
"v_lshlrev_b32 v186, 16, v192\n"
"v_mfma_f32_16x16x32_bf16 a[28:31], v[132:135], v[20:23], a[28:31]\n"
"ds_read_b128 v[100:103], v180 offset:256\n"
"v_and_b32 v192, 0xffff0000, v192\n"
"v_mfma_f32_16x16x32_bf16 a[60:63], v[128:131], v[24:27], a[60:63]\n"
"ds_read_b64_tr_b16 v[120:121], v181 offset:16384\n"
"ds_read_b64_tr_b16 v[124:125], v181 offset:24576\n"
"v_mfma_f32_16x16x32_bf16 a[60:63], v[132:135], v[28:31], a[60:63]\n"
"ds_read_b64_tr_b16 v[122:123], v181 offset:20480\n"
"ds_read_b64_tr_b16 v[126:127], v181 offset:28672\n"
"v_mfma_f32_16x16x32_bf16 v[12:15], v[112:115], a[216:219], v[148:151]\n"
"ds_read_b128 v[112:115], v180 offset:512\n"
"v_mul_f32 v186, s56, v186\n"
"v_mfma_f32_16x16x32_bf16 v[12:15], v[116:119], a[220:223], v[12:15]\n"
"ds_read_b128 v[116:119], v180 offset:768\n"
"v_mul_f32 v192, s56, v192\n"
"v_mfma_f32_16x16x32_bf16 v[12:15], v[104:107], a[208:211], v[12:15]\n"
"s_add_u32 m0, s54, 0x8000\n"
"v_cvt_pk_bf16_f32 v192, v186, v192\n"
"v_mfma_f32_16x16x32_bf16 v[12:15], v[108:111], a[212:215], v[12:15]\n"
"buffer_load_dwordx4 v183, s[40:43], s52 offen offset:0 lds\n"
"v_lshlrev_b32 v188, 16, v193\n"
"v_mfma_f32_16x16x32_bf16 a[92:95], v[128:131], v[32:35], a[92:95]\n"
"v_exp_f32 v0, v0\n"
"v_mfma_f32_16x16x32_bf16 a[92:95], v[132:135], v[36:39], a[92:95]\n"
"v_exp_f32 v1, v1\n"
"v_mfma_f32_16x16x32_bf16 a[124:127], v[128:131], v[40:43], a[124:127]\n"
"v_exp_f32 v2, v2\n"
"v_mfma_f32_16x16x32_bf16 a[124:127], v[132:135], v[44:47], a[124:127]\n"
"v_exp_f32 v3, v3\n"
"v_mfma_f32_16x16x32_bf16 v[168:171], v[176:179], v[40:43], v[168:171]\n"
"v_cvt_pk_bf16_f32 v62, v0, v1\n"
"v_cvt_pk_bf16_f32 v63, v2, v3\n"
"v_mfma_f32_16x16x32_bf16 v[168:171], v[176:179], v[44:47], v[168:171]\n"
"v_and_b32 v193, 0xffff0000, v193\n"
"v_exp_f32 v4, v4\n"
"v_exp_f32 v5, v5\n"
"v_exp_f32 v6, v6\n"
"v_exp_f32 v7, v7\n"
"v_exp_f32 v8, v8\n"
"v_exp_f32 v9, v9\n"
"v_exp_f32 v10, v10\n"
"v_exp_f32 v11, v11\n"
"s_nop 0\n"
"v_cvt_pk_bf16_f32 v70, v4, v5\n"
"v_cvt_pk_bf16_f32 v71, v6, v7\n"
"v_cvt_pk_bf16_f32 v78, v8, v9\n"
"v_cvt_pk_bf16_f32 v79, v10, v11\n"
"s_waitcnt lgkmcnt(2)\n"
"v_mfma_f32_16x16x32_bf16 v[0:3], v[96:99], a[160:163], v[136:139]\n"
"v_exp_f32 v12, v12\n"
"v_mfma_f32_16x16x32_bf16 v[0:3], v[100:103], a[164:167], v[0:3]\n"
"v_mul_f32 v188, s56, v188\n"
"s_waitcnt lgkmcnt(0)\n"
"v_mfma_f32_16x16x32_bf16 v[0:3], v[112:115], a[168:171], v[0:3]\n"
"buffer_load_dwordx4 v184, s[40:43], s52 offen offset:1024 lds\n"
"v_mul_f32 v193, s56, v193\n"
"v_mfma_f32_16x16x32_bf16 v[0:3], v[116:119], a[172:175], v[0:3]\n"
"v_exp_f32 v13, v13\n"
"v_mfma_f32_16x16x32_bf16 a[0:3], v[120:123], v[56:59], a[0:3]\n"
"ds_read_b128 v[104:107], v180 offset:4096\n"
"v_cvt_pk_bf16_f32 v86, v12, v13\n"
"v_mfma_f32_16x16x32_bf16 a[0:3], v[124:127], v[60:63], a[0:3]\n"
"ds_read_b64_tr_b16 v[128:129], v182 offset:16384\n"
"v_cvt_pk_bf16_f32 v193, v188, v193\n"
"v_mfma_f32_16x16x32_bf16 a[32:35], v[120:123], v[64:67], a[32:35]\n"
"ds_read_b64_tr_b16 v[130:131], v182 offset:20480\n"
"v_lshlrev_b32 v186, 16, v194\n"
"v_mfma_f32_16x16x32_bf16 a[32:35], v[124:127], v[68:71], a[32:35]\n"
"v_exp_f32 v14, v14\n"
"v_mfma_f32_16x16x32_bf16 v[4:7], v[96:99], a[176:179], v[140:143]\n"
"ds_read_b64_tr_b16 v[132:133], v182 offset:24576\n"
"v_and_b32 v194, 0xffff0000, v194\n"
"v_mfma_f32_16x16x32_bf16 v[4:7], v[100:103], a[180:183], v[4:7]\n"
"v_exp_f32 v15, v15\n"
"v_mfma_f32_16x16x32_bf16 v[4:7], v[112:115], a[184:187], v[4:7]\n"
"ds_read_b64_tr_b16 v[134:135], v182 offset:28672\n"
"v_cvt_pk_bf16_f32 v87, v14, v15\n"
"v_mfma_f32_16x16x32_bf16 v[4:7], v[116:119], a[188:191], v[4:7]\n"
"buffer_load_dwordx4 v183, s[40:43], s52 offen offset:2048 lds\n"
"v_mul_f32 v186, s56, v186\n"
"v_mfma_f32_16x16x32_bf16 a[64:67], v[120:123], v[72:75], a[64:67]\n"
"ds_read_b128 v[108:111], v180 offset:4352\n"
"v_mul_f32 v194, s56, v194\n"
"v_mfma_f32_16x16x32_bf16 a[64:67], v[124:127], v[76:79], a[64:67]\n"
"v_exp_f32 v0, v0\n"
"v_mfma_f32_16x16x32_bf16 a[96:99], v[120:123], v[80:83], a[96:99]\n"
"v_exp_f32 v1, v1\n"
"v_mfma_f32_16x16x32_bf16 a[96:99], v[124:127], v[84:87], a[96:99]\n"
"v_exp_f32 v2, v2\n"
"v_mfma_f32_16x16x32_bf16 v[8:11], v[96:99], a[192:195], v[144:147]\n"
"ds_read_b64_tr_b16 v[120:121], v181 offset:16640\n"
"v_cvt_pk_bf16_f32 v16, v0, v1\n"
"v_mfma_f32_16x16x32_bf16 v[8:11], v[100:103], a[196:199], v[8:11]\n"
"v_exp_f32 v3, v3\n"
"v_mfma_f32_16x16x32_bf16 v[8:11], v[112:115], a[200:203], v[8:11]\n"
"ds_read_b64_tr_b16 v[122:123], v181 offset:20736\n"
"v_cvt_pk_bf16_f32 v17, v2, v3\n"
"v_mfma_f32_16x16x32_bf16 v[8:11], v[116:119], a[204:207], v[8:11]\n"
"v_cvt_pk_bf16_f32 v194, v186, v194\n"
"s_waitcnt lgkmcnt(3)\n"
"v_mfma_f32_16x16x32_bf16 a[4:7], v[128:131], v[56:59], a[4:7]\n"
"buffer_load_dwordx4 v184, s[40:43], s52 offen offset:3072 lds\n"
"v_lshlrev_b32 v188, 16, v195\n"
"v_mfma_f32_16x16x32_bf16 a[4:7], v[132:135], v[60:63], a[4:7]\n"
"ds_read_b64_tr_b16 v[124:125], v181 offset:24832\n"
"v_and_b32 v195, 0xffff0000, v195\n"
"v_mfma_f32_16x16x32_bf16 a[36:39], v[128:131], v[64:67], a[36:39]\n"
"v_exp_f32 v4, v4\n"
"v_mfma_f32_16x16x32_bf16 a[36:39], v[132:135], v[68:71], a[36:39]\n"
"v_exp_f32 v5, v5\n"
"v_mfma_f32_16x16x32_bf16 v[12:15], v[112:115], a[216:219], v[148:151]\n"
"ds_read_b128 v[112:115], v180 offset:4608\n"
"v_cvt_pk_bf16_f32 v24, v4, v5\n"
"v_mfma_f32_16x16x32_bf16 v[12:15], v[116:119], a[220:223], v[12:15]\n"
"ds_read_b128 v[116:119], v180 offset:4864\n"
"v_mul_f32 v188, s56, v188\n"
"v_mfma_f32_16x16x32_bf16 v[12:15], v[96:99], a[208:211], v[12:15]\n"
"ds_read_b64_tr_b16 v[126:127], v181 offset:28928\n"
"v_mul_f32 v195, s56, v195\n"
"v_mfma_f32_16x16x32_bf16 v[12:15], v[100:103], a[212:215], v[12:15]\n"
"v_exp_f32 v6, v6\n"
"v_mfma_f32_16x16x32_bf16 a[68:71], v[128:131], v[72:75], a[68:71]\n"
"v_exp_f32 v7, v7\n"
"v_mfma_f32_16x16x32_bf16 a[68:71], v[132:135], v[76:79], a[68:71]\n"
"buffer_load_dwordx4 v[240:243], v185, s[44:47], s73 offen offset:0 nt\n"
"v_cvt_pk_bf16_f32 v25, v6, v7\n"
"v_mfma_f32_16x16x32_bf16 a[100:103], v[128:131], v[80:83], a[100:103]\n"
"v_exp_f32 v8, v8\n"
"v_mfma_f32_16x16x32_bf16 a[100:103], v[132:135], v[84:87], a[100:103]\n"
"v_exp_f32 v9, v9\n"
"v_mfma_f32_16x16x32_bf16 v[156:159], v[176:179], v[56:59], v[156:159]\n"
"ds_read_b64_tr_b16 v[128:129], v182 offset:16640\n"
"v_cvt_pk_bf16_f32 v32, v8, v9\n"
"v_mfma_f32_16x16x32_bf16 v[156:159], v[176:179], v[60:63], v[156:159]\n"
"v_cvt_pk_bf16_f32 v195, v188, v195\n"
"s_waitcnt lgkmcnt(7)\n"
"v_mfma_f32_16x16x32_bf16 v[0:3], v[104:107], a[160:163], v[136:139]\n"
"ds_read_b64_tr_b16 v[130:131], v182 offset:20736\n"
"v_lshlrev_b32 v186, 16, v196\n"
"v_mfma_f32_16x16x32_bf16 v[0:3], v[108:111], a[164:167], v[0:3]\n"
"v_and_b32 v196, 0xffff0000, v196\n"
"s_waitcnt lgkmcnt(2)\n"
"v_mfma_f32_16x16x32_bf16 v[0:3], v[112:115], a[168:171], v[0:3]\n"
"v_exp_f32 v10, v10\n"
"v_mfma_f32_16x16x32_bf16 v[0:3], v[116:119], a[172:175], v[0:3]\n"
"ds_read_b64_tr_b16 v[132:133], v182 offset:24832\n"
"v_mul_f32 v186, s56, v186\n"
"v_mfma_f32_16x16x32_bf16 a[8:11], v[120:123], v[56:59], a[8:11]\n"
"ds_read_b128 v[96:99], v180 offset:8192\n"
"v_mul_f32 v196, s56, v196\n"
"v_mfma_f32_16x16x32_bf16 a[8:11], v[124:127], v[60:63], a[8:11]\n"
"s_add_u32 m0, s53, 0xc000\n"
"v_cvt_pk_bf16_f32 v196, v186, v196\n"
"v_mfma_f32_16x16x32_bf16 a[40:43], v[120:123], v[64:67], a[40:43]\n"
"ds_read_b64_tr_b16 v[134:135], v182 offset:28928\n"
"v_lshlrev_b32 v188, 16, v197\n"
"v_mfma_f32_16x16x32_bf16 a[40:43], v[124:127], v[68:71], a[40:43]\n"
"v_exp_f32 v11, v11\n"
"v_mfma_f32_16x16x32_bf16 v[4:7], v[104:107], a[176:179], v[140:143]\n"
"v_exp_f32 v12, v12\n"
"v_mfma_f32_16x16x32_bf16 v[4:7], v[108:111], a[180:183], v[4:7]\n"
"v_exp_f32 v13, v13\n"
"v_mfma_f32_16x16x32_bf16 v[4:7], v[112:115], a[184:187], v[4:7]\n"
"v_cvt_pk_bf16_f32 v33, v10, v11\n"
"v_cvt_pk_bf16_f32 v40, v12, v13\n"
"v_mfma_f32_16x16x32_bf16 v[4:7], v[116:119], a[188:191], v[4:7]\n"
"v_exp_f32 v14, v14\n"
"v_mfma_f32_16x16x32_bf16 a[72:75], v[120:123], v[72:75], a[72:75]\n"
"ds_read_b128 v[100:103], v180 offset:8448\n"
"v_and_b32 v197, 0xffff0000, v197\n"
"v_mfma_f32_16x16x32_bf16 a[72:75], v[124:127], v[76:79], a[72:75]\n"
"buffer_load_dwordx4 v183, s[36:39], s52 offen offset:0 lds\n"
"v_mul_f32 v188, s56, v188\n"
"v_mfma_f32_16x16x32_bf16 a[104:107], v[120:123], v[80:83], a[104:107]\n"
"v_exp_f32 v15, v15\n"
"v_mfma_f32_16x16x32_bf16 a[104:107], v[124:127], v[84:87], a[104:107]\n"
"v_exp_f32 v0, v0\n"
"v_mfma_f32_16x16x32_bf16 v[8:11], v[104:107], a[192:195], v[144:147]\n"
"ds_read_b64_tr_b16 v[120:121], v181 offset:16896\n"
"v_cvt_pk_bf16_f32 v41, v14, v15\n"
"v_mfma_f32_16x16x32_bf16 v[8:11], v[108:111], a[196:199], v[8:11]\n"
"v_exp_f32 v1, v1\n"
"v_mfma_f32_16x16x32_bf16 v[8:11], v[112:115], a[200:203], v[8:11]\n"
"ds_read_b64_tr_b16 v[122:123], v181 offset:20992\n"
"v_cvt_pk_bf16_f32 v18, v0, v1\n"
"v_mfma_f32_16x16x32_bf16 v[8:11], v[116:119], a[204:207], v[8:11]\n"
"v_mul_f32 v197, s56, v197\n"
"s_waitcnt lgkmcnt(3)\n"
"v_mfma_f32_16x16x32_bf16 a[12:15], v[128:131], v[56:59], a[12:15]\n"
"v_exp_f32 v2, v2\n"
"v_mfma_f32_16x16x32_bf16 a[12:15], v[132:135], v[60:63], a[12:15]\n"
"ds_read_b64_tr_b16 v[124:125], v181 offset:25088\n"
"v_cvt_pk_bf16_f32 v197, v188, v197\n"
"v_mfma_f32_16x16x32_bf16 a[44:47], v[128:131], v[64:67], a[44:47]\n"
"buffer_load_dwordx4 v[244:247], v185, s[44:47], s73 offen offset:64 nt\n"
"v_lshlrev_b32 v186, 16, v198\n"
"v_mfma_f32_16x16x32_bf16 a[44:47], v[132:135], v[68:71], a[44:47]\n"
"v_exp_f32 v3, v3\n"
"v_mfma_f32_16x16x32_bf16 v[12:15], v[112:115], a[216:219], v[148:151]\n"
"ds_read_b128 v[112:115], v180 offset:8704\n"
"v_cvt_pk_bf16_f32 v19, v2, v3\n"
"v_mfma_f32_16x16x32_bf16 v[12:15], v[116:119], a[220:223], v[12:15]\n"
"ds_read_b128 v[116:119], v180 offset:8960\n"
"v_and_b32 v198, 0xffff0000, v198\n"
"v_mfma_f32_16x16x32_bf16 v[12:15], v[104:107], a[208:211], v[12:15]\n"
"ds_read_b64_tr_b16 v[126:127], v181 offset:29184\n"
"v_mul_f32 v186, s56, v186\n"
"v_mfma_f32_16x16x32_bf16 v[12:15], v[108:111], a[212:215], v[12:15]\n"
"v_exp_f32 v4, v4\n"
"v_mfma_f32_16x16x32_bf16 a[76:79], v[128:131], v[72:75], a[76:79]\n"
"v_exp_f32 v5, v5\n"
"v_mfma_f32_16x16x32_bf16 a[76:79], v[132:135], v[76:79], a[76:79]\n"
"v_exp_f32 v6, v6\n"
"v_mfma_f32_16x16x32_bf16 a[108:111], v[128:131], v[80:83], a[108:111]\n"
"v_exp_f32 v7, v7\n"
"v_mfma_f32_16x16x32_bf16 a[108:111], v[132:135], v[84:87], a[108:111]\n"
"buffer_load_dwordx4 v184, s[36:39], s52 offen offset:1024 lds\n"
"v_cvt_pk_bf16_f32 v26, v4, v5\n"
"v_mfma_f32_16x16x32_bf16 v[160:163], v[176:179], v[64:67], v[160:163]\n"
"ds_read_b64_tr_b16 v[128:129], v182 offset:16896\n"
"v_cvt_pk_bf16_f32 v27, v6, v7\n"
"v_mfma_f32_16x16x32_bf16 v[160:163], v[176:179], v[68:71], v[160:163]\n"
"v_mul_f32 v198, s56, v198\n"
"s_waitcnt lgkmcnt(7)\n"
"v_mfma_f32_16x16x32_bf16 v[0:3], v[96:99], a[160:163], v[136:139]\n"
"ds_read_b64_tr_b16 v[130:131], v182 offset:20992\n"
"v_cvt_pk_bf16_f32 v198, v186, v198\n"
"v_mfma_f32_16x16x32_bf16 v[0:3], v[100:103], a[164:167], v[0:3]\n"
"v_lshlrev_b32 v188, 16, v199\n"
"s_waitcnt lgkmcnt(2)\n"
"v_mfma_f32_16x16x32_bf16 v[0:3], v[112:115], a[168:171], v[0:3]\n"
"v_exp_f32 v8, v8\n"
"v_mfma_f32_16x16x32_bf16 v[0:3], v[116:119], a[172:175], v[0:3]\n"
"ds_read_b64_tr_b16 v[132:133], v182 offset:25088\n"
"v_and_b32 v199, 0xffff0000, v199\n"
"v_mfma_f32_16x16x32_bf16 a[16:19], v[120:123], v[56:59], a[16:19]\n"
"ds_read_b128 v[104:107], v180 offset:12288\n"
"v_mul_f32 v188, s56, v188\n"
"v_mfma_f32_16x16x32_bf16 a[16:19], v[124:127], v[60:63], a[16:19]\n"
"v_exp_f32 v9, v9\n"
"v_mfma_f32_16x16x32_bf16 a[48:51], v[120:123], v[64:67], a[48:51]\n"
"ds_read_b64_tr_b16 v[134:135], v182 offset:29184\n"
"v_cvt_pk_bf16_f32 v34, v8, v9\n"
"v_mfma_f32_16x16x32_bf16 a[48:51], v[124:127], v[68:71], a[48:51]\n"
"buffer_load_dwordx4 v183, s[36:39], s52 offen offset:2048 lds\n"
"v_mul_f32 v199, s56, v199\n"
"v_mfma_f32_16x16x32_bf16 v[4:7], v[96:99], a[176:179], v[140:143]\n"
"v_exp_f32 v10, v10\n"
"v_mfma_f32_16x16x32_bf16 v[4:7], v[100:103], a[180:183], v[4:7]\n"
"v_exp_f32 v11, v11\n"
"v_mfma_f32_16x16x32_bf16 v[4:7], v[112:115], a[184:187], v[4:7]\n"
"v_exp_f32 v12, v12\n"
"v_mfma_f32_16x16x32_bf16 v[4:7], v[116:119], a[188:191], v[4:7]\n"
"v_exp_f32 v13, v13\n"
"v_mfma_f32_16x16x32_bf16 a[80:83], v[120:123], v[72:75], a[80:83]\n"
"ds_read_b128 v[108:111], v180 offset:12544\n"
"v_cvt_pk_bf16_f32 v35, v10, v11\n"
"v_mfma_f32_16x16x32_bf16 a[80:83], v[124:127], v[76:79], a[80:83]\n"
"v_exp_f32 v14, v14\n"
"v_mfma_f32_16x16x32_bf16 a[112:115], v[120:123], v[80:83], a[112:115]\n"
"v_exp_f32 v15, v15\n"
"v_mfma_f32_16x16x32_bf16 a[112:115], v[124:127], v[84:87], a[112:115]\n"
"buffer_load_dwordx4 v[248:251], v185, s[44:47], s73 offen offset:128 nt\n"
"v_cvt_pk_bf16_f32 v42, v12, v13\n"
"v_mfma_f32_16x16x32_bf16 v[8:11], v[96:99], a[192:195], v[144:147]\n"
"ds_read_b64_tr_b16 v[120:121], v181 offset:17152\n"
"v_cvt_pk_bf16_f32 v43, v14, v15\n"
"v_mfma_f32_16x16x32_bf16 v[8:11], v[100:103], a[196:199], v[8:11]\n"
"v_exp_f32 v0, v0\n"
"v_mfma_f32_16x16x32_bf16 v[8:11], v[112:115], a[200:203], v[8:11]\n"
"ds_read_b64_tr_b16 v[122:123], v181 offset:21248\n"
"v_cvt_pk_bf16_f32 v199, v188, v199\n"
"v_mfma_f32_16x16x32_bf16 v[8:11], v[116:119], a[204:207], v[8:11]\n"
"v_lshlrev_b32 v186, 16, v200\n"
"s_waitcnt lgkmcnt(3)\n"
"v_mfma_f32_16x16x32_bf16 a[20:23], v[128:131], v[56:59], a[20:23]\n"
"v_exp_f32 v1, v1\n"
"v_mfma_f32_16x16x32_bf16 a[20:23], v[132:135], v[60:63], a[20:23]\n"
"ds_read_b64_tr_b16 v[124:125], v181 offset:25344\n"
"v_cvt_pk_bf16_f32 v20, v0, v1\n"
"v_mfma_f32_16x16x32_bf16 a[52:55], v[128:131], v[64:67], a[52:55]\n"
"v_exp_f32 v2, v2\n"
"v_mfma_f32_16x16x32_bf16 a[52:55], v[132:135], v[68:71], a[52:55]\n"
"v_exp_f32 v3, v3\n"
"v_mfma_f32_16x16x32_bf16 v[12:15], v[112:115], a[216:219], v[148:151]\n"
"ds_read_b128 v[112:115], v180 offset:12800\n"
"v_cvt_pk_bf16_f32 v21, v2, v3\n"
"v_mfma_f32_16x16x32_bf16 v[12:15], v[116:119], a[220:223], v[12:15]\n"
"ds_read_b128 v[116:119], v180 offset:13056\n"
"v_and_b32 v200, 0xffff0000, v200\n"
"v_mfma_f32_16x16x32_bf16 v[12:15], v[96:99], a[208:211], v[12:15]\n"
"ds_read_b64_tr_b16 v[126:127], v181 offset:29440\n"
"v_mul_f32 v186, s56, v186\n"
"v_mfma_f32_16x16x32_bf16 v[12:15], v[100:103], a[212:215], v[12:15]\n"
"buffer_load_dwordx4 v184, s[36:39], s52 offen offset:3072 lds\n"
"v_mul_f32 v200, s56, v200\n"
"v_mfma_f32_16x16x32_bf16 a[84:87], v[128:131], v[72:75], a[84:87]\n"
"v_exp_f32 v4, v4\n"
"v_mfma_f32_16x16x32_bf16 a[84:87], v[132:135], v[76:79], a[84:87]\n"
"v_exp_f32 v5, v5\n"
"v_mfma_f32_16x16x32_bf16 a[116:119], v[128:131], v[80:83], a[116:119]\n"
"v_exp_f32 v6, v6\n"
"v_mfma_f32_16x16x32_bf16 a[116:119], v[132:135], v[84:87], a[116:119]\n"
"v_exp_f32 v7, v7\n"
"v_mfma_f32_16x16x32_bf16 v[164:167], v[176:179], v[72:75], v[164:167]\n"
"ds_read_b64_tr_b16 v[128:129], v182 offset:17152\n"
"v_cvt_pk_bf16_f32 v28, v4, v5\n"
"v_mfma_f32_16x16x32_bf16 v[164:167], v[176:179], v[76:79], v[164:167]\n"
"v_cvt_pk_bf16_f32 v29, v6, v7\n"
"s_waitcnt lgkmcnt(7)\n"
"v_mfma_f32_16x16x32_bf16 v[0:3], v[104:107], a[160:163], v[136:139]\n"
"ds_read_b64_tr_b16 v[130:131], v182 offset:21248\n"
"v_cvt_pk_bf16_f32 v200, v186, v200\n"
"v_mfma_f32_16x16x32_bf16 v[0:3], v[108:111], a[164:167], v[0:3]\n"
"v_lshlrev_b32 v188, 16, v201\n"
"s_waitcnt lgkmcnt(2)\n"
"v_mfma_f32_16x16x32_bf16 v[0:3], v[112:115], a[168:171], v[0:3]\n"
"s_add_u32 s52, s52, 0x4000\n"
"v_and_b32 v201, 0xffff0000, v201\n"
"v_mfma_f32_16x16x32_bf16 v[0:3], v[116:119], a[172:175], v[0:3]\n"
"ds_read_b64_tr_b16 v[132:133], v182 offset:25344\n"
"v_mul_f32 v188, s56, v188\n"
"v_mfma_f32_16x16x32_bf16 a[24:27], v[120:123], v[56:59], a[24:27]\n"
"v_exp_f32 v8, v8\n"
"v_mfma_f32_16x16x32_bf16 a[24:27], v[124:127], v[60:63], a[24:27]\n"
"v_exp_f32 v9, v9\n"
"v_mfma_f32_16x16x32_bf16 a[56:59], v[120:123], v[64:67], a[56:59]\n"
"ds_read_b64_tr_b16 v[134:135], v182 offset:29440\n"
"v_cvt_pk_bf16_f32 v36, v8, v9\n"
"v_mfma_f32_16x16x32_bf16 a[56:59], v[124:127], v[68:71], a[56:59]\n"
"v_exp_f32 v10, v10\n"
"v_mfma_f32_16x16x32_bf16 v[4:7], v[104:107], a[176:179], v[140:143]\n"
"buffer_load_dwordx4 v[252:255], v185, s[44:47], s73 offen offset:192 nt\n"
"v_mul_f32 v201, s56, v201\n"
"v_mfma_f32_16x16x32_bf16 v[4:7], v[108:111], a[180:183], v[4:7]\n"
"v_exp_f32 v11, v11\n"
"v_mfma_f32_16x16x32_bf16 v[4:7], v[112:115], a[184:187], v[4:7]\n"
"v_exp_f32 v12, v12\n"
"v_mfma_f32_16x16x32_bf16 v[4:7], v[116:119], a[188:191], v[4:7]\n"
"v_exp_f32 v13, v13\n"
"v_mfma_f32_16x16x32_bf16 a[88:91], v[120:123], v[72:75], a[88:91]\n"
"v_cvt_pk_bf16_f32 v37, v10, v11\n"
"v_cvt_pk_bf16_f32 v44, v12, v13\n"
"v_mfma_f32_16x16x32_bf16 a[88:91], v[124:127], v[76:79], a[88:91]\n"
"v_exp_f32 v14, v14\n"
"v_mfma_f32_16x16x32_bf16 a[120:123], v[120:123], v[80:83], a[120:123]\n"
"v_exp_f32 v15, v15\n"
"v_mfma_f32_16x16x32_bf16 a[120:123], v[124:127], v[84:87], a[120:123]\n"
"v_exp_f32 v0, v0\n"
"v_mfma_f32_16x16x32_bf16 v[8:11], v[104:107], a[192:195], v[144:147]\n"
"v_exp_f32 v1, v1\n"
"v_mfma_f32_16x16x32_bf16 v[8:11], v[108:111], a[196:199], v[8:11]\n"
"v_cvt_pk_bf16_f32 v45, v14, v15\n"
"v_cvt_pk_bf16_f32 v22, v0, v1\n"
"v_mfma_f32_16x16x32_bf16 v[8:11], v[112:115], a[200:203], v[8:11]\n"
"v_exp_f32 v2, v2\n"
"v_mfma_f32_16x16x32_bf16 v[8:11], v[116:119], a[204:207], v[8:11]\n"
"s_waitcnt vmcnt(12) lgkmcnt(0)\n"
"v_xor_b32 v181, 0x8000, v181\n"
"v_xor_b32 v182, 0x8000, v182\n"
"s_barrier\n"
"v_mfma_f32_16x16x32_bf16 a[28:31], v[128:131], v[56:59], a[28:31]\n"
"ds_read_b128 v[96:99], v180 offset:16384\n"
"v_cvt_pk_bf16_f32 v201, v188, v201\n"
"v_mfma_f32_16x16x32_bf16 a[28:31], v[132:135], v[60:63], a[28:31]\n"
"ds_read_b128 v[100:103], v180 offset:16640\n"
"v_lshlrev_b32 v186, 16, v202\n"
"v_mfma_f32_16x16x32_bf16 a[60:63], v[128:131], v[64:67], a[60:63]\n"
"ds_read_b64_tr_b16 v[120:121], v181 offset:0\n"
"ds_read_b64_tr_b16 v[124:125], v181 offset:8192\n"
"v_mfma_f32_16x16x32_bf16 a[60:63], v[132:135], v[68:71], a[60:63]\n"
"ds_read_b64_tr_b16 v[122:123], v181 offset:4096\n"
"ds_read_b64_tr_b16 v[126:127], v181 offset:12288\n"
"v_mfma_f32_16x16x32_bf16 v[12:15], v[112:115], a[216:219], v[148:151]\n"
"ds_read_b128 v[112:115], v180 offset:16896\n"
"v_and_b32 v202, 0xffff0000, v202\n"
"v_mfma_f32_16x16x32_bf16 v[12:15], v[116:119], a[220:223], v[12:15]\n"
"ds_read_b128 v[116:119], v180 offset:17152\n"
"v_mul_f32 v186, s56, v186\n"
"v_mfma_f32_16x16x32_bf16 v[12:15], v[104:107], a[208:211], v[12:15]\n"
"s_add_u32 m0, s54, 0xc000\n"
"v_mul_f32 v202, s56, v202\n"
"v_mfma_f32_16x16x32_bf16 v[12:15], v[108:111], a[212:215], v[12:15]\n"
"buffer_load_dwordx4 v183, s[40:43], s52 offen offset:0 lds\n"
"v_cvt_pk_bf16_f32 v202, v186, v202\n"
"v_mfma_f32_16x16x32_bf16 a[92:95], v[128:131], v[72:75], a[92:95]\n"
"v_exp_f32 v3, v3\n"
"v_mfma_f32_16x16x32_bf16 a[92:95], v[132:135], v[76:79], a[92:95]\n"
"v_exp_f32 v4, v4\n"
"v_mfma_f32_16x16x32_bf16 a[124:127], v[128:131], v[80:83], a[124:127]\n"
"v_exp_f32 v5, v5\n"
"v_mfma_f32_16x16x32_bf16 a[124:127], v[132:135], v[84:87], a[124:127]\n"
"v_cvt_pk_bf16_f32 v23, v2, v3\n"
"v_cvt_pk_bf16_f32 v30, v4, v5\n"
"v_mfma_f32_16x16x32_bf16 v[168:171], v[176:179], v[80:83], v[168:171]\n"
"v_exp_f32 v6, v6\n"
"v_mfma_f32_16x16x32_bf16 v[168:171], v[176:179], v[84:87], v[168:171]\n"
"v_lshlrev_b32 v188, 16, v203\n"
"v_exp_f32 v7, v7\n"
"v_exp_f32 v8, v8\n"
"v_exp_f32 v9, v9\n"
"v_exp_f32 v10, v10\n"
"v_exp_f32 v11, v11\n"
"s_nop 0\n"
"v_cvt_pk_bf16_f32 v31, v6, v7\n"
"v_cvt_pk_bf16_f32 v38, v8, v9\n"
"v_cvt_pk_bf16_f32 v39, v10, v11\n"
"s_waitcnt lgkmcnt(2)\n"
"v_mfma_f32_16x16x32_bf16 v[0:3], v[96:99], a[160:163], v[136:139]\n"
"v_exp_f32 v12, v12\n"
"v_mfma_f32_16x16x32_bf16 v[0:3], v[100:103], a[164:167], v[0:3]\n"
"v_and_b32 v203, 0xffff0000, v203\n"
"s_waitcnt lgkmcnt(0)\n"
"v_mfma_f32_16x16x32_bf16 v[0:3], v[112:115], a[168:171], v[0:3]\n"
"buffer_load_dwordx4 v184, s[40:43], s52 offen offset:1024 lds\n"
"v_mul_f32 v188, s56, v188\n"
"v_mfma_f32_16x16x32_bf16 v[0:3], v[116:119], a[172:175], v[0:3]\n"
"v_exp_f32 v13, v13\n"
"v_mfma_f32_16x16x32_bf16 a[0:3], v[120:123], v[16:19], a[0:3]\n"
"ds_read_b128 v[104:107], v180 offset:20480\n"
"v_cvt_pk_bf16_f32 v46, v12, v13\n"
"v_mfma_f32_16x16x32_bf16 a[0:3], v[124:127], v[20:23], a[0:3]\n"
"ds_read_b64_tr_b16 v[128:129], v182 offset:0\n"
"v_mul_f32 v203, s56, v203\n"
"v_mfma_f32_16x16x32_bf16 a[32:35], v[120:123], v[24:27], a[32:35]\n"
"ds_read_b64_tr_b16 v[130:131], v182 offset:4096\n"
"v_cvt_pk_bf16_f32 v203, v188, v203\n"
"v_mfma_f32_16x16x32_bf16 a[32:35], v[124:127], v[28:31], a[32:35]\n"
"v_exp_f32 v14, v14\n"
"v_mfma_f32_16x16x32_bf16 v[4:7], v[96:99], a[176:179], v[140:143]\n"
"ds_read_b64_tr_b16 v[132:133], v182 offset:8192\n"
"v_lshlrev_b32 v186, 16, v204\n"
"v_mfma_f32_16x16x32_bf16 v[4:7], v[100:103], a[180:183], v[4:7]\n"
"v_exp_f32 v15, v15\n"
"v_mfma_f32_16x16x32_bf16 v[4:7], v[112:115], a[184:187], v[4:7]\n"
"ds_read_b64_tr_b16 v[134:135], v182 offset:12288\n"
"v_cvt_pk_bf16_f32 v47, v14, v15\n"
"v_mfma_f32_16x16x32_bf16 v[4:7], v[116:119], a[188:191], v[4:7]\n"
"v_exp_f32 v0, v0\n"
"v_mfma_f32_16x16x32_bf16 a[64:67], v[120:123], v[32:35], a[64:67]\n"
"ds_read_b128 v[108:111], v180 offset:20736\n"
"v_and_b32 v204, 0xffff0000, v204\n"
"v_mfma_f32_16x16x32_bf16 a[64:67], v[124:127], v[36:39], a[64:67]\n"
"v_exp_f32 v1, v1\n"
"v_mfma_f32_16x16x32_bf16 a[96:99], v[120:123], v[40:43], a[96:99]\n"
"v_exp_f32 v2, v2\n"
"v_mfma_f32_16x16x32_bf16 a[96:99], v[124:127], v[44:47], a[96:99]\n"
"buffer_load_dwordx4 v183, s[40:43], s52 offen offset:2048 lds\n"
"v_cvt_pk_bf16_f32 v56, v0, v1\n"
"v_mfma_f32_16x16x32_bf16 v[8:11], v[96:99], a[192:195], v[144:147]\n"
"ds_read_b64_tr_b16 v[120:121], v181 offset:256\n"
"v_mul_f32 v186, s56, v186\n"
"v_mfma_f32_16x16x32_bf16 v[8:11], v[100:103], a[196:199], v[8:11]\n"
"v_exp_f32 v3, v3\n"
"v_mfma_f32_16x16x32_bf16 v[8:11], v[112:115], a[200:203], v[8:11]\n"
"ds_read_b64_tr_b16 v[122:123], v181 offset:4352\n"
"v_cvt_pk_bf16_f32 v57, v2, v3\n"
"v_mfma_f32_16x16x32_bf16 v[8:11], v[116:119], a[204:207], v[8:11]\n"
"v_mul_f32 v204, s56, v204\n"
"s_waitcnt lgkmcnt(3)\n"
"v_mfma_f32_16x16x32_bf16 a[4:7], v[128:131], v[16:19], a[4:7]\n"
"v_exp_f32 v4, v4\n"
"v_mfma_f32_16x16x32_bf16 a[4:7], v[132:135], v[20:23], a[4:7]\n"
"ds_read_b64_tr_b16 v[124:125], v181 offset:8448\n"
"v_cvt_pk_bf16_f32 v204, v186, v204\n"
"v_mfma_f32_16x16x32_bf16 a[36:39], v[128:131], v[24:27], a[36:39]\n"
"v_exp_f32 v5, v5\n"
"v_mfma_f32_16x16x32_bf16 a[36:39], v[132:135], v[28:31], a[36:39]\n"
"v_exp_f32 v6, v6\n"
"v_mfma_f32_16x16x32_bf16 v[12:15], v[112:115], a[216:219], v[148:151]\n"
"ds_read_b128 v[112:115], v180 offset:20992\n"
"v_cvt_pk_bf16_f32 v64, v4, v5\n"
"v_mfma_f32_16x16x32_bf16 v[12:15], v[116:119], a[220:223], v[12:15]\n"
"ds_read_b128 v[116:119], v180 offset:21248\n"
"v_lshlrev_b32 v188, 16, v205\n"
"v_mfma_f32_16x16x32_bf16 v[12:15], v[96:99], a[208:211], v[12:15]\n"
"ds_read_b64_tr_b16 v[126:127], v181 offset:12544\n"
"v_and_b32 v205, 0xffff0000, v205\n"
"v_mfma_f32_16x16x32_bf16 v[12:15], v[100:103], a[212:215], v[12:15]\n"
"v_exp_f32 v7, v7\n"
"v_mfma_f32_16x16x32_bf16 a[68:71], v[128:131], v[32:35], a[68:71]\n"
"buffer_load_dwordx4 v184, s[40:43], s52 offen offset:3072 lds\n"
"v_cvt_pk_bf16_f32 v65, v6, v7\n"
"v_mfma_f32_16x16x32_bf16 a[68:71], v[132:135], v[36:39], a[68:71]\n"
"v_exp_f32 v8, v8\n"
"v_mfma_f32_16x16x32_bf16 a[100:103], v[128:131], v[40:43], a[100:103]\n"
"v_exp_f32 v9, v9\n"
"v_mfma_f32_16x16x32_bf16 a[100:103], v[132:135], v[44:47], a[100:103]\n"
"v_exp_f32 v10, v10\n"
"v_mfma_f32_16x16x32_bf16 v[156:159], v[176:179], v[16:19], v[156:159]\n"
"ds_read_b64_tr_b16 v[128:129], v182 offset:256\n"
"v_cvt_pk_bf16_f32 v72, v8, v9\n"
"v_mfma_f32_16x16x32_bf16 v[156:159], v[176:179], v[20:23], v[156:159]\n"
"v_mul_f32 v188, s56, v188\n"
"s_waitcnt lgkmcnt(7)\n"
"v_mfma_f32_16x16x32_bf16 v[0:3], v[104:107], a[160:163], v[136:139]\n"
"ds_read_b64_tr_b16 v[130:131], v182 offset:4352\n"
"v_mul_f32 v205, s56, v205\n"
"v_mfma_f32_16x16x32_bf16 v[0:3], v[108:111], a[164:167], v[0:3]\n"
"v_cvt_pk_bf16_f32 v205, v188, v205\n"
"s_waitcnt lgkmcnt(2)\n"
"v_mfma_f32_16x16x32_bf16 v[0:3], v[112:115], a[168:171], v[0:3]\n"
"v_exp_f32 v11, v11\n"
"v_mfma_f32_16x16x32_bf16 v[0:3], v[116:119], a[172:175], v[0:3]\n"
"ds_read_b64_tr_b16 v[132:133], v182 offset:8448\n"
"v_cvt_pk_bf16_f32 v73, v10, v11\n"
"v_mfma_f32_16x16x32_bf16 a[8:11], v[120:123], v[16:19], a[8:11]\n"
"ds_read_b128 v[96:99], v180 offset:24576\n"
"v_lshlrev_b32 v186, 16, v206\n"
"v_mfma_f32_16x16x32_bf16 a[8:11], v[124:127], v[20:23], a[8:11]\n"
"v_exp_f32 v12, v12\n"
"v_mfma_f32_16x16x32_bf16 a[40:43], v[120:123], v[24:27], a[40:43]\n"
"ds_read_b64_tr_b16 v[134:135], v182 offset:12544\n"
"v_and_b32 v206, 0xffff0000, v206\n"
"v_mfma_f32_16x16x32_bf16 a[40:43], v[124:127], v[28:31], a[40:43]\n"
"s_add_u32 m0, s53, 0x0\n"
"v_mul_f32 v186, s56, v186\n"
"v_mfma_f32_16x16x32_bf16 v[4:7], v[104:107], a[176:179], v[140:143]\n"
"v_exp_f32 v13, v13\n"
"v_mfma_f32_16x16x32_bf16 v[4:7], v[108:111], a[180:183], v[4:7]\n"
"v_exp_f32 v14, v14\n"
"v_mfma_f32_16x16x32_bf16 v[4:7], v[112:115], a[184:187], v[4:7]\n"
"v_exp_f32 v15, v15\n"
"v_mfma_f32_16x16x32_bf16 v[4:7], v[116:119], a[188:191], v[4:7]\n"
"v_cvt_pk_bf16_f32 v80, v12, v13\n"
"v_cvt_pk_bf16_f32 v81, v14, v15\n"
"v_mfma_f32_16x16x32_bf16 a[72:75], v[120:123], v[32:35], a[72:75]\n"
"ds_read_b128 v[100:103], v180 offset:24832\n"
"v_mul_f32 v206, s56, v206\n"
"v_mfma_f32_16x16x32_bf16 a[72:75], v[124:127], v[36:39], a[72:75]\n"
"v_exp_f32 v0, v0\n"
"v_mfma_f32_16x16x32_bf16 a[104:107], v[120:123], v[40:43], a[104:107]\n"
"v_exp_f32 v1, v1\n"
"v_mfma_f32_16x16x32_bf16 a[104:107], v[124:127], v[44:47], a[104:107]\n"
"v_exp_f32 v2, v2\n"
"v_mfma_f32_16x16x32_bf16 v[8:11], v[104:107], a[192:195], v[144:147]\n"
"ds_read_b64_tr_b16 v[120:121], v181 offset:512\n"
"v_cvt_pk_bf16_f32 v58, v0, v1\n"
"v_mfma_f32_16x16x32_bf16 v[8:11], v[108:111], a[196:199], v[8:11]\n"
"v_exp_f32 v3, v3\n"
"v_mfma_f32_16x16x32_bf16 v[8:11], v[112:115], a[200:203], v[8:11]\n"
"ds_read_b64_tr_b16 v[122:123], v181 offset:4608\n"
"v_cvt_pk_bf16_f32 v59, v2, v3\n"
"v_mfma_f32_16x16x32_bf16 v[8:11], v[116:119], a[204:207], v[8:11]\n"
"v_cvt_pk_bf16_f32 v206, v186, v206\n"
"s_waitcnt lgkmcnt(3)\n"
"v_mfma_f32_16x16x32_bf16 a[12:15], v[128:131], v[16:19], a[12:15]\n"
"buffer_load_dwordx4 v183, s[36:39], s52 offen offset:0 lds\n"
"v_lshlrev_b32 v188, 16, v207\n"
"v_mfma_f32_16x16x32_bf16 a[12:15], v[132:135], v[20:23], a[12:15]\n"
"ds_read_b64_tr_b16 v[124:125], v181 offset:8704\n"
"v_and_b32 v207, 0xffff0000, v207\n"
"v_mfma_f32_16x16x32_bf16 a[44:47], v[128:131], v[24:27], a[44:47]\n"
"v_exp_f32 v4, v4\n"
"v_mfma_f32_16x16x32_bf16 a[44:47], v[132:135], v[28:31], a[44:47]\n"
"v_exp_f32 v5, v5\n"
"v_mfma_f32_16x16x32_bf16 v[12:15], v[112:115], a[216:219], v[148:151]\n"
"ds_read_b128 v[112:115], v180 offset:25088\n"
"v_cvt_pk_bf16_f32 v66, v4, v5\n"
"v_mfma_f32_16x16x32_bf16 v[12:15], v[116:119], a[220:223], v[12:15]\n"
"ds_read_b128 v[116:119], v180 offset:25344\n"
"v_mul_f32 v188, s56, v188\n"
"v_mfma_f32_16x16x32_bf16 v[12:15], v[104:107], a[208:211], v[12:15]\n"
"ds_read_b64_tr_b16 v[126:127], v181 offset:12800\n"
"v_mul_f32 v207, s56, v207\n"
"v_mfma_f32_16x16x32_bf16 v[12:15], v[108:111], a[212:215], v[12:15]\n"
"v_exp_f32 v6, v6\n"
"v_mfma_f32_16x16x32_bf16 a[76:79], v[128:131], v[32:35], a[76:79]\n"
"v_exp_f32 v7, v7\n"
"v_mfma_f32_16x16x32_bf16 a[76:79], v[132:135], v[36:39], a[76:79]\n"
"v_exp_f32 v8, v8\n"
"v_mfma_f32_16x16x32_bf16 a[108:111], v[128:131], v[40:43], a[108:111]\n"
"v_exp_f32 v9, v9\n"
"v_mfma_f32_16x16x32_bf16 a[108:111], v[132:135], v[44:47], a[108:111]\n"
"v_cvt_pk_bf16_f32 v67, v6, v7\n"
"v_cvt_pk_bf16_f32 v74, v8, v9\n"
"v_mfma_f32_16x16x32_bf16 v[160:163], v[176:179], v[24:27], v[160:163]\n"
"ds_read_b64_tr_b16 v[128:129], v182 offset:512\n"
"v_cvt_pk_bf16_f32 v207, v188, v207\n"
"v_mfma_f32_16x16x32_bf16 v[160:163], v[176:179], v[28:31], v[160:163]\n"
"v_lshlrev_b32 v186, 16, v208\n"
"s_waitcnt lgkmcnt(7)\n"
"v_mfma_f32_16x16x32_bf16 v[0:3], v[96:99], a[160:163], v[136:139]\n"
"ds_read_b64_tr_b16 v[130:131], v182 offset:4608\n"
"v_and_b32 v208, 0xffff0000, v208\n"
"v_mfma_f32_16x16x32_bf16 v[0:3], v[100:103], a[164:167], v[0:3]\n"
"v_mul_f32 v186, s56, v186\n"
"s_waitcnt lgkmcnt(2)\n"
"v_mfma_f32_16x16x32_bf16 v[0:3], v[112:115], a[168:171], v[0:3]\n"
"buffer_load_dwordx4 v184, s[36:39], s52 offen offset:1024 lds\n"
"v_mul_f32 v208, s56, v208\n"
"v_mfma_f32_16x16x32_bf16 v[0:3], v[116:119], a[172:175], v[0:3]\n"
"ds_read_b64_tr_b16 v[132:133], v182 offset:8704\n"
"v_cvt_pk_bf16_f32 v208, v186, v208\n"
"v_mfma_f32_16x16x32_bf16 a[16:19], v[120:123], v[16:19], a[16:19]\n"
"ds_read_b128 v[104:107], v180 offset:28672\n"
"v_lshlrev_b32 v188, 16, v209\n"
"v_mfma_f32_16x16x32_bf16 a[16:19], v[124:127], v[20:23], a[16:19]\n"
"v_exp_f32 v10, v10\n"
"v_mfma_f32_16x16x32_bf16 a[48:51], v[120:123], v[24:27], a[48:51]\n"
"ds_read_b64_tr_b16 v[134:135], v182 offset:12800\n"
"v_and_b32 v209, 0xffff0000, v209\n"
"v_mfma_f32_16x16x32_bf16 a[48:51], v[124:127], v[28:31], a[48:51]\n"
"v_exp_f32 v11, v11\n"
"v_mfma_f32_16x16x32_bf16 v[4:7], v[96:99], a[176:179], v[140:143]\n"
"v_exp_f32 v12, v12\n"
"v_mfma_f32_16x16x32_bf16 v[4:7], v[100:103], a[180:183], v[4:7]\n"
"v_exp_f32 v13, v13\n"
"v_mfma_f32_16x16x32_bf16 v[4:7], v[112:115], a[184:187], v[4:7]\n"
"v_cvt_pk_bf16_f32 v75, v10, v11\n"
"v_cvt_pk_bf16_f32 v82, v12, v13\n"
"v_mfma_f32_16x16x32_bf16 v[4:7], v[116:119], a[188:191], v[4:7]\n"
"v_exp_f32 v14, v14\n"
"v_mfma_f32_16x16x32_bf16 a[80:83], v[120:123], v[32:35], a[80:83]\n"
"ds_read_b128 v[108:111], v180 offset:28928\n"
"v_mul_f32 v188, s56, v188\n"
"v_mfma_f32_16x16x32_bf16 a[80:83], v[124:127], v[36:39], a[80:83]\n"
"buffer_load_dwordx4 v183, s[36:39], s52 offen offset:2048 lds\n"
"v_mul_f32 v209, s56, v209\n"
"v_mfma_f32_16x16x32_bf16 a[112:115], v[120:123], v[40:43], a[112:115]\n"
"v_exp_f32 v15, v15\n"
"v_mfma_f32_16x16x32_bf16 a[112:115], v[124:127], v[44:47], a[112:115]\n"
"v_exp_f32 v0, v0\n"
"v_mfma_f32_16x16x32_bf16 v[8:11], v[96:99], a[192:195], v[144:147]\n"
"ds_read_b64_tr_b16 v[120:121], v181 offset:768\n"
"v_cvt_pk_bf16_f32 v83, v14, v15\n"
"v_mfma_f32_16x16x32_bf16 v[8:11], v[100:103], a[196:199], v[8:11]\n"
"v_exp_f32 v1, v1\n"
"v_mfma_f32_16x16x32_bf16 v[8:11], v[112:115], a[200:203], v[8:11]\n"
"ds_read_b64_tr_b16 v[122:123], v181 offset:4864\n"
"v_cvt_pk_bf16_f32 v60, v0, v1\n"
"v_mfma_f32_16x16x32_bf16 v[8:11], v[116:119], a[204:207], v[8:11]\n"
"v_cvt_pk_bf16_f32 v209, v188, v209\n"
"s_waitcnt lgkmcnt(3)\n"
"v_mfma_f32_16x16x32_bf16 a[20:23], v[128:131], v[16:19], a[20:23]\n"
"v_exp_f32 v2, v2\n"
"v_mfma_f32_16x16x32_bf16 a[20:23], v[132:135], v[20:23], a[20:23]\n"
"ds_read_b64_tr_b16 v[124:125], v181 offset:8960\n"
"v_lshlrev_b32 v186, 16, v210\n"
"v_mfma_f32_16x16x32_bf16 a[52:55], v[128:131], v[24:27], a[52:55]\n"
"v_exp_f32 v3, v3\n"
"v_mfma_f32_16x16x32_bf16 a[52:55], v[132:135], v[28:31], a[52:55]\n"
"v_exp_f32 v4, v4\n"
"v_mfma_f32_16x16x32_bf16 v[12:15], v[112:115], a[216:219], v[148:151]\n"
"ds_read_b128 v[112:115], v180 offset:29184\n"
"v_cvt_pk_bf16_f32 v61, v2, v3\n"
"v_mfma_f32_16x16x32_bf16 v[12:15], v[116:119], a[220:223], v[12:15]\n"
"ds_read_b128 v[116:119], v180 offset:29440\n"
"v_and_b32 v210, 0xffff0000, v210\n"
"v_mfma_f32_16x16x32_bf16 v[12:15], v[96:99], a[208:211], v[12:15]\n"
"ds_read_b64_tr_b16 v[126:127], v181 offset:13056\n"
"v_mul_f32 v186, s56, v186\n"
"v_mfma_f32_16x16x32_bf16 v[12:15], v[100:103], a[212:215], v[12:15]\n"
"buffer_load_dwordx4 v184, s[36:39], s52 offen offset:3072 lds\n"
"v_mul_f32 v210, s56, v210\n"
"v_mfma_f32_16x16x32_bf16 a[84:87], v[128:131], v[32:35], a[84:87]\n"
"v_exp_f32 v5, v5\n"
"v_mfma_f32_16x16x32_bf16 a[84:87], v[132:135], v[36:39], a[84:87]\n"
"v_exp_f32 v6, v6\n"
"v_mfma_f32_16x16x32_bf16 a[116:119], v[128:131], v[40:43], a[116:119]\n"
"v_exp_f32 v7, v7\n"
"v_mfma_f32_16x16x32_bf16 a[116:119], v[132:135], v[44:47], a[116:119]\n"
"v_cvt_pk_bf16_f32 v68, v4, v5\n"
"v_cvt_pk_bf16_f32 v69, v6, v7\n"
"v_mfma_f32_16x16x32_bf16 v[164:167], v[176:179], v[32:35], v[164:167]\n"
"ds_read_b64_tr_b16 v[128:129], v182 offset:768\n"
"v_cvt_pk_bf16_f32 v210, v186, v210\n"
"v_mfma_f32_16x16x32_bf16 v[164:167], v[176:179], v[36:39], v[164:167]\n"
"v_lshlrev_b32 v188, 16, v211\n"
"s_waitcnt lgkmcnt(7)\n"
"v_mfma_f32_16x16x32_bf16 v[0:3], v[104:107], a[160:163], v[136:139]\n"
"ds_read_b64_tr_b16 v[130:131], v182 offset:4864\n"
"v_and_b32 v211, 0xffff0000, v211\n"
"v_mfma_f32_16x16x32_bf16 v[0:3], v[108:111], a[164:167], v[0:3]\n"
"v_mul_f32 v188, s56, v188\n"
"s_waitcnt lgkmcnt(2)\n"
"v_mfma_f32_16x16x32_bf16 v[0:3], v[112:115], a[168:171], v[0:3]\n"
"v_exp_f32 v8, v8\n"
"v_mfma_f32_16x16x32_bf16 v[0:3], v[116:119], a[172:175], v[0:3]\n"
"ds_read_b64_tr_b16 v[132:133], v182 offset:8960\n"
"v_mul_f32 v211, s56, v211\n"
"v_mfma_f32_16x16x32_bf16 a[24:27], v[120:123], v[16:19], a[24:27]\n"
"s_add_u32 s52, s52, 0x4000\n"
"v_cvt_pk_bf16_f32 v211, v188, v211\n"
"v_mfma_f32_16x16x32_bf16 a[24:27], v[124:127], v[20:23], a[24:27]\n"
"v_exp_f32 v9, v9\n"
"v_mfma_f32_16x16x32_bf16 a[56:59], v[120:123], v[24:27], a[56:59]\n"
"ds_read_b64_tr_b16 v[134:135], v182 offset:13056\n"
"v_cvt_pk_bf16_f32 v76, v8, v9\n"
"v_mfma_f32_16x16x32_bf16 a[56:59], v[124:127], v[28:31], a[56:59]\n"
"v_exp_f32 v10, v10\n"
"v_mfma_f32_16x16x32_bf16 v[4:7], v[104:107], a[176:179], v[140:143]\n"
"v_exp_f32 v11, v11\n"
"v_mfma_f32_16x16x32_bf16 v[4:7], v[108:111], a[180:183], v[4:7]\n"
"v_exp_f32 v12, v12\n"
"v_mfma_f32_16x16x32_bf16 v[4:7], v[112:115], a[184:187], v[4:7]\n"
"v_exp_f32 v13, v13\n"
"v_mfma_f32_16x16x32_bf16 v[4:7], v[116:119], a[188:191], v[4:7]\n"
"v_cvt_pk_bf16_f32 v77, v10, v11\n"
"v_cvt_pk_bf16_f32 v84, v12, v13\n"
"v_mfma_f32_16x16x32_bf16 a[88:91], v[120:123], v[32:35], a[88:91]\n"
"v_exp_f32 v14, v14\n"
"v_mfma_f32_16x16x32_bf16 a[88:91], v[124:127], v[36:39], a[88:91]\n"
"v_exp_f32 v15, v15\n"
"v_mfma_f32_16x16x32_bf16 a[120:123], v[120:123], v[40:43], a[120:123]\n"
"v_exp_f32 v0, v0\n"
"v_mfma_f32_16x16x32_bf16 a[120:123], v[124:127], v[44:47], a[120:123]\n"
"v_exp_f32 v1, v1\n"
"v_mfma_f32_16x16x32_bf16 v[8:11], v[104:107], a[192:195], v[144:147]\n"
"v_cvt_pk_bf16_f32 v85, v14, v15\n"
"v_cvt_pk_bf16_f32 v62, v0, v1\n"
"v_mfma_f32_16x16x32_bf16 v[8:11], v[108:111], a[196:199], v[8:11]\n"
"v_exp_f32 v2, v2\n"
"v_mfma_f32_16x16x32_bf16 v[8:11], v[112:115], a[200:203], v[8:11]\n"
"v_exp_f32 v3, v3\n"
"v_mfma_f32_16x16x32_bf16 v[8:11], v[116:119], a[204:207], v[8:11]\n"
"s_waitcnt vmcnt(16) lgkmcnt(0)\n"
"v_xor_b32 v180, 0x8000, v180\n"
"s_barrier\n"
"v_mfma_f32_16x16x32_bf16 a[28:31], v[128:131], v[16:19], a[28:31]\n"
"ds_read_b128 v[96:99], v180 offset:0\n"
"v_cvt_pk_bf16_f32 v63, v2, v3\n"
"v_mfma_f32_16x16x32_bf16 a[28:31], v[132:135], v[20:23], a[28:31]\n"
"ds_read_b128 v[100:103], v180 offset:256\n"
"v_lshlrev_b32 v186, 16, v212\n"
"v_mfma_f32_16x16x32_bf16 a[60:63], v[128:131], v[24:27], a[60:63]\n"
"ds_read_b64_tr_b16 v[120:121], v181 offset:16384\n"
"ds_read_b64_tr_b16 v[124:125], v181 offset:24576\n"
"v_mfma_f32_16x16x32_bf16 a[60:63], v[132:135], v[28:31], a[60:63]\n"
"ds_read_b64_tr_b16 v[122:123], v181 offset:20480\n"
"ds_read_b64_tr_b16 v[126:127], v181 offset:28672\n"
"v_mfma_f32_16x16x32_bf16 v[12:15], v[112:115], a[216:219], v[148:151]\n"
"ds_read_b128 v[112:115], v180 offset:512\n"
"v_and_b32 v212, 0xffff0000, v212\n"
"v_mfma_f32_16x16x32_bf16 v[12:15], v[116:119], a[220:223], v[12:15]\n"
"ds_read_b128 v[116:119], v180 offset:768\n"
"v_mul_f32 v186, s56, v186\n"
"v_mfma_f32_16x16x32_bf16 v[12:15], v[104:107], a[208:211], v[12:15]\n"
"s_add_u32 m0, s54, 0x0\n"
"v_mul_f32 v212, s56, v212\n"
"v_mfma_f32_16x16x32_bf16 v[12:15], v[108:111], a[212:215], v[12:15]\n"
"buffer_load_dwordx4 v183, s[40:43], s52 offen offset:0 lds\n"
"v_cvt_pk_bf16_f32 v212, v186, v212\n"
"v_mfma_f32_16x16x32_bf16 a[92:95], v[128:131], v[32:35], a[92:95]\n"
"v_exp_f32 v4, v4\n"
"v_mfma_f32_16x16x32_bf16 a[92:95], v[132:135], v[36:39], a[92:95]\n"
"v_exp_f32 v5, v5\n"
"v_mfma_f32_16x16x32_bf16 a[124:127], v[128:131], v[40:43], a[124:127]\n"
"v_exp_f32 v6, v6\n"
"v_mfma_f32_16x16x32_bf16 a[124:127], v[132:135], v[44:47], a[124:127]\n"
"v_exp_f32 v7, v7\n"
"v_mfma_f32_16x16x32_bf16 v[168:171], v[176:179], v[40:43], v[168:171]\n"
"v_cvt_pk_bf16_f32 v70, v4, v5\n"
"v_cvt_pk_bf16_f32 v71, v6, v7\n"
"v_mfma_f32_16x16x32_bf16 v[168:171], v[176:179], v[44:47], v[168:171]\n"
"v_lshlrev_b32 v188, 16, v213\n"
"v_exp_f32 v8, v8\n"
"v_exp_f32 v9, v9\n"
"v_exp_f32 v10, v10\n"
"v_exp_f32 v11, v11\n"
"s_nop 0\n"
"v_cvt_pk_bf16_f32 v78, v8, v9\n"
"v_cvt_pk_bf16_f32 v79, v10, v11\n"
"s_waitcnt lgkmcnt(2)\n"
"s_mov_b32 s55, 3\n"
"s_branch L_loop_b_%=\n"
"L_seam_b_%=:\n"
"s_cmp_lg_u32 s55, 0\n"
"s_cbranch_scc1 L_final_b_%=\n"
"s_nop 3\n"
"v_exp_f32 v12, v12\n"
"v_exp_f32 v13, v13\n"
"v_exp_f32 v14, v14\n"
"v_exp_f32 v15, v15\n"
"s_nop 0\n"
"v_cvt_pk_bf16_f32 v86, v12, v13\n"
"v_cvt_pk_bf16_f32 v87, v14, v15\n"
"s_nop 1\n"
"v_mfma_f32_16x16x32_bf16 a[0:3], v[120:123], v[56:59], a[0:3]\n"
"v_and_b32 v213, 0xffff0000, v213\n"
"v_mul_f32 v188, s56, v188\n"
"v_mfma_f32_16x16x32_bf16 a[0:3], v[124:127], v[60:63], a[0:3]\n"
"ds_read_b64_tr_b16 v[128:129], v182 offset:16384\n"
"v_mul_f32 v213, s56, v213\n"
"v_mfma_f32_16x16x32_bf16 a[32:35], v[120:123], v[64:67], a[32:35]\n"
"ds_read_b64_tr_b16 v[130:131], v182 offset:20480\n"
"v_cvt_pk_bf16_f32 v213, v188, v213\n"
"v_mfma_f32_16x16x32_bf16 a[32:35], v[124:127], v[68:71], a[32:35]\n"
"ds_read_b64_tr_b16 v[132:133], v182 offset:24576\n"
"v_accvgpr_write_b32 a160, v192\n"
"v_mfma_f32_16x16x32_bf16 a[64:67], v[120:123], v[72:75], a[64:67]\n"
"ds_read_b64_tr_b16 v[134:135], v182 offset:28672\n"
"v_accvgpr_write_b32 a161, v193\n"
"v_mfma_f32_16x16x32_bf16 a[64:67], v[124:127], v[76:79], a[64:67]\n"
"buffer_load_dwordx4 v184, s[40:43], s52 offen offset:1024 lds\n"
"v_accvgpr_write_b32 a162, v194\n"
"v_mfma_f32_16x16x32_bf16 a[96:99], v[120:123], v[80:83], a[96:99]\n"
"v_accvgpr_write_b32 a163, v195\n"
"v_accvgpr_write_b32 a164, v196\n"
"v_mfma_f32_16x16x32_bf16 a[96:99], v[124:127], v[84:87], a[96:99]\n"
"v_accvgpr_write_b32 a165, v197\n"
"s_waitcnt lgkmcnt(0)\n"
"v_mfma_f32_16x16x32_bf16 a[4:7], v[128:131], v[56:59], a[4:7]\n"
"ds_read_b64_tr_b16 v[120:121], v181 offset:16640\n"
"v_accvgpr_write_b32 a166, v198\n"
"v_mfma_f32_16x16x32_bf16 a[4:7], v[132:135], v[60:63], a[4:7]\n"
"ds_read_b64_tr_b16 v[122:123], v181 offset:20736\n"
"v_accvgpr_write_b32 a167, v199\n"
"v_mfma_f32_16x16x32_bf16 a[36:39], v[128:131], v[64:67], a[36:39]\n"
"ds_read_b64_tr_b16 v[124:125], v181 offset:24832\n"
"v_accvgpr_write_b32 a168, v200\n"
"v_mfma_f32_16x16x32_bf16 a[36:39], v[132:135], v[68:71], a[36:39]\n"
"ds_read_b64_tr_b16 v[126:127], v181 offset:28928\n"
"v_accvgpr_write_b32 a169, v201\n"
"v_mfma_f32_16x16x32_bf16 a[68:71], v[128:131], v[72:75], a[68:71]\n"
"buffer_load_dwordx4 v183, s[40:43], s52 offen offset:2048 lds\n"
"v_accvgpr_write_b32 a170, v202\n"
"v_mfma_f32_16x16x32_bf16 a[68:71], v[132:135], v[76:79], a[68:71]\n"
"v_accvgpr_write_b32 a171, v203\n"
"v_accvgpr_write_b32 a172, v204\n"
"v_mfma_f32_16x16x32_bf16 a[100:103], v[128:131], v[80:83], a[100:103]\n"
"v_accvgpr_write_b32 a173, v205\n"
"v_accvgpr_write_b32 a174, v206\n"
"v_mfma_f32_16x16x32_bf16 a[100:103], v[132:135], v[84:87], a[100:103]\n"
"v_accvgpr_write_b32 a175, v207\n"
"v_accvgpr_write_b32 a176, v208\n"
"v_mfma_f32_16x16x32_bf16 v[156:159], v[176:179], v[56:59], v[156:159]\n"
"ds_read_b64_tr_b16 v[128:129], v182 offset:16640\n"
"v_accvgpr_write_b32 a177, v209\n"
"v_mfma_f32_16x16x32_bf16 v[156:159], v[176:179], v[60:63], v[156:159]\n"
"v_accvgpr_write_b32 a178, v210\n"
"s_waitcnt lgkmcnt(1)\n"
"v_mfma_f32_16x16x32_bf16 a[8:11], v[120:123], v[56:59], a[8:11]\n"
"ds_read_b64_tr_b16 v[130:131], v182 offset:20736\n"
"v_accvgpr_write_b32 a179, v211\n"
"v_mfma_f32_16x16x32_bf16 a[8:11], v[124:127], v[60:63], a[8:11]\n"
"ds_read_b64_tr_b16 v[132:133], v182 offset:24832\n"
"v_accvgpr_write_b32 a180, v212\n"
"v_mfma_f32_16x16x32_bf16 a[40:43], v[120:123], v[64:67], a[40:43]\n"
"ds_read_b64_tr_b16 v[134:135], v182 offset:28928\n"
"v_accvgpr_write_b32 a181, v213\n"
"v_mfma_f32_16x16x32_bf16 a[40:43], v[124:127], v[68:71], a[40:43]\n"
"buffer_load_dwordx4 v184, s[40:43], s52 offen offset:3072 lds\n"
"v_lshlrev_b32 v0, 16, v214\n"
"v_mfma_f32_16x16x32_bf16 a[72:75], v[120:123], v[72:75], a[72:75]\n"
"v_and_b32 v1, 0xffff0000, v214\n"
"v_mul_f32 v0, s56, v0\n"
"v_mfma_f32_16x16x32_bf16 a[72:75], v[124:127], v[76:79], a[72:75]\n"
"v_mul_f32 v1, s56, v1\n"
"v_cvt_pk_bf16_f32 v2, v0, v1\n"
"v_mfma_f32_16x16x32_bf16 a[104:107], v[120:123], v[80:83], a[104:107]\n"
"s_add_u32 m0, s53, 0xc000\n"
"v_accvgpr_write_b32 a182, v2\n"
"v_mfma_f32_16x16x32_bf16 a[104:107], v[124:127], v[84:87], a[104:107]\n"
"v_lshlrev_b32 v4, 16, v215\n"
"s_waitcnt lgkmcnt(0)\n"
"v_mfma_f32_16x16x32_bf16 a[12:15], v[128:131], v[56:59], a[12:15]\n"
"ds_read_b64_tr_b16 v[120:121], v181 offset:16896\n"
"v_and_b32 v5, 0xffff0000, v215\n"
"v_mfma_f32_16x16x32_bf16 a[12:15], v[132:135], v[60:63], a[12:15]\n"
"ds_read_b64_tr_b16 v[122:123], v181 offset:20992\n"
"v_mul_f32 v4, s56, v4\n"
"v_mfma_f32_16x16x32_bf16 a[44:47], v[128:131], v[64:67], a[44:47]\n"
"ds_read_b64_tr_b16 v[124:125], v181 offset:25088\n"
"v_mul_f32 v5, s56, v5\n"
"v_mfma_f32_16x16x32_bf16 a[44:47], v[132:135], v[68:71], a[44:47]\n"
"ds_read_b64_tr_b16 v[126:127], v181 offset:29184\n"
"v_cvt_pk_bf16_f32 v6, v4, v5\n"
"v_mfma_f32_16x16x32_bf16 a[76:79], v[128:131], v[72:75], a[76:79]\n"
"v_accvgpr_write_b32 a183, v6\n"
"v_lshlrev_b32 v0, 16, v216\n"
"v_mfma_f32_16x16x32_bf16 a[76:79], v[132:135], v[76:79], a[76:79]\n"
"buffer_load_dwordx4 v183, s[36:39], s52 offen offset:0 lds\n"
"v_and_b32 v1, 0xffff0000, v216\n"
"v_mfma_f32_16x16x32_bf16 a[108:111], v[128:131], v[80:83], a[108:111]\n"
"v_mul_f32 v0, s56, v0\n"
"v_mul_f32 v1, s56, v1\n"
"v_mfma_f32_16x16x32_bf16 a[108:111], v[132:135], v[84:87], a[108:111]\n"
"v_cvt_pk_bf16_f32 v2, v0, v1\n"
"v_accvgpr_write_b32 a184, v2\n"
"v_mfma_f32_16x16x32_bf16 v[160:163], v[176:179], v[64:67], v[160:163]\n"
"ds_read_b64_tr_b16 v[128:129], v182 offset:16896\n"
"v_lshlrev_b32 v4, 16, v217\n"
"v_mfma_f32_16x16x32_bf16 v[160:163], v[176:179], v[68:71], v[160:163]\n"
"v_and_b32 v5, 0xffff0000, v217\n"
"s_waitcnt lgkmcnt(1)\n"
"v_mfma_f32_16x16x32_bf16 a[16:19], v[120:123], v[56:59], a[16:19]\n"
"ds_read_b64_tr_b16 v[130:131], v182 offset:20992\n"
"v_mul_f32 v4, s56, v4\n"
"v_mfma_f32_16x16x32_bf16 a[16:19], v[124:127], v[60:63], a[16:19]\n"
"ds_read_b64_tr_b16 v[132:133], v182 offset:25088\n"
"v_mul_f32 v5, s56, v5\n"
"v_mfma_f32_16x16x32_bf16 a[48:51], v[120:123], v[64:67], a[48:51]\n"
"ds_read_b64_tr_b16 v[134:135], v182 offset:29184\n"
"v_cvt_pk_bf16_f32 v6, v4, v5\n"
"v_mfma_f32_16x16x32_bf16 a[48:51], v[124:127], v[68:71], a[48:51]\n"
"buffer_load_dwordx4 v184, s[36:39], s52 offen offset:1024 lds\n"
"v_accvgpr_write_b32 a185, v6\n"
"v_mfma_f32_16x16x32_bf16 a[80:83], v[120:123], v[72:75], a[80:83]\n"
"v_lshlrev_b32 v0, 16, v218\n"
"v_and_b32 v1, 0xffff0000, v218\n"
"v_mfma_f32_16x16x32_bf16 a[80:83], v[124:127], v[76:79], a[80:83]\n"
"v_mul_f32 v0, s56, v0\n"
"v_mul_f32 v1, s56, v1\n"
"v_mfma_f32_16x16x32_bf16 a[112:115], v[120:123], v[80:83], a[112:115]\n"
"v_cvt_pk_bf16_f32 v2, v0, v1\n"
"v_accvgpr_write_b32 a186, v2\n"
"v_mfma_f32_16x16x32_bf16 a[112:115], v[124:127], v[84:87], a[112:115]\n"
"v_lshlrev_b32 v4, 16, v219\n"
"s_waitcnt lgkmcnt(0)\n"
"v_mfma_f32_16x16x32_bf16 a[20:23], v[128:131], v[56:59], a[20:23]\n"
"ds_read_b64_tr_b16 v[120:121], v181 offset:17152\n"
"v_and_b32 v5, 0xffff0000, v219\n"
"v_mfma_f32_16x16x32_bf16 a[20:23], v[132:135], v[60:63], a[20:23]\n"
"ds_read_b64_tr_b16 v[122:123], v181 offset:21248\n"
"v_mul_f32 v4, s56, v4\n"
"v_mfma_f32_16x16x32_bf16 a[52:55], v[128:131], v[64:67], a[52:55]\n"
"ds_read_b64_tr_b16 v[124:125], v181 offset:25344\n"
"v_mul_f32 v5, s56, v5\n"
"v_mfma_f32_16x16x32_bf16 a[52:55], v[132:135], v[68:71], a[52:55]\n"
"ds_read_b64_tr_b16 v[126:127], v181 offset:29440\n"
"v_cvt_pk_bf16_f32 v6, v4, v5\n"
"v_mfma_f32_16x16x32_bf16 a[84:87], v[128:131], v[72:75], a[84:87]\n"
"buffer_load_dwordx4 v183, s[36:39], s52 offen offset:2048 lds\n"
"v_accvgpr_write_b32 a187, v6\n"
"v_mfma_f32_16x16x32_bf16 a[84:87], v[132:135], v[76:79], a[84:87]\n"
"v_lshlrev_b32 v0, 16, v220\n"
"v_and_b32 v1, 0xffff0000, v220\n"
"v_mfma_f32_16x16x32_bf16 a[116:119], v[128:131], v[80:83], a[116:119]\n"
"v_mul_f32 v0, s56, v0\n"
"v_mul_f32 v1, s56, v1\n"
"v_mfma_f32_16x16x32_bf16 a[116:119], v[132:135], v[84:87], a[116:119]\n"
"v_cvt_pk_bf16_f32 v2, v0, v1\n"
"v_accvgpr_write_b32 a188, v2\n"
"v_mfma_f32_16x16x32_bf16 v[164:167], v[176:179], v[72:75], v[164:167]\n"
"ds_read_b64_tr_b16 v[128:129], v182 offset:17152\n"
"v_lshlrev_b32 v4, 16, v221\n"
"v_mfma_f32_16x16x32_bf16 v[164:167], v[176:179], v[76:79], v[164:167]\n"
"v_and_b32 v5, 0xffff0000, v221\n"
"s_waitcnt lgkmcnt(1)\n"
"v_mfma_f32_16x16x32_bf16 a[24:27], v[120:123], v[56:59], a[24:27]\n"
"ds_read_b64_tr_b16 v[130:131], v182 offset:21248\n"
"v_mul_f32 v4, s56, v4\n"
"v_mfma_f32_16x16x32_bf16 a[24:27], v[124:127], v[60:63], a[24:27]\n"
"ds_read_b64_tr_b16 v[132:133], v182 offset:25344\n"
"v_mul_f32 v5, s56, v5\n"
"v_mfma_f32_16x16x32_bf16 a[56:59], v[120:123], v[64:67], a[56:59]\n"
"ds_read_b64_tr_b16 v[134:135], v182 offset:29440\n"
"v_cvt_pk_bf16_f32 v6, v4, v5\n"
"v_mfma_f32_16x16x32_bf16 a[56:59], v[124:127], v[68:71], a[56:59]\n"
"buffer_load_dwordx4 v184, s[36:39], s52 offen offset:3072 lds\n"
"v_accvgpr_write_b32 a189, v6\n"
"v_mfma_f32_16x16x32_bf16 a[88:91], v[120:123], v[72:75], a[88:91]\n"
"v_lshlrev_b32 v0, 16, v222\n"
"v_and_b32 v1, 0xffff0000, v222\n"
"v_mfma_f32_16x16x32_bf16 a[88:91], v[124:127], v[76:79], a[88:91]\n"
"v_mul_f32 v0, s56, v0\n"
"v_mul_f32 v1, s56, v1\n"
"v_mfma_f32_16x16x32_bf16 a[120:123], v[120:123], v[80:83], a[120:123]\n"
"v_cvt_pk_bf16_f32 v2, v0, v1\n"
"v_accvgpr_write_b32 a190, v2\n"
"v_mfma_f32_16x16x32_bf16 a[120:123], v[124:127], v[84:87], a[120:123]\n"
"v_lshlrev_b32 v4, 16, v223\n"
"s_waitcnt lgkmcnt(0)\n"
"v_mfma_f32_16x16x32_bf16 a[28:31], v[128:131], v[56:59], a[28:31]\n"
"s_add_u32 s52, s52, 0x4000\n"
"v_and_b32 v5, 0xffff0000, v223\n"
"v_mfma_f32_16x16x32_bf16 a[28:31], v[132:135], v[60:63], a[28:31]\n"
"v_mul_f32 v4, s56, v4\n"
"v_mul_f32 v5, s56, v5\n"
"v_mfma_f32_16x16x32_bf16 a[60:63], v[128:131], v[64:67], a[60:63]\n"
"v_cvt_pk_bf16_f32 v6, v4, v5\n"
"v_accvgpr_write_b32 a191, v6\n"
"v_mfma_f32_16x16x32_bf16 a[60:63], v[132:135], v[68:71], a[60:63]\n"
"v_lshlrev_b32 v0, 16, v224\n"
"v_and_b32 v1, 0xffff0000, v224\n"
"v_mfma_f32_16x16x32_bf16 a[92:95], v[128:131], v[72:75], a[92:95]\n"
"v_mul_f32 v0, s56, v0\n"
"v_mul_f32 v1, s56, v1\n"
"v_mfma_f32_16x16x32_bf16 a[92:95], v[132:135], v[76:79], a[92:95]\n"
"v_cvt_pk_bf16_f32 v2, v0, v1\n"
"v_accvgpr_write_b32 a192, v2\n"
"v_mfma_f32_16x16x32_bf16 a[124:127], v[128:131], v[80:83], a[124:127]\n"
"v_mfma_f32_16x16x32_bf16 a[124:127], v[132:135], v[84:87], a[124:127]\n"
"v_mfma_f32_16x16x32_bf16 v[168:171], v[176:179], v[80:83], v[168:171]\n"
"v_mfma_f32_16x16x32_bf16 v[168:171], v[176:179], v[84:87], v[168:171]\n"
"v_lshlrev_b32 v0, 16, v225\n"
"v_and_b32 v1, 0xffff0000, v225\n"
"v_pk_mul_f32 v[0:1], v[0:1], s[56:57]\n"
"v_cvt_pk_bf16_f32 v2, v0, v1\n"
"v_accvgpr_write_b32 a193, v2\n"
"v_lshlrev_b32 v4, 16, v226\n"
"v_and_b32 v5, 0xffff0000, v226\n"
"v_pk_mul_f32 v[4:5], v[4:5], s[56:57]\n"
"v_cvt_pk_bf16_f32 v6, v4, v5\n"
"v_accvgpr_write_b32 a194, v6\n"
"v_lshlrev_b32 v0, 16, v227\n"
"v_and_b32 v1, 0xffff0000, v227\n"
"v_pk_mul_f32 v[0:1], v[0:1], s[56:57]\n"
"v_cvt_pk_bf16_f32 v2, v0, v1\n"
"v_accvgpr_write_b32 a195, v2\n"
"v_lshlrev_b32 v4, 16, v228\n"
"v_and_b32 v5, 0xffff0000, v228\n"
"v_pk_mul_f32 v[4:5], v[4:5], s[56:57]\n"
"v_cvt_pk_bf16_f32 v6, v4, v5\n"
"v_accvgpr_write_b32 a196, v6\n"
"v_lshlrev_b32 v0, 16, v229\n"
"v_and_b32 v1, 0xffff0000, v229\n"
"v_pk_mul_f32 v[0:1], v[0:1], s[56:57]\n"
"v_cvt_pk_bf16_f32 v2, v0, v1\n"
"v_accvgpr_write_b32 a197, v2\n"
"v_lshlrev_b32 v4, 16, v230\n"
"v_and_b32 v5, 0xffff0000, v230\n"
"v_pk_mul_f32 v[4:5], v[4:5], s[56:57]\n"
"v_cvt_pk_bf16_f32 v6, v4, v5\n"
"v_accvgpr_write_b32 a198, v6\n"
"v_lshlrev_b32 v0, 16, v231\n"
"v_and_b32 v1, 0xffff0000, v231\n"
"v_pk_mul_f32 v[0:1], v[0:1], s[56:57]\n"
"v_cvt_pk_bf16_f32 v2, v0, v1\n"
"v_accvgpr_write_b32 a199, v2\n"
"v_lshlrev_b32 v4, 16, v232\n"
"v_and_b32 v5, 0xffff0000, v232\n"
"v_pk_mul_f32 v[4:5], v[4:5], s[56:57]\n"
"v_cvt_pk_bf16_f32 v6, v4, v5\n"
"v_accvgpr_write_b32 a200, v6\n"
"v_lshlrev_b32 v0, 16, v233\n"
"v_and_b32 v1, 0xffff0000, v233\n"
"v_pk_mul_f32 v[0:1], v[0:1], s[56:57]\n"
"v_cvt_pk_bf16_f32 v2, v0, v1\n"
"v_accvgpr_write_b32 a201, v2\n"
"v_lshlrev_b32 v4, 16, v234\n"
"v_and_b32 v5, 0xffff0000, v234\n"
"v_pk_mul_f32 v[4:5], v[4:5], s[56:57]\n"
"v_cvt_pk_bf16_f32 v6, v4, v5\n"
"v_accvgpr_write_b32 a202, v6\n"
"v_lshlrev_b32 v0, 16, v235\n"
"v_and_b32 v1, 0xffff0000, v235\n"
"v_pk_mul_f32 v[0:1], v[0:1], s[56:57]\n"
"v_cvt_pk_bf16_f32 v2, v0, v1\n"
"v_accvgpr_write_b32 a203, v2\n"
"v_lshlrev_b32 v4, 16, v236\n"
"v_and_b32 v5, 0xffff0000, v236\n"
"v_pk_mul_f32 v[4:5], v[4:5], s[56:57]\n"
"v_cvt_pk_bf16_f32 v6, v4, v5\n"
"v_accvgpr_write_b32 a204, v6\n"
"v_lshlrev_b32 v0, 16, v237\n"
"v_and_b32 v1, 0xffff0000, v237\n"
"v_pk_mul_f32 v[0:1], v[0:1], s[56:57]\n"
"v_cvt_pk_bf16_f32 v2, v0, v1\n"
"v_accvgpr_write_b32 a205, v2\n"
"v_lshlrev_b32 v4, 16, v238\n"
"v_and_b32 v5, 0xffff0000, v238\n"
"v_pk_mul_f32 v[4:5], v[4:5], s[56:57]\n"
"v_cvt_pk_bf16_f32 v6, v4, v5\n"
"v_accvgpr_write_b32 a206, v6\n"
"v_lshlrev_b32 v0, 16, v239\n"
"v_and_b32 v1, 0xffff0000, v239\n"
"v_pk_mul_f32 v[0:1], v[0:1], s[56:57]\n"
"v_cvt_pk_bf16_f32 v2, v0, v1\n"
"v_accvgpr_write_b32 a207, v2\n"
"v_lshlrev_b32 v4, 16, v240\n"
"v_and_b32 v5, 0xffff0000, v240\n"
"v_pk_mul_f32 v[4:5], v[4:5], s[56:57]\n"
"v_cvt_pk_bf16_f32 v6, v4, v5\n"
"v_accvgpr_write_b32 a208, v6\n"
"v_lshlrev_b32 v0, 16, v241\n"
"v_and_b32 v1, 0xffff0000, v241\n"
"v_pk_mul_f32 v[0:1], v[0:1], s[56:57]\n"
"v_cvt_pk_bf16_f32 v2, v0, v1\n"
"v_accvgpr_write_b32 a209, v2\n"
"v_lshlrev_b32 v4, 16, v242\n"
"v_and_b32 v5, 0xffff0000, v242\n"
"v_pk_mul_f32 v[4:5], v[4:5], s[56:57]\n"
"v_cvt_pk_bf16_f32 v6, v4, v5\n"
"v_accvgpr_write_b32 a210, v6\n"
"v_lshlrev_b32 v0, 16, v243\n"
"v_and_b32 v1, 0xffff0000, v243\n"
"v_pk_mul_f32 v[0:1], v[0:1], s[56:57]\n"
"v_cvt_pk_bf16_f32 v2, v0, v1\n"
"v_accvgpr_write_b32 a211, v2\n"
"v_lshlrev_b32 v4, 16, v244\n"
"v_and_b32 v5, 0xffff0000, v244\n"
"v_pk_mul_f32 v[4:5], v[4:5], s[56:57]\n"
"v_cvt_pk_bf16_f32 v6, v4, v5\n"
"v_accvgpr_write_b32 a212, v6\n"
"v_lshlrev_b32 v0, 16, v245\n"
"v_and_b32 v1, 0xffff0000, v245\n"
"v_pk_mul_f32 v[0:1], v[0:1], s[56:57]\n"
"v_cvt_pk_bf16_f32 v2, v0, v1\n"
"v_accvgpr_write_b32 a213, v2\n"
"v_lshlrev_b32 v4, 16, v246\n"
"v_and_b32 v5, 0xffff0000, v246\n"
"v_pk_mul_f32 v[4:5], v[4:5], s[56:57]\n"
"v_cvt_pk_bf16_f32 v6, v4, v5\n"
"v_accvgpr_write_b32 a214, v6\n"
"v_lshlrev_b32 v0, 16, v247\n"
"v_and_b32 v1, 0xffff0000, v247\n"
"v_pk_mul_f32 v[0:1], v[0:1], s[56:57]\n"
"v_cvt_pk_bf16_f32 v2, v0, v1\n"
"v_accvgpr_write_b32 a215, v2\n"
"v_lshlrev_b32 v4, 16, v248\n"
"v_and_b32 v5, 0xffff0000, v248\n"
"v_pk_mul_f32 v[4:5], v[4:5], s[56:57]\n"
"v_cvt_pk_bf16_f32 v6, v4, v5\n"
"v_accvgpr_write_b32 a216, v6\n"
"v_lshlrev_b32 v0, 16, v249\n"
"v_and_b32 v1, 0xffff0000, v249\n"
"v_pk_mul_f32 v[0:1], v[0:1], s[56:57]\n"
"v_cvt_pk_bf16_f32 v2, v0, v1\n"
"v_accvgpr_write_b32 a217, v2\n"
"v_lshlrev_b32 v4, 16, v250\n"
"v_and_b32 v5, 0xffff0000, v250\n"
"v_pk_mul_f32 v[4:5], v[4:5], s[56:57]\n"
"v_cvt_pk_bf16_f32 v6, v4, v5\n"
"v_accvgpr_write_b32 a218, v6\n"
"v_lshlrev_b32 v0, 16, v251\n"
"v_and_b32 v1, 0xffff0000, v251\n"
"v_pk_mul_f32 v[0:1], v[0:1], s[56:57]\n"
"v_cvt_pk_bf16_f32 v2, v0, v1\n"
"v_accvgpr_write_b32 a219, v2\n"
"v_lshlrev_b32 v4, 16, v252\n"
"v_and_b32 v5, 0xffff0000, v252\n"
"v_pk_mul_f32 v[4:5], v[4:5], s[56:57]\n"
"v_cvt_pk_bf16_f32 v6, v4, v5\n"
"v_accvgpr_write_b32 a220, v6\n"
"v_lshlrev_b32 v0, 16, v253\n"
"v_and_b32 v1, 0xffff0000, v253\n"
"v_pk_mul_f32 v[0:1], v[0:1], s[56:57]\n"
"v_cvt_pk_bf16_f32 v2, v0, v1\n"
"v_accvgpr_write_b32 a221, v2\n"
"v_lshlrev_b32 v4, 16, v254\n"
"v_and_b32 v5, 0xffff0000, v254\n"
"v_pk_mul_f32 v[4:5], v[4:5], s[56:57]\n"
"v_cvt_pk_bf16_f32 v6, v4, v5\n"
"v_accvgpr_write_b32 a222, v6\n"
"v_lshlrev_b32 v0, 16, v255\n"
"v_and_b32 v1, 0xffff0000, v255\n"
"v_pk_mul_f32 v[0:1], v[0:1], s[56:57]\n"
"v_cvt_pk_bf16_f32 v2, v0, v1\n"
"v_accvgpr_write_b32 a223, v2\n"
"v_mbcnt_lo_u32_b32 v0, -1, 0\n"
"v_mbcnt_hi_u32_b32 v0, -1, v0\n"
"v_and_b32 v1, 15, v0\n"
"v_lshrrev_b32 v2, 4, v0\n"
"v_and_b32 v3, 1, v2\n"
"v_lshrrev_b32 v4, 1, v2\n"
"v_lshl_add_u32 v3, v3, 1, v4\n"
"v_mov_b32 v7, 1.0\n"
"s_nop 1\n"
"v_mfma_f32_16x16x4_f32 v[136:139], v7, v156, 0\n"
"v_mfma_f32_16x16x4_f32 v[140:143], v7, v160, 0\n"
"v_mfma_f32_16x16x4_f32 v[144:147], v7, v164, 0\n"
"v_mfma_f32_16x16x4_f32 v[148:151], v7, v168, 0\n"
"v_mov_b32 v6, v156\n"
"v_add_f32 v6, v6, v160\n"
"v_add_f32 v6, v6, v164\n"
"v_add_f32 v6, v6, v168\n"
"v_cmp_ngt_f32 vcc, s65, v6\n"
"s_or_b64 s[60:61], s[60:61], vcc\n"
"v_lshlrev_b32 v8, 4, v3\n"
"v_lshl_add_u32 v185, v1, 8, v8\n"
"s_nop 4\n"
"v_rcp_f32 v136, v136\n"
"v_rcp_f32 v140, v140\n"
"v_rcp_f32 v144, v144\n"
"v_rcp_f32 v148, v148\n"
"v_accvgpr_read_b32 v16, a0\n"
"v_accvgpr_read_b32 v17, a1\n"
"v_accvgpr_read_b32 v18, a2\n"
"v_accvgpr_read_b32 v19, a3\n"
"v_accvgpr_read_b32 v20, a4\n"
"v_accvgpr_read_b32 v21, a5\n"
"v_accvgpr_read_b32 v22, a6\n"
"v_accvgpr_read_b32 v23, a7\n"
"v_pk_mul_f32 v[16:17], v[16:17], v[136:137] op_sel_hi:[1,0]\n"
"v_pk_mul_f32 v[18:19], v[18:19], v[136:137] op_sel_hi:[1,0]\n"
"v_pk_mul_f32 v[20:21], v[20:21], v[136:137] op_sel_hi:[1,0]\n"
"v_pk_mul_f32 v[22:23], v[22:23], v[136:137] op_sel_hi:[1,0]\n"
"v_cvt_pk_bf16_f32 v192, v16, v17\n"
"v_cvt_pk_bf16_f32 v193, v18, v19\n"
"v_cvt_pk_bf16_f32 v194, v20, v21\n"
"v_cvt_pk_bf16_f32 v195, v22, v23\n"
"v_accvgpr_read_b32 v24, a8\n"
"v_accvgpr_read_b32 v25, a9\n"
"v_accvgpr_read_b32 v26, a10\n"
"v_permlane16_swap_b32 v192, v194\n"
"v_permlane16_swap_b32 v193, v195\n"
"v_accvgpr_read_b32 v27, a11\n"
"v_accvgpr_read_b32 v28, a12\n"
"v_accvgpr_read_b32 v29, a13\n"
"v_accvgpr_read_b32 v30, a14\n"
"v_accvgpr_read_b32 v31, a15\n"
"v_pk_mul_f32 v[24:25], v[24:25], v[136:137] op_sel_hi:[1,0]\n"
"v_pk_mul_f32 v[26:27], v[26:27], v[136:137] op_sel_hi:[1,0]\n"
"v_pk_mul_f32 v[28:29], v[28:29], v[136:137] op_sel_hi:[1,0]\n"
"v_pk_mul_f32 v[30:31], v[30:31], v[136:137] op_sel_hi:[1,0]\n"
"v_cvt_pk_bf16_f32 v196, v24, v25\n"
"v_cvt_pk_bf16_f32 v197, v26, v27\n"
"v_cvt_pk_bf16_f32 v198, v28, v29\n"
"v_cvt_pk_bf16_f32 v199, v30, v31\n"
"v_accvgpr_read_b32 v16, a16\n"
"v_accvgpr_read_b32 v17, a17\n"
"v_accvgpr_read_b32 v18, a18\n"
"v_permlane16_swap_b32 v196, v198\n"
"v_permlane16_swap_b32 v197, v199\n"
"v_accvgpr_read_b32 v19, a19\n"
"v_accvgpr_read_b32 v20, a20\n"
"v_accvgpr_read_b32 v21, a21\n"
"v_accvgpr_read_b32 v22, a22\n"
"v_accvgpr_read_b32 v23, a23\n"
"v_pk_mul_f32 v[16:17], v[16:17], v[136:137] op_sel_hi:[1,0]\n"
"v_pk_mul_f32 v[18:19], v[18:19], v[136:137] op_sel_hi:[1,0]\n"
"v_pk_mul_f32 v[20:21], v[20:21], v[136:137] op_sel_hi:[1,0]\n"
"v_pk_mul_f32 v[22:23], v[22:23], v[136:137] op_sel_hi:[1,0]\n"
"v_cvt_pk_bf16_f32 v200, v16, v17\n"
"v_cvt_pk_bf16_f32 v201, v18, v19\n"
"v_cvt_pk_bf16_f32 v202, v20, v21\n"
"v_cvt_pk_bf16_f32 v203, v22, v23\n"
"v_accvgpr_read_b32 v24, a24\n"
"v_accvgpr_read_b32 v25, a25\n"
"v_accvgpr_read_b32 v26, a26\n"
"v_permlane16_swap_b32 v200, v202\n"
"v_permlane16_swap_b32 v201, v203\n"
"v_accvgpr_read_b32 v27, a27\n"
"v_accvgpr_read_b32 v28, a28\n"
"v_accvgpr_read_b32 v29, a29\n"
"v_accvgpr_read_b32 v30, a30\n"
"v_accvgpr_read_b32 v31, a31\n"
"v_pk_mul_f32 v[24:25], v[24:25], v[136:137] op_sel_hi:[1,0]\n"
"v_pk_mul_f32 v[26:27], v[26:27], v[136:137] op_sel_hi:[1,0]\n"
"v_pk_mul_f32 v[28:29], v[28:29], v[136:137] op_sel_hi:[1,0]\n"
"v_pk_mul_f32 v[30:31], v[30:31], v[136:137] op_sel_hi:[1,0]\n"
"v_cvt_pk_bf16_f32 v204, v24, v25\n"
"v_cvt_pk_bf16_f32 v205, v26, v27\n"
"v_cvt_pk_bf16_f32 v206, v28, v29\n"
"v_cvt_pk_bf16_f32 v207, v30, v31\n"
"s_nop 1\n"
"v_permlane16_swap_b32 v204, v206\n"
"v_permlane16_swap_b32 v205, v207\n"
"v_accvgpr_read_b32 v16, a32\n"
"v_accvgpr_read_b32 v17, a33\n"
"v_accvgpr_read_b32 v18, a34\n"
"v_accvgpr_read_b32 v19, a35\n"
"v_accvgpr_read_b32 v20, a36\n"
"v_accvgpr_read_b32 v21, a37\n"
"v_accvgpr_read_b32 v22, a38\n"
"v_accvgpr_read_b32 v23, a39\n"
"v_pk_mul_f32 v[16:17], v[16:17], v[140:141] op_sel_hi:[1,0]\n"
"v_pk_mul_f32 v[18:19], v[18:19], v[140:141] op_sel_hi:[1,0]\n"
"v_pk_mul_f32 v[20:21], v[20:21], v[140:141] op_sel_hi:[1,0]\n"
"v_pk_mul_f32 v[22:23], v[22:23], v[140:141] op_sel_hi:[1,0]\n"
"v_cvt_pk_bf16_f32 v208, v16, v17\n"
"v_cvt_pk_bf16_f32 v209, v18, v19\n"
"v_cvt_pk_bf16_f32 v210, v20, v21\n"
"v_cvt_pk_bf16_f32 v211, v22, v23\n"
"v_accvgpr_read_b32 v24, a40\n"
"v_accvgpr_read_b32 v25, a41\n"
"v_accvgpr_read_b32 v26, a42\n"
"v_permlane16_swap_b32 v208, v210\n"
"v_permlane16_swap_b32 v209, v211\n"
"v_accvgpr_read_b32 v27, a43\n"
"v_accvgpr_read_b32 v28, a44\n"
"v_accvgpr_read_b32 v29, a45\n"
"v_accvgpr_read_b32 v30, a46\n"
"v_accvgpr_read_b32 v31, a47\n"
"v_pk_mul_f32 v[24:25], v[24:25], v[140:141] op_sel_hi:[1,0]\n"
"v_pk_mul_f32 v[26:27], v[26:27], v[140:141] op_sel_hi:[1,0]\n"
"v_pk_mul_f32 v[28:29], v[28:29], v[140:141] op_sel_hi:[1,0]\n"
"v_pk_mul_f32 v[30:31], v[30:31], v[140:141] op_sel_hi:[1,0]\n"
"v_cvt_pk_bf16_f32 v212, v24, v25\n"
"v_cvt_pk_bf16_f32 v213, v26, v27\n"
"v_cvt_pk_bf16_f32 v214, v28, v29\n"
"v_cvt_pk_bf16_f32 v215, v30, v31\n"
"v_accvgpr_read_b32 v16, a48\n"
"v_accvgpr_read_b32 v17, a49\n"
"v_accvgpr_read_b32 v18, a50\n"
"v_permlane16_swap_b32 v212, v214\n"
"v_permlane16_swap_b32 v213, v215\n"
"v_accvgpr_read_b32 v19, a51\n"
"v_accvgpr_read_b32 v20, a52\n"
"v_accvgpr_read_b32 v21, a53\n"
"v_accvgpr_read_b32 v22, a54\n"
"v_accvgpr_read_b32 v23, a55\n"
"v_pk_mul_f32 v[16:17], v[16:17], v[140:141] op_sel_hi:[1,0]\n"
"v_pk_mul_f32 v[18:19], v[18:19], v[140:141] op_sel_hi:[1,0]\n"
"v_pk_mul_f32 v[20:21], v[20:21], v[140:141] op_sel_hi:[1,0]\n"
"v_pk_mul_f32 v[22:23], v[22:23], v[140:141] op_sel_hi:[1,0]\n"
"v_cvt_pk_bf16_f32 v216, v16, v17\n"
"v_cvt_pk_bf16_f32 v217, v18, v19\n"
"v_cvt_pk_bf16_f32 v218, v20, v21\n"
"v_cvt_pk_bf16_f32 v219, v22, v23\n"
"v_accvgpr_read_b32 v24, a56\n"
"v_accvgpr_read_b32 v25, a57\n"
"v_accvgpr_read_b32 v26, a58\n"
"v_permlane16_swap_b32 v216, v218\n"
"v_permlane16_swap_b32 v217, v219\n"
"v_accvgpr_read_b32 v27, a59\n"
"buffer_store_dwordx4 v[192:195], v185, s[48:51], s70 offen offset:0\n"
"v_accvgpr_read_b32 v28, a60\n"
"v_accvgpr_read_b32 v29, a61\n"
"v_accvgpr_read_b32 v30, a62\n"
"v_accvgpr_read_b32 v31, a63\n"
"v_pk_mul_f32 v[24:25], v[24:25], v[140:141] op_sel_hi:[1,0]\n"
"v_pk_mul_f32 v[26:27], v[26:27], v[140:141] op_sel_hi:[1,0]\n"
"v_pk_mul_f32 v[28:29], v[28:29], v[140:141] op_sel_hi:[1,0]\n"
"v_pk_mul_f32 v[30:31], v[30:31], v[140:141] op_sel_hi:[1,0]\n"
"v_cvt_pk_bf16_f32 v220, v24, v25\n"
"v_cvt_pk_bf16_f32 v221, v26, v27\n"
"v_cvt_pk_bf16_f32 v222, v28, v29\n"
"v_cvt_pk_bf16_f32 v223, v30, v31\n"
"s_nop 1\n"
"v_permlane16_swap_b32 v220, v222\n"
"v_permlane16_swap_b32 v221, v223\n"
"v_accvgpr_read_b32 v16, a64\n"
"v_accvgpr_read_b32 v17, a65\n"
"v_accvgpr_read_b32 v18, a66\n"
"v_accvgpr_read_b32 v19, a67\n"
"v_accvgpr_read_b32 v20, a68\n"
"v_accvgpr_read_b32 v21, a69\n"
"v_accvgpr_read_b32 v22, a70\n"
"v_accvgpr_read_b32 v23, a71\n"
"v_pk_mul_f32 v[16:17], v[16:17], v[144:145] op_sel_hi:[1,0]\n"
"v_pk_mul_f32 v[18:19], v[18:19], v[144:145] op_sel_hi:[1,0]\n"
"v_pk_mul_f32 v[20:21], v[20:21], v[144:145] op_sel_hi:[1,0]\n"
"v_pk_mul_f32 v[22:23], v[22:23], v[144:145] op_sel_hi:[1,0]\n"
"v_cvt_pk_bf16_f32 v224, v16, v17\n"
"v_cvt_pk_bf16_f32 v225, v18, v19\n"
"v_cvt_pk_bf16_f32 v226, v20, v21\n"
"v_cvt_pk_bf16_f32 v227, v22, v23\n"
"v_accvgpr_read_b32 v24, a72\n"
"v_accvgpr_read_b32 v25, a73\n"
"v_accvgpr_read_b32 v26, a74\n"
"v_permlane16_swap_b32 v224, v226\n"
"v_permlane16_swap_b32 v225, v227\n"
"v_accvgpr_read_b32 v27, a75\n"
"v_accvgpr_read_b32 v28, a76\n"
"v_accvgpr_read_b32 v29, a77\n"
"v_accvgpr_read_b32 v30, a78\n"
"v_accvgpr_read_b32 v31, a79\n"
"v_pk_mul_f32 v[24:25], v[24:25], v[144:145] op_sel_hi:[1,0]\n"
"v_pk_mul_f32 v[26:27], v[26:27], v[144:145] op_sel_hi:[1,0]\n"
"v_pk_mul_f32 v[28:29], v[28:29], v[144:145] op_sel_hi:[1,0]\n"
"v_pk_mul_f32 v[30:31], v[30:31], v[144:145] op_sel_hi:[1,0]\n"
"v_cvt_pk_bf16_f32 v228, v24, v25\n"
"v_cvt_pk_bf16_f32 v229, v26, v27\n"
"v_cvt_pk_bf16_f32 v230, v28, v29\n"
"v_cvt_pk_bf16_f32 v231, v30, v31\n"
"v_accvgpr_read_b32 v16, a80\n"
"v_accvgpr_read_b32 v17, a81\n"
"v_accvgpr_read_b32 v18, a82\n"
"v_permlane16_swap_b32 v228, v230\n"
"v_permlane16_swap_b32 v229, v231\n"
"v_accvgpr_read_b32 v19, a83\n"
"v_accvgpr_read_b32 v20, a84\n"
"v_accvgpr_read_b32 v21, a85\n"
"v_accvgpr_read_b32 v22, a86\n"
"v_accvgpr_read_b32 v23, a87\n"
"v_pk_mul_f32 v[16:17], v[16:17], v[144:145] op_sel_hi:[1,0]\n"
"v_pk_mul_f32 v[18:19], v[18:19], v[144:145] op_sel_hi:[1,0]\n"
"v_pk_mul_f32 v[20:21], v[20:21], v[144:145] op_sel_hi:[1,0]\n"
"v_pk_mul_f32 v[22:23], v[22:23], v[144:145] op_sel_hi:[1,0]\n"
"v_cvt_pk_bf16_f32 v232, v16, v17\n"
"v_cvt_pk_bf16_f32 v233, v18, v19\n"
"v_cvt_pk_bf16_f32 v234, v20, v21\n"
"v_cvt_pk_bf16_f32 v235, v22, v23\n"
"v_accvgpr_read_b32 v24, a88\n"
"v_accvgpr_read_b32 v25, a89\n"
"v_accvgpr_read_b32 v26, a90\n"
"v_permlane16_swap_b32 v232, v234\n"
"v_permlane16_swap_b32 v233, v235\n"
"v_accvgpr_read_b32 v27, a91\n"
"buffer_store_dwordx4 v[196:199], v185, s[48:51], s70 offen offset:64\n"
"v_accvgpr_read_b32 v28, a92\n"
"v_accvgpr_read_b32 v29, a93\n"
"v_accvgpr_read_b32 v30, a94\n"
"v_accvgpr_read_b32 v31, a95\n"
"v_pk_mul_f32 v[24:25], v[24:25], v[144:145] op_sel_hi:[1,0]\n"
"v_pk_mul_f32 v[26:27], v[26:27], v[144:145] op_sel_hi:[1,0]\n"
"v_pk_mul_f32 v[28:29], v[28:29], v[144:145] op_sel_hi:[1,0]\n"
"v_pk_mul_f32 v[30:31], v[30:31], v[144:145] op_sel_hi:[1,0]\n"
"v_cvt_pk_bf16_f32 v236, v24, v25\n"
"v_cvt_pk_bf16_f32 v237, v26, v27\n"
"v_cvt_pk_bf16_f32 v238, v28, v29\n"
"v_cvt_pk_bf16_f32 v239, v30, v31\n"
"s_nop 1\n"
"v_permlane16_swap_b32 v236, v238\n"
"v_permlane16_swap_b32 v237, v239\n"
"v_accvgpr_read_b32 v16, a96\n"
"v_accvgpr_read_b32 v17, a97\n"
"v_accvgpr_read_b32 v18, a98\n"
"v_accvgpr_read_b32 v19, a99\n"
"v_accvgpr_read_b32 v20, a100\n"
"v_accvgpr_read_b32 v21, a101\n"
"v_accvgpr_read_b32 v22, a102\n"
"v_accvgpr_read_b32 v23, a103\n"
"v_pk_mul_f32 v[16:17], v[16:17], v[148:149] op_sel_hi:[1,0]\n"
"v_pk_mul_f32 v[18:19], v[18:19], v[148:149] op_sel_hi:[1,0]\n"
"v_pk_mul_f32 v[20:21], v[20:21], v[148:149] op_sel_hi:[1,0]\n"
"v_pk_mul_f32 v[22:23], v[22:23], v[148:149] op_sel_hi:[1,0]\n"
"v_cvt_pk_bf16_f32 v240, v16, v17\n"
"v_cvt_pk_bf16_f32 v241, v18, v19\n"
"v_cvt_pk_bf16_f32 v242, v20, v21\n"
"v_cvt_pk_bf16_f32 v243, v22, v23\n"
"v_accvgpr_read_b32 v24, a104\n"
"v_accvgpr_read_b32 v25, a105\n"
"v_accvgpr_read_b32 v26, a106\n"
"v_permlane16_swap_b32 v240, v242\n"
"v_permlane16_swap_b32 v241, v243\n"
"v_accvgpr_read_b32 v27, a107\n"
"v_accvgpr_read_b32 v28, a108\n"
"v_accvgpr_read_b32 v29, a109\n"
"v_accvgpr_read_b32 v30, a110\n"
"v_accvgpr_read_b32 v31, a111\n"
"v_pk_mul_f32 v[24:25], v[24:25], v[148:149] op_sel_hi:[1,0]\n"
"v_pk_mul_f32 v[26:27], v[26:27], v[148:149] op_sel_hi:[1,0]\n"
"v_pk_mul_f32 v[28:29], v[28:29], v[148:149] op_sel_hi:[1,0]\n"
"v_pk_mul_f32 v[30:31], v[30:31], v[148:149] op_sel_hi:[1,0]\n"
"v_cvt_pk_bf16_f32 v244, v24, v25\n"
"v_cvt_pk_bf16_f32 v245, v26, v27\n"
"v_cvt_pk_bf16_f32 v246, v28, v29\n"
"v_cvt_pk_bf16_f32 v247, v30, v31\n"
"v_accvgpr_read_b32 v16, a112\n"
"v_accvgpr_read_b32 v17, a113\n"
"v_accvgpr_read_b32 v18, a114\n"
"v_permlane16_swap_b32 v244, v246\n"
"v_permlane16_swap_b32 v245, v247\n"
"v_accvgpr_read_b32 v19, a115\n"
"v_accvgpr_read_b32 v20, a116\n"
"v_accvgpr_read_b32 v21, a117\n"
"v_accvgpr_read_b32 v22, a118\n"
"v_accvgpr_read_b32 v23, a119\n"
"v_pk_mul_f32 v[16:17], v[16:17], v[148:149] op_sel_hi:[1,0]\n"
"v_pk_mul_f32 v[18:19], v[18:19], v[148:149] op_sel_hi:[1,0]\n"
"v_pk_mul_f32 v[20:21], v[20:21], v[148:149] op_sel_hi:[1,0]\n"
"v_pk_mul_f32 v[22:23], v[22:23], v[148:149] op_sel_hi:[1,0]\n"
"v_cvt_pk_bf16_f32 v248, v16, v17\n"
"v_cvt_pk_bf16_f32 v249, v18, v19\n"
"v_cvt_pk_bf16_f32 v250, v20, v21\n"
"v_cvt_pk_bf16_f32 v251, v22, v23\n"
"v_accvgpr_read_b32 v24, a120\n"
"v_accvgpr_read_b32 v25, a121\n"
"v_accvgpr_read_b32 v26, a122\n"
"v_permlane16_swap_b32 v248, v250\n"
"v_permlane16_swap_b32 v249, v251\n"
"v_accvgpr_read_b32 v27, a123\n"
"buffer_store_dwordx4 v[200:203], v185, s[48:51], s70 offen offset:128\n"
"v_accvgpr_read_b32 v28, a124\n"
"v_accvgpr_read_b32 v29, a125\n"
"v_accvgpr_read_b32 v30, a126\n"
"v_accvgpr_read_b32 v31, a127\n"
"v_pk_mul_f32 v[24:25], v[24:25], v[148:149] op_sel_hi:[1,0]\n"
"v_pk_mul_f32 v[26:27], v[26:27], v[148:149] op_sel_hi:[1,0]\n"
"v_pk_mul_f32 v[28:29], v[28:29], v[148:149] op_sel_hi:[1,0]\n"
"v_pk_mul_f32 v[30:31], v[30:31], v[148:149] op_sel_hi:[1,0]\n"
"v_cvt_pk_bf16_f32 v252, v24, v25\n"
"v_cvt_pk_bf16_f32 v253, v26, v27\n"
"v_cvt_pk_bf16_f32 v254, v28, v29\n"
"v_cvt_pk_bf16_f32 v255, v30, v31\n"
"s_nop 1\n"
"v_permlane16_swap_b32 v252, v254\n"
"v_permlane16_swap_b32 v253, v255\n"
"ds_read_b128 v[96:99], v180 offset:0\n"
"ds_read_b128 v[100:103], v180 offset:256\n"
"ds_read_b128 v[112:115], v180 offset:512\n"
"ds_read_b128 v[116:119], v180 offset:768\n"
"ds_read_b128 v[104:107], v180 offset:4096\n"
"ds_read_b128 v[108:111], v180 offset:4352\n"
"s_waitcnt lgkmcnt(2)\n"
"v_mfma_f32_16x16x32_bf16 v[56:59], v[96:99], a[160:163], 0\n"
"v_mfma_f32_16x16x32_bf16 v[56:59], v[100:103], a[164:167], v[56:59]\n"
"v_mfma_f32_16x16x32_bf16 v[56:59], v[112:115], a[168:171], v[56:59]\n"
"v_mfma_f32_16x16x32_bf16 v[56:59], v[116:119], a[172:175], v[56:59]\n"
"v_mfma_f32_16x16x32_bf16 v[60:63], v[96:99], a[176:179], 0\n"
"v_mfma_f32_16x16x32_bf16 v[60:63], v[100:103], a[180:183], v[60:63]\n"
"v_mfma_f32_16x16x32_bf16 v[60:63], v[112:115], a[184:187], v[60:63]\n"
"v_mfma_f32_16x16x32_bf16 v[60:63], v[116:119], a[188:191], v[60:63]\n"
"buffer_store_dwordx4 v[204:207], v185, s[48:51], s70 offen offset:192\n"
"v_mfma_f32_16x16x32_bf16 v[64:67], v[96:99], a[192:195], 0\n"
"v_mfma_f32_16x16x32_bf16 v[64:67], v[100:103], a[196:199], v[64:67]\n"
"v_mfma_f32_16x16x32_bf16 v[64:67], v[112:115], a[200:203], v[64:67]\n"
"v_mfma_f32_16x16x32_bf16 v[64:67], v[116:119], a[204:207], v[64:67]\n"
"v_mfma_f32_16x16x32_bf16 v[68:71], v[112:115], a[216:219], 0\n"
"ds_read_b128 v[112:115], v180 offset:4608\n"
"v_mfma_f32_16x16x32_bf16 v[68:71], v[116:119], a[220:223], v[68:71]\n"
"ds_read_b128 v[116:119], v180 offset:4864\n"
"v_mfma_f32_16x16x32_bf16 v[68:71], v[96:99], a[208:211], v[68:71]\n"
"v_mfma_f32_16x16x32_bf16 v[68:71], v[100:103], a[212:215], v[68:71]\n"
"s_nop 5\n"
"s_cmp_lg_u32 s66, 0\n"
"s_cbranch_scc1 L_given_max_b_%=\n"
"v_max3_f32 v136, v56, v57, v58\n"
"v_max_f32 v137, v136, v59\n"
"v_max_f32 v136, v136, v59\n"
"v_max3_f32 v140, v60, v61, v62\n"
"v_max_f32 v141, v140, v63\n"
"v_max_f32 v140, v140, v63\n"
"v_max3_f32 v144, v64, v65, v66\n"
"v_max_f32 v145, v144, v67\n"
"v_max_f32 v144, v144, v67\n"
"v_max3_f32 v148, v68, v69, v70\n"
"v_max_f32 v149, v148, v71\n"
"v_max_f32 v148, v148, v71\n"
"s_nop 1\n"
"v_permlane32_swap_b32 v136, v137\n"
"v_permlane32_swap_b32 v140, v141\n"
"v_permlane32_swap_b32 v144, v145\n"
"v_permlane32_swap_b32 v148, v149\n"
"s_nop 1\n"
"v_max_f32 v138, v136, v137\n"
"v_max_f32 v139, v136, v137\n"
"v_max_f32 v142, v140, v141\n"
"v_max_f32 v143, v140, v141\n"
"v_max_f32 v146, v144, v145\n"
"v_max_f32 v147, v144, v145\n"
"v_max_f32 v150, v148, v149\n"
"v_max_f32 v151, v148, v149\n"
"s_nop 1\n"
"v_permlane16_swap_b32 v138, v139\n"
"v_permlane16_swap_b32 v142, v143\n"
"v_permlane16_swap_b32 v146, v147\n"
"v_permlane16_swap_b32 v150, v151\n"
"s_nop 1\n"
"v_max_f32 v136, v138, v139\n"
"v_max_f32 v140, v142, v143\n"
"v_max_f32 v144, v146, v147\n"
"v_max_f32 v148, v150, v151\n"
"v_sub_f32 v136, 0, v136\n"
"v_sub_f32 v140, 0, v140\n"
"v_sub_f32 v144, 0, v144\n"
"v_sub_f32 v148, 0, v148\n"
"s_branch L_have_max_b_%=\n"
"L_given_max_b_%=:\n"
"v_mbcnt_lo_u32_b32 v186, -1, 0\n"
"v_mbcnt_hi_u32_b32 v186, -1, v186\n"
"v_lshlrev_b32 v186, 2, v186\n"
"global_load_dword v136, v186, s[68:69] offset:1280\n"
"global_load_dword v140, v186, s[68:69] offset:1536\n"
"global_load_dword v144, v186, s[68:69] offset:1792\n"
"global_load_dword v148, v186, s[68:69] offset:2048\n"
"s_waitcnt vmcnt(0)\n"
"L_have_max_b_%=:\n"
"v_mov_b32 v137, v136\n"
"v_mov_b32 v138, v136\n"
"v_mov_b32 v139, v136\n"
"v_pk_add_f32 v[56:57], v[56:57], v[136:137]\n"
"v_pk_add_f32 v[58:59], v[58:59], v[136:137]\n"
"v_mov_b32 v141, v140\n"
"v_mov_b32 v142, v140\n"
"v_mov_b32 v143, v140\n"
"v_pk_add_f32 v[60:61], v[60:61], v[140:141]\n"
"v_pk_add_f32 v[62:63], v[62:63], v[140:141]\n"
"v_mov_b32 v145, v144\n"
"v_mov_b32 v146, v144\n"
"v_mov_b32 v147, v144\n"
"v_pk_add_f32 v[64:65], v[64:65], v[144:145]\n"
"v_pk_add_f32 v[66:67], v[66:67], v[144:145]\n"
"v_mov_b32 v149, v148\n"
"v_mov_b32 v150, v148\n"
"v_mov_b32 v151, v148\n"
"v_pk_add_f32 v[68:69], v[68:69], v[148:149]\n"
"v_pk_add_f32 v[70:71], v[70:71], v[148:149]\n"
"s_waitcnt lgkmcnt(0)\n"
"v_mfma_f32_16x16x32_bf16 v[0:3], v[104:107], a[160:163], v[136:139]\n"
"v_exp_f32 v56, v56\n"
"v_exp_f32 v57, v57\n"
"v_mfma_f32_16x16x32_bf16 v[0:3], v[108:111], a[164:167], v[0:3]\n"
"v_exp_f32 v58, v58\n"
"v_cvt_pk_bf16_f32 v16, v56, v57\n"
"v_mfma_f32_16x16x32_bf16 v[0:3], v[112:115], a[168:171], v[0:3]\n"
"buffer_store_dwordx4 v[208:211], v185, s[48:51], s71 offen offset:0\n"
"v_exp_f32 v59, v59\n"
"v_mfma_f32_16x16x32_bf16 v[0:3], v[116:119], a[172:175], v[0:3]\n"
"v_exp_f32 v60, v60\n"
"v_exp_f32 v61, v61\n"
"v_mfma_f32_16x16x32_bf16 v[4:7], v[104:107], a[176:179], v[140:143]\n"
"ds_read_b128 v[96:99], v180 offset:8192\n"
"v_cvt_pk_bf16_f32 v17, v58, v59\n"
"v_exp_f32 v62, v62\n"
"v_mfma_f32_16x16x32_bf16 v[4:7], v[108:111], a[180:183], v[4:7]\n"
"v_exp_f32 v63, v63\n"
"v_exp_f32 v64, v64\n"
"v_mfma_f32_16x16x32_bf16 v[4:7], v[112:115], a[184:187], v[4:7]\n"
"v_cvt_pk_bf16_f32 v24, v60, v61\n"
"v_exp_f32 v65, v65\n"
"v_cvt_pk_bf16_f32 v25, v62, v63\n"
"v_mfma_f32_16x16x32_bf16 v[4:7], v[116:119], a[188:191], v[4:7]\n"
"ds_read_b128 v[100:103], v180 offset:8448\n"
"v_exp_f32 v66, v66\n"
"v_cvt_pk_bf16_f32 v32, v64, v65\n"
"v_mfma_f32_16x16x32_bf16 v[8:11], v[104:107], a[192:195], v[144:147]\n"
"v_exp_f32 v67, v67\n"
"v_exp_f32 v68, v68\n"
"v_mfma_f32_16x16x32_bf16 v[8:11], v[108:111], a[196:199], v[8:11]\n"
"v_exp_f32 v69, v69\n"
"v_exp_f32 v70, v70\n"
"v_mfma_f32_16x16x32_bf16 v[8:11], v[112:115], a[200:203], v[8:11]\n"
"v_cvt_pk_bf16_f32 v33, v66, v67\n"
"v_exp_f32 v71, v71\n"
"v_cvt_pk_bf16_f32 v40, v68, v69\n"
"v_mfma_f32_16x16x32_bf16 v[8:11], v[116:119], a[204:207], v[8:11]\n"
"buffer_store_dwordx4 v[212:215], v185, s[48:51], s71 offen offset:64\n"
"v_exp_f32 v0, v0\n"
"v_cvt_pk_bf16_f32 v41, v70, v71\n"
"v_mfma_f32_16x16x32_bf16 v[12:15], v[112:115], a[216:219], v[148:151]\n"
"ds_read_b128 v[112:115], v180 offset:8704\n"
"v_exp_f32 v1, v1\n"
"v_mfma_f32_16x16x32_bf16 v[12:15], v[116:119], a[220:223], v[12:15]\n"
"ds_read_b128 v[116:119], v180 offset:8960\n"
"v_exp_f32 v2, v2\n"
"v_cvt_pk_bf16_f32 v18, v0, v1\n"
"v_mfma_f32_16x16x32_bf16 v[12:15], v[104:107], a[208:211], v[12:15]\n"
"v_exp_f32 v3, v3\n"
"v_exp_f32 v4, v4\n"
"v_mfma_f32_16x16x32_bf16 v[12:15], v[108:111], a[212:215], v[12:15]\n"
"v_exp_f32 v5, v5\n"
"v_cvt_pk_bf16_f32 v19, v2, v3\n"
"s_waitcnt lgkmcnt(2)\n"
"v_mfma_f32_16x16x32_bf16 v[0:3], v[96:99], a[160:163], v[136:139]\n"
"v_exp_f32 v6, v6\n"
"v_exp_f32 v7, v7\n"
"v_mfma_f32_16x16x32_bf16 v[0:3], v[100:103], a[164:167], v[0:3]\n"
"v_cvt_pk_bf16_f32 v26, v4, v5\n"
"v_exp_f32 v8, v8\n"
"s_waitcnt lgkmcnt(0)\n"
"v_mfma_f32_16x16x32_bf16 v[0:3], v[112:115], a[168:171], v[0:3]\n"
"v_exp_f32 v9, v9\n"
"v_exp_f32 v10, v10\n"
"v_mfma_f32_16x16x32_bf16 v[0:3], v[116:119], a[172:175], v[0:3]\n"
"v_cvt_pk_bf16_f32 v27, v6, v7\n"
"v_exp_f32 v11, v11\n"
"v_cvt_pk_bf16_f32 v34, v8, v9\n"
"v_mfma_f32_16x16x32_bf16 v[4:7], v[96:99], a[176:179], v[140:143]\n"
"ds_read_b128 v[104:107], v180 offset:12288\n"
"v_exp_f32 v12, v12\n"
"v_cvt_pk_bf16_f32 v35, v10, v11\n"
"v_mfma_f32_16x16x32_bf16 v[4:7], v[100:103], a[180:183], v[4:7]\n"
"buffer_store_dwordx4 v[216:219], v185, s[48:51], s71 offen offset:128\n"
"v_exp_f32 v13, v13\n"
"v_mfma_f32_16x16x32_bf16 v[4:7], v[112:115], a[184:187], v[4:7]\n"
"v_exp_f32 v14, v14\n"
"v_exp_f32 v15, v15\n"
"v_mfma_f32_16x16x32_bf16 v[4:7], v[116:119], a[188:191], v[4:7]\n"
"ds_read_b128 v[108:111], v180 offset:12544\n"
"v_cvt_pk_bf16_f32 v42, v12, v13\n"
"v_cvt_pk_bf16_f32 v43, v14, v15\n"
"v_mfma_f32_16x16x32_bf16 v[8:11], v[96:99], a[192:195], v[144:147]\n"
"v_exp_f32 v0, v0\n"
"v_exp_f32 v1, v1\n"
"v_mfma_f32_16x16x32_bf16 v[8:11], v[100:103], a[196:199], v[8:11]\n"
"v_exp_f32 v2, v2\n"
"v_exp_f32 v3, v3\n"
"v_mfma_f32_16x16x32_bf16 v[8:11], v[112:115], a[200:203], v[8:11]\n"
"v_cvt_pk_bf16_f32 v20, v0, v1\n"
"v_cvt_pk_bf16_f32 v21, v2, v3\n"
"v_mfma_f32_16x16x32_bf16 v[8:11], v[116:119], a[204:207], v[8:11]\n"
"v_mfma_f32_16x16x32_bf16 v[12:15], v[112:115], a[216:219], v[148:151]\n"
"ds_read_b128 v[112:115], v180 offset:12800\n"
"v_exp_f32 v4, v4\n"
"v_mfma_f32_16x16x32_bf16 v[12:15], v[116:119], a[220:223], v[12:15]\n"
"ds_read_b128 v[116:119], v180 offset:13056\n"
"v_exp_f32 v5, v5\n"
"v_mfma_f32_16x16x32_bf16 v[12:15], v[96:99], a[208:211], v[12:15]\n"
"v_exp_f32 v6, v6\n"
"v_exp_f32 v7, v7\n"
"v_mfma_f32_16x16x32_bf16 v[12:15], v[100:103], a[212:215], v[12:15]\n"
"v_cvt_pk_bf16_f32 v28, v4, v5\n"
"v_cvt_pk_bf16_f32 v29, v6, v7\n"
"s_waitcnt lgkmcnt(2)\n"
"v_mfma_f32_16x16x32_bf16 v[0:3], v[104:107], a[160:163], v[136:139]\n"
"v_exp_f32 v8, v8\n"
"v_exp_f32 v9, v9\n"
"v_mfma_f32_16x16x32_bf16 v[0:3], v[108:111], a[164:167], v[0:3]\n"
"s_waitcnt vmcnt(23) lgkmcnt(0)\n"
"v_xor_b32 v181, 0x8000, v181\n"
"v_xor_b32 v182, 0x8000, v182\n"
"s_barrier\n"
"v_mfma_f32_16x16x32_bf16 v[0:3], v[112:115], a[168:171], v[0:3]\n"
"v_exp_f32 v10, v10\n"
"v_exp_f32 v11, v11\n"
"v_mfma_f32_16x16x32_bf16 v[0:3], v[116:119], a[172:175], v[0:3]\n"
"ds_read_b64_tr_b16 v[120:121], v181 offset:0\n"
"v_cvt_pk_bf16_f32 v36, v8, v9\n"
"v_cvt_pk_bf16_f32 v37, v10, v11\n"
"v_mfma_f32_16x16x32_bf16 v[4:7], v[104:107], a[176:179], v[140:143]\n"
"ds_read_b128 v[96:99], v180 offset:16384\n"
"v_exp_f32 v12, v12\n"
"v_mfma_f32_16x16x32_bf16 v[4:7], v[108:111], a[180:183], v[4:7]\n"
"ds_read_b64_tr_b16 v[122:123], v181 offset:4096\n"
"v_exp_f32 v13, v13\n"
"v_mfma_f32_16x16x32_bf16 v[4:7], v[112:115], a[184:187], v[4:7]\n"
"s_add_u32 m0, s54, 0xc000\n"
"v_exp_f32 v14, v14\n"
"v_cvt_pk_bf16_f32 v44, v12, v13\n"
"v_mfma_f32_16x16x32_bf16 v[4:7], v[116:119], a[188:191], v[4:7]\n"
"ds_read_b128 v[100:103], v180 offset:16640\n"
"v_exp_f32 v15, v15\n"
"v_mfma_f32_16x16x32_bf16 v[8:11], v[104:107], a[192:195], v[144:147]\n"
"ds_read_b64_tr_b16 v[124:125], v181 offset:8192\n"
"v_exp_f32 v0, v0\n"
"v_cvt_pk_bf16_f32 v45, v14, v15\n"
"v_mfma_f32_16x16x32_bf16 v[8:11], v[108:111], a[196:199], v[8:11]\n"
"ds_read_b64_tr_b16 v[126:127], v181 offset:12288\n"
"v_exp_f32 v1, v1\n"
"v_mfma_f32_16x16x32_bf16 v[8:11], v[112:115], a[200:203], v[8:11]\n"
"buffer_load_dwordx4 v183, s[40:43], s52 offen offset:0 lds\n"
"v_exp_f32 v2, v2\n"
"v_cvt_pk_bf16_f32 v22, v0, v1\n"
"v_mfma_f32_16x16x32_bf16 v[8:11], v[116:119], a[204:207], v[8:11]\n"
"buffer_store_dwordx4 v[220:223], v185, s[48:51], s71 offen offset:192\n"
"v_exp_f32 v3, v3\n"
"v_mfma_f32_16x16x32_bf16 v[12:15], v[112:115], a[216:219], v[148:151]\n"
"ds_read_b128 v[112:115], v180 offset:16896\n"
"v_exp_f32 v4, v4\n"
"v_cvt_pk_bf16_f32 v23, v2, v3\n"
"v_mfma_f32_16x16x32_bf16 v[12:15], v[116:119], a[220:223], v[12:15]\n"
"ds_read_b128 v[116:119], v180 offset:17152\n"
"v_exp_f32 v5, v5\n"
"v_mfma_f32_16x16x32_bf16 v[12:15], v[104:107], a[208:211], v[12:15]\n"
"buffer_store_dwordx4 v[224:227], v185, s[48:51], s72 offen offset:0\n"
"v_exp_f32 v6, v6\n"
"v_cvt_pk_bf16_f32 v30, v4, v5\n"
"v_mfma_f32_16x16x32_bf16 v[12:15], v[108:111], a[212:215], v[12:15]\n"
"v_exp_f32 v7, v7\n"
"s_nop 7\n"
"v_exp_f32 v8, v8\n"
"v_exp_f32 v9, v9\n"
"v_exp_f32 v10, v10\n"
"v_exp_f32 v11, v11\n"
"s_nop 0\n"
"v_cvt_pk_bf16_f32 v31, v6, v7\n"
"v_cvt_pk_bf16_f32 v38, v8, v9\n"
"v_cvt_pk_bf16_f32 v39, v10, v11\n"
"s_waitcnt lgkmcnt(2)\n"
"v_mfma_f32_16x16x32_bf16 v[0:3], v[96:99], a[160:163], v[136:139]\n"
"v_exp_f32 v12, v12\n"
"v_mfma_f32_16x16x32_bf16 v[0:3], v[100:103], a[164:167], v[0:3]\n"
"s_waitcnt lgkmcnt(0)\n"
"v_mfma_f32_16x16x32_bf16 v[0:3], v[112:115], a[168:171], v[0:3]\n"
"buffer_load_dwordx4 v184, s[40:43], s52 offen offset:1024 lds\n"
"v_mfma_f32_16x16x32_bf16 v[0:3], v[116:119], a[172:175], v[0:3]\n"
"v_exp_f32 v13, v13\n"
"v_mfma_f32_16x16x32_bf16 a[0:3], v[120:123], v[16:19], 0\n"
"ds_read_b128 v[104:107], v180 offset:20480\n"
"v_cvt_pk_bf16_f32 v46, v12, v13\n"
"v_mfma_f32_16x16x32_bf16 a[0:3], v[124:127], v[20:23], a[0:3]\n"
"ds_read_b64_tr_b16 v[128:129], v182 offset:0\n"
"v_mfma_f32_16x16x32_bf16 a[32:35], v[120:123], v[24:27], 0\n"
"ds_read_b64_tr_b16 v[130:131], v182 offset:4096\n"
"v_mfma_f32_16x16x32_bf16 a[32:35], v[124:127], v[28:31], a[32:35]\n"
"v_exp_f32 v14, v14\n"
"v_mfma_f32_16x16x32_bf16 v[4:7], v[96:99], a[176:179], v[140:143]\n"
"ds_read_b64_tr_b16 v[132:133], v182 offset:8192\n"
"v_mfma_f32_16x16x32_bf16 v[4:7], v[100:103], a[180:183], v[4:7]\n"
"buffer_load_dwordx4 v183, s[40:43], s52 offen offset:2048 lds\n"
"v_mfma_f32_16x16x32_bf16 v[4:7], v[112:115], a[184:187], v[4:7]\n"
"ds_read_b64_tr_b16 v[134:135], v182 offset:12288\n"
"v_mfma_f32_16x16x32_bf16 v[4:7], v[116:119], a[188:191], v[4:7]\n"
"v_exp_f32 v15, v15\n"
"v_mfma_f32_16x16x32_bf16 a[64:67], v[120:123], v[32:35], 0\n"
"ds_read_b128 v[108:111], v180 offset:20736\n"
"v_cvt_pk_bf16_f32 v47, v14, v15\n"
"v_mfma_f32_16x16x32_bf16 a[64:67], v[124:127], v[36:39], a[64:67]\n"
"v_exp_f32 v0, v0\n"
"v_mfma_f32_16x16x32_bf16 a[96:99], v[120:123], v[40:43], 0\n"
"v_exp_f32 v1, v1\n"
"v_mfma_f32_16x16x32_bf16 a[96:99], v[124:127], v[44:47], a[96:99]\n"
"v_exp_f32 v2, v2\n"
"v_mfma_f32_16x16x32_bf16 v[8:11], v[96:99], a[192:195], v[144:147]\n"
"ds_read_b64_tr_b16 v[120:121], v181 offset:256\n"
"v_cvt_pk_bf16_f32 v56, v0, v1\n"
"v_mfma_f32_16x16x32_bf16 v[8:11], v[100:103], a[196:199], v[8:11]\n"
"buffer_store_dwordx4 v[228:231], v185, s[48:51], s72 offen offset:64\n"
"v_mfma_f32_16x16x32_bf16 v[8:11], v[112:115], a[200:203], v[8:11]\n"
"ds_read_b64_tr_b16 v[122:123], v181 offset:4352\n"
"v_mfma_f32_16x16x32_bf16 v[8:11], v[116:119], a[204:207], v[8:11]\n"
"s_waitcnt lgkmcnt(3)\n"
"v_mfma_f32_16x16x32_bf16 a[4:7], v[128:131], v[16:19], 0\n"
"v_exp_f32 v3, v3\n"
"v_mfma_f32_16x16x32_bf16 a[4:7], v[132:135], v[20:23], a[4:7]\n"
"ds_read_b64_tr_b16 v[124:125], v181 offset:8448\n"
"v_cvt_pk_bf16_f32 v57, v2, v3\n"
"v_mfma_f32_16x16x32_bf16 a[36:39], v[128:131], v[24:27], 0\n"
"v_exp_f32 v4, v4\n"
"v_mfma_f32_16x16x32_bf16 a[36:39], v[132:135], v[28:31], a[36:39]\n"
"v_exp_f32 v5, v5\n"
"v_mfma_f32_16x16x32_bf16 v[12:15], v[112:115], a[216:219], v[148:151]\n"
"ds_read_b128 v[112:115], v180 offset:20992\n"
"v_cvt_pk_bf16_f32 v64, v4, v5\n"
"v_mfma_f32_16x16x32_bf16 v[12:15], v[116:119], a[220:223], v[12:15]\n"
"ds_read_b128 v[116:119], v180 offset:21248\n"
"v_mfma_f32_16x16x32_bf16 v[12:15], v[96:99], a[208:211], v[12:15]\n"
"ds_read_b64_tr_b16 v[126:127], v181 offset:12544\n"
"v_mfma_f32_16x16x32_bf16 v[12:15], v[100:103], a[212:215], v[12:15]\n"
"buffer_load_dwordx4 v184, s[40:43], s52 offen offset:3072 lds\n"
"v_mfma_f32_16x16x32_bf16 a[68:71], v[128:131], v[32:35], 0\n"
"v_exp_f32 v6, v6\n"
"v_mfma_f32_16x16x32_bf16 a[68:71], v[132:135], v[36:39], a[68:71]\n"
"v_exp_f32 v7, v7\n"
"v_mfma_f32_16x16x32_bf16 a[100:103], v[128:131], v[40:43], 0\n"
"v_exp_f32 v8, v8\n"
"v_mfma_f32_16x16x32_bf16 a[100:103], v[132:135], v[44:47], a[100:103]\n"
"v_exp_f32 v9, v9\n"
"v_mfma_f32_16x16x32_bf16 v[156:159], v[176:179], v[16:19], 0\n"
"ds_read_b64_tr_b16 v[128:129], v182 offset:256\n"
"v_cvt_pk_bf16_f32 v65, v6, v7\n"
"v_mfma_f32_16x16x32_bf16 v[156:159], v[176:179], v[20:23], v[156:159]\n"
"v_cvt_pk_bf16_f32 v72, v8, v9\n"
"s_waitcnt lgkmcnt(7)\n"
"v_mfma_f32_16x16x32_bf16 v[0:3], v[104:107], a[160:163], v[136:139]\n"
"ds_read_b64_tr_b16 v[130:131], v182 offset:4352\n"
"v_mfma_f32_16x16x32_bf16 v[0:3], v[108:111], a[164:167], v[0:3]\n"
"s_waitcnt lgkmcnt(2)\n"
"v_mfma_f32_16x16x32_bf16 v[0:3], v[112:115], a[168:171], v[0:3]\n"
"buffer_store_dwordx4 v[232:235], v185, s[48:51], s72 offen offset:128\n"
"v_mfma_f32_16x16x32_bf16 v[0:3], v[116:119], a[172:175], v[0:3]\n"
"ds_read_b64_tr_b16 v[132:133], v182 offset:8448\n"
"v_mfma_f32_16x16x32_bf16 a[8:11], v[120:123], v[16:19], 0\n"
"ds_read_b128 v[96:99], v180 offset:24576\n"
"v_mfma_f32_16x16x32_bf16 a[8:11], v[124:127], v[20:23], a[8:11]\n"
"v_exp_f32 v10, v10\n"
"v_mfma_f32_16x16x32_bf16 a[40:43], v[120:123], v[24:27], 0\n"
"ds_read_b64_tr_b16 v[134:135], v182 offset:12544\n"
"v_mfma_f32_16x16x32_bf16 a[40:43], v[124:127], v[28:31], a[40:43]\n"
"s_add_u32 m0, s53, 0x0\n"
"v_mfma_f32_16x16x32_bf16 v[4:7], v[104:107], a[176:179], v[140:143]\n"
"v_exp_f32 v11, v11\n"
"v_mfma_f32_16x16x32_bf16 v[4:7], v[108:111], a[180:183], v[4:7]\n"
"v_exp_f32 v12, v12\n"
"v_mfma_f32_16x16x32_bf16 v[4:7], v[112:115], a[184:187], v[4:7]\n"
"v_exp_f32 v13, v13\n"
"v_mfma_f32_16x16x32_bf16 v[4:7], v[116:119], a[188:191], v[4:7]\n"
"v_cvt_pk_bf16_f32 v73, v10, v11\n"
"v_cvt_pk_bf16_f32 v80, v12, v13\n"
"v_mfma_f32_16x16x32_bf16 a[72:75], v[120:123], v[32:35], 0\n"
"ds_read_b128 v[100:103], v180 offset:24832\n"
"v_mfma_f32_16x16x32_bf16 a[72:75], v[124:127], v[36:39], a[72:75]\n"
"v_exp_f32 v14, v14\n"
"v_mfma_f32_16x16x32_bf16 a[104:107], v[120:123], v[40:43], 0\n"
"buffer_load_dwordx4 v183, s[36:39], s52 offen offset:0 lds\n"
"v_mfma_f32_16x16x32_bf16 a[104:107], v[124:127], v[44:47], a[104:107]\n"
"v_exp_f32 v15, v15\n"
"v_mfma_f32_16x16x32_bf16 v[8:11], v[104:107], a[192:195], v[144:147]\n"
"ds_read_b64_tr_b16 v[120:121], v181 offset:512\n"
"v_cvt_pk_bf16_f32 v81, v14, v15\n"
"v_mfma_f32_16x16x32_bf16 v[8:11], v[108:111], a[196:199], v[8:11]\n"
"v_exp_f32 v0, v0\n"
"v_mfma_f32_16x16x32_bf16 v[8:11], v[112:115], a[200:203], v[8:11]\n"
"ds_read_b64_tr_b16 v[122:123], v181 offset:4608\n"
"v_mfma_f32_16x16x32_bf16 v[8:11], v[116:119], a[204:207], v[8:11]\n"
"s_waitcnt lgkmcnt(3)\n"
"v_mfma_f32_16x16x32_bf16 a[12:15], v[128:131], v[16:19], 0\n"
"v_exp_f32 v1, v1\n"
"v_mfma_f32_16x16x32_bf16 a[12:15], v[132:135], v[20:23], a[12:15]\n"
"ds_read_b64_tr_b16 v[124:125], v181 offset:8704\n"
"v_cvt_pk_bf16_f32 v58, v0, v1\n"
"v_mfma_f32_16x16x32_bf16 a[44:47], v[128:131], v[24:27], 0\n"
"buffer_store_dwordx4 v[236:239], v185, s[48:51], s72 offen offset:192\n"
"v_mfma_f32_16x16x32_bf16 a[44:47], v[132:135], v[28:31], a[44:47]\n"
"v_exp_f32 v2, v2\n"
"v_mfma_f32_16x16x32_bf16 v[12:15], v[112:115], a[216:219], v[148:151]\n"
"ds_read_b128 v[112:115], v180 offset:25088\n"
"v_mfma_f32_16x16x32_bf16 v[12:15], v[116:119], a[220:223], v[12:15]\n"
"ds_read_b128 v[116:119], v180 offset:25344\n"
"v_mfma_f32_16x16x32_bf16 v[12:15], v[104:107], a[208:211], v[12:15]\n"
"ds_read_b64_tr_b16 v[126:127], v181 offset:12800\n"
"v_mfma_f32_16x16x32_bf16 v[12:15], v[108:111], a[212:215], v[12:15]\n"
"v_exp_f32 v3, v3\n"
"v_mfma_f32_16x16x32_bf16 a[76:79], v[128:131], v[32:35], 0\n"
"v_exp_f32 v4, v4\n"
"v_mfma_f32_16x16x32_bf16 a[76:79], v[132:135], v[36:39], a[76:79]\n"
"v_exp_f32 v5, v5\n"
"v_mfma_f32_16x16x32_bf16 a[108:111], v[128:131], v[40:43], 0\n"
"buffer_load_dwordx4 v184, s[36:39], s52 offen offset:1024 lds\n"
"v_cvt_pk_bf16_f32 v59, v2, v3\n"
"v_mfma_f32_16x16x32_bf16 a[108:111], v[132:135], v[44:47], a[108:111]\n"
"v_exp_f32 v6, v6\n"
"v_mfma_f32_16x16x32_bf16 v[160:163], v[176:179], v[24:27], 0\n"
"ds_read_b64_tr_b16 v[128:129], v182 offset:512\n"
"v_cvt_pk_bf16_f32 v66, v4, v5\n"
"v_mfma_f32_16x16x32_bf16 v[160:163], v[176:179], v[28:31], v[160:163]\n"
"s_waitcnt lgkmcnt(7)\n"
"v_mfma_f32_16x16x32_bf16 v[0:3], v[96:99], a[160:163], v[136:139]\n"
"ds_read_b64_tr_b16 v[130:131], v182 offset:4608\n"
"v_mfma_f32_16x16x32_bf16 v[0:3], v[100:103], a[164:167], v[0:3]\n"
"s_waitcnt lgkmcnt(2)\n"
"v_mfma_f32_16x16x32_bf16 v[0:3], v[112:115], a[168:171], v[0:3]\n"
"v_exp_f32 v7, v7\n"
"v_mfma_f32_16x16x32_bf16 v[0:3], v[116:119], a[172:175], v[0:3]\n"
"ds_read_b64_tr_b16 v[132:133], v182 offset:8704\n"
"v_cvt_pk_bf16_f32 v67, v6, v7\n"
"v_mfma_f32_16x16x32_bf16 a[16:19], v[120:123], v[16:19], 0\n"
"ds_read_b128 v[104:107], v180 offset:28672\n"
"v_mfma_f32_16x16x32_bf16 a[16:19], v[124:127], v[20:23], a[16:19]\n"
"buffer_store_dwordx4 v[240:243], v185, s[48:51], s73 offen offset:0\n"
"v_mfma_f32_16x16x32_bf16 a[48:51], v[120:123], v[24:27], 0\n"
"ds_read_b64_tr_b16 v[134:135], v182 offset:12800\n"
"v_mfma_f32_16x16x32_bf16 a[48:51], v[124:127], v[28:31], a[48:51]\n"
"v_exp_f32 v8, v8\n"
"v_mfma_f32_16x16x32_bf16 v[4:7], v[96:99], a[176:179], v[140:143]\n"
"v_exp_f32 v9, v9\n"
"v_mfma_f32_16x16x32_bf16 v[4:7], v[100:103], a[180:183], v[4:7]\n"
"v_exp_f32 v10, v10\n"
"v_mfma_f32_16x16x32_bf16 v[4:7], v[112:115], a[184:187], v[4:7]\n"
"v_exp_f32 v11, v11\n"
"v_mfma_f32_16x16x32_bf16 v[4:7], v[116:119], a[188:191], v[4:7]\n"
"v_cvt_pk_bf16_f32 v74, v8, v9\n"
"v_cvt_pk_bf16_f32 v75, v10, v11\n"
"v_mfma_f32_16x16x32_bf16 a[80:83], v[120:123], v[32:35], 0\n"
"ds_read_b128 v[108:111], v180 offset:28928\n"
"v_mfma_f32_16x16x32_bf16 a[80:83], v[124:127], v[36:39], a[80:83]\n"
"buffer_load_dwordx4 v183, s[36:39], s52 offen offset:2048 lds\n"
"v_mfma_f32_16x16x32_bf16 a[112:115], v[120:123], v[40:43], 0\n"
"v_exp_f32 v12, v12\n"
"v_mfma_f32_16x16x32_bf16 a[112:115], v[124:127], v[44:47], a[112:115]\n"
"v_exp_f32 v13, v13\n"
"v_mfma_f32_16x16x32_bf16 v[8:11], v[96:99], a[192:195], v[144:147]\n"
"ds_read_b64_tr_b16 v[120:121], v181 offset:768\n"
"v_cvt_pk_bf16_f32 v82, v12, v13\n"
"v_mfma_f32_16x16x32_bf16 v[8:11], v[100:103], a[196:199], v[8:11]\n"
"v_exp_f32 v14, v14\n"
"v_mfma_f32_16x16x32_bf16 v[8:11], v[112:115], a[200:203], v[8:11]\n"
"ds_read_b64_tr_b16 v[122:123], v181 offset:4864\n"
"v_mfma_f32_16x16x32_bf16 v[8:11], v[116:119], a[204:207], v[8:11]\n"
"s_waitcnt lgkmcnt(3)\n"
"v_mfma_f32_16x16x32_bf16 a[20:23], v[128:131], v[16:19], 0\n"
"buffer_load_dwordx4 v184, s[36:39], s52 offen offset:3072 lds\n"
"v_mfma_f32_16x16x32_bf16 a[20:23], v[132:135], v[20:23], a[20:23]\n"
"ds_read_b64_tr_b16 v[124:125], v181 offset:8960\n"
"v_mfma_f32_16x16x32_bf16 a[52:55], v[128:131], v[24:27], 0\n"
"v_exp_f32 v15, v15\n"
"v_mfma_f32_16x16x32_bf16 a[52:55], v[132:135], v[28:31], a[52:55]\n"
"v_exp_f32 v0, v0\n"
"v_cvt_pk_bf16_f32 v83, v14, v15\n"
"v_mfma_f32_16x16x32_bf16 v[12:15], v[112:115], a[216:219], v[148:151]\n"
"ds_read_b128 v[112:115], v180 offset:29184\n"
"v_mfma_f32_16x16x32_bf16 v[12:15], v[116:119], a[220:223], v[12:15]\n"
"ds_read_b128 v[116:119], v180 offset:29440\n"
"v_mfma_f32_16x16x32_bf16 v[12:15], v[96:99], a[208:211], v[12:15]\n"
"ds_read_b64_tr_b16 v[126:127], v181 offset:13056\n"
"v_mfma_f32_16x16x32_bf16 v[12:15], v[100:103], a[212:215], v[12:15]\n"
"buffer_store_dwordx4 v[244:247], v185, s[48:51], s73 offen offset:64\n"
"v_mfma_f32_16x16x32_bf16 a[84:87], v[128:131], v[32:35], 0\n"
"v_exp_f32 v1, v1\n"
"v_mfma_f32_16x16x32_bf16 a[84:87], v[132:135], v[36:39], a[84:87]\n"
"v_exp_f32 v2, v2\n"
"v_mfma_f32_16x16x32_bf16 a[116:119], v[128:131], v[40:43], 0\n"
"v_exp_f32 v3, v3\n"
"v_mfma_f32_16x16x32_bf16 a[116:119], v[132:135], v[44:47], a[116:119]\n"
"v_cvt_pk_bf16_f32 v60, v0, v1\n"
"v_cvt_pk_bf16_f32 v61, v2, v3\n"
"v_mfma_f32_16x16x32_bf16 v[164:167], v[176:179], v[32:35], 0\n"
"ds_read_b64_tr_b16 v[128:129], v182 offset:768\n"
"v_mfma_f32_16x16x32_bf16 v[164:167], v[176:179], v[36:39], v[164:167]\n"
"s_waitcnt lgkmcnt(7)\n"
"v_mfma_f32_16x16x32_bf16 v[0:3], v[104:107], a[160:163], v[136:139]\n"
"ds_read_b64_tr_b16 v[130:131], v182 offset:4864\n"
"v_mfma_f32_16x16x32_bf16 v[0:3], v[108:111], a[164:167], v[0:3]\n"
"s_waitcnt lgkmcnt(2)\n"
"v_mfma_f32_16x16x32_bf16 v[0:3], v[112:115], a[168:171], v[0:3]\n"
"s_add_u32 s52, s52, 0x4000\n"
"v_mfma_f32_16x16x32_bf16 v[0:3], v[116:119], a[172:175], v[0:3]\n"
"ds_read_b64_tr_b16 v[132:133], v182 offset:8960\n"
"v_mfma_f32_16x16x32_bf16 a[24:27], v[120:123], v[16:19], 0\n"
"v_exp_f32 v4, v4\n"
"v_mfma_f32_16x16x32_bf16 a[24:27], v[124:127], v[20:23], a[24:27]\n"
"v_exp_f32 v5, v5\n"
"v_mfma_f32_16x16x32_bf16 a[56:59], v[120:123], v[24:27], 0\n"
"ds_read_b64_tr_b16 v[134:135], v182 offset:13056\n"
"v_cvt_pk_bf16_f32 v68, v4, v5\n"
"v_mfma_f32_16x16x32_bf16 a[56:59], v[124:127], v[28:31], a[56:59]\n"
"v_exp_f32 v6, v6\n"
"v_exp_f32 v7, v7\n"
"s_nop 0\n"
"v_cvt_pk_bf16_f32 v69, v6, v7\n"
"v_mfma_f32_16x16x32_bf16 v[4:7], v[104:107], a[176:179], v[140:143]\n"
"v_exp_f32 v8, v8\n"
"v_mfma_f32_16x16x32_bf16 v[4:7], v[108:111], a[180:183], v[4:7]\n"
"buffer_store_dwordx4 v[248:251], v185, s[48:51], s73 offen offset:128\n"
"v_mfma_f32_16x16x32_bf16 v[4:7], v[112:115], a[184:187], v[4:7]\n"
"v_exp_f32 v9, v9\n"
"v_mfma_f32_16x16x32_bf16 v[4:7], v[116:119], a[188:191], v[4:7]\n"
"v_exp_f32 v10, v10\n"
"v_mfma_f32_16x16x32_bf16 a[88:91], v[120:123], v[32:35], 0\n"
"v_exp_f32 v11, v11\n"
"v_mfma_f32_16x16x32_bf16 a[88:91], v[124:127], v[36:39], a[88:91]\n"
"v_cvt_pk_bf16_f32 v76, v8, v9\n"
"v_cvt_pk_bf16_f32 v77, v10, v11\n"
"v_mfma_f32_16x16x32_bf16 a[120:123], v[120:123], v[40:43], 0\n"
"v_exp_f32 v12, v12\n"
"v_mfma_f32_16x16x32_bf16 a[120:123], v[124:127], v[44:47], a[120:123]\n"
"v_exp_f32 v13, v13\n"
"v_mfma_f32_16x16x32_bf16 v[8:11], v[104:107], a[192:195], v[144:147]\n"
"v_exp_f32 v14, v14\n"
"v_mfma_f32_16x16x32_bf16 v[8:11], v[108:111], a[196:199], v[8:11]\n"
"v_exp_f32 v15, v15\n"
"v_mfma_f32_16x16x32_bf16 v[8:11], v[112:115], a[200:203], v[8:11]\n"
"v_cvt_pk_bf16_f32 v84, v12, v13\n"
"v_cvt_pk_bf16_f32 v85, v14, v15\n"
"v_mfma_f32_16x16x32_bf16 v[8:11], v[116:119], a[204:207], v[8:11]\n"
"s_waitcnt vmcnt(31) lgkmcnt(0)\n"
"v_xor_b32 v180, 0x8000, v180\n"
"s_barrier\n"
"v_mfma_f32_16x16x32_bf16 a[28:31], v[128:131], v[16:19], 0\n"
"ds_read_b128 v[96:99], v180 offset:0\n"
"v_mfma_f32_16x16x32_bf16 a[28:31], v[132:135], v[20:23], a[28:31]\n"
"ds_read_b128 v[100:103], v180 offset:256\n"
"v_mfma_f32_16x16x32_bf16 a[60:63], v[128:131], v[24:27], 0\n"
"ds_read_b64_tr_b16 v[120:121], v181 offset:16384\n"
"ds_read_b64_tr_b16 v[124:125], v181 offset:24576\n"
"v_mfma_f32_16x16x32_bf16 a[60:63], v[132:135], v[28:31], a[60:63]\n"
"ds_read_b64_tr_b16 v[122:123], v181 offset:20480\n"
"ds_read_b64_tr_b16 v[126:127], v181 offset:28672\n"
"v_mfma_f32_16x16x32_bf16 v[12:15], v[112:115], a[216:219], v[148:151]\n"
"ds_read_b128 v[112:115], v180 offset:512\n"
"v_mfma_f32_16x16x32_bf16 v[12:15], v[116:119], a[220:223], v[12:15]\n"
"ds_read_b128 v[116:119], v180 offset:768\n"
"v_mfma_f32_16x16x32_bf16 v[12:15], v[104:107], a[208:211], v[12:15]\n"
"s_add_u32 m0, s54, 0x0\n"
"v_mfma_f32_16x16x32_bf16 v[12:15], v[108:111], a[212:215], v[12:15]\n"
"buffer_load_dwordx4 v183, s[40:43], s52 offen offset:0 lds\n"
"v_mfma_f32_16x16x32_bf16 a[92:95], v[128:131], v[32:35], 0\n"
"buffer_store_dwordx4 v[252:255], v185, s[48:51], s73 offen offset:192\n"
"v_mfma_f32_16x16x32_bf16 a[92:95], v[132:135], v[36:39], a[92:95]\n"
"v_exp_f32 v0, v0\n"
"v_mfma_f32_16x16x32_bf16 a[124:127], v[128:131], v[40:43], 0\n"
"v_exp_f32 v1, v1\n"
"v_mfma_f32_16x16x32_bf16 a[124:127], v[132:135], v[44:47], a[124:127]\n"
"v_exp_f32 v2, v2\n"
"v_mfma_f32_16x16x32_bf16 v[168:171], v[176:179], v[40:43], 0\n"
"v_exp_f32 v3, v3\n"
"v_mfma_f32_16x16x32_bf16 v[168:171], v[176:179], v[44:47], v[168:171]\n"
"v_cvt_pk_bf16_f32 v62, v0, v1\n"
"v_cvt_pk_bf16_f32 v63, v2, v3\n"
"v_exp_f32 v4, v4\n"
"v_exp_f32 v5, v5\n"
"v_exp_f32 v6, v6\n"
"v_exp_f32 v7, v7\n"
"v_exp_f32 v8, v8\n"
"v_exp_f32 v9, v9\n"
"v_exp_f32 v10, v10\n"
"v_exp_f32 v11, v11\n"
"s_nop 0\n"
"v_cvt_pk_bf16_f32 v70, v4, v5\n"
"v_cvt_pk_bf16_f32 v71, v6, v7\n"
"v_cvt_pk_bf16_f32 v78, v8, v9\n"
"v_cvt_pk_bf16_f32 v79, v10, v11\n"
"s_waitcnt lgkmcnt(2)\n"
"s_mov_b32 s55, 23\n"
"s_mov_b32 s59, s53\n"
"s_xor_b32 s58, s53, 0x8000\n"
"s_add_u32 s48, s48, 155648\n"
"s_addc_u32 s49, s49, 0\n"
"s_branch L_loop_b_%=\n"
"L_final_b_%=:\n"
"s_waitcnt lgkmcnt(0)\n"
"ds_read_b64_tr_b16 v[16:17], v182 offset:16384\n"
"ds_read_b64_tr_b16 v[18:19], v182 offset:20480\n"
"ds_read_b64_tr_b16 v[20:21], v182 offset:24576\n"
"ds_read_b64_tr_b16 v[22:23], v182 offset:28672\n"
"s_nop 3\n"
"v_exp_f32 v12, v12\n"
"v_exp_f32 v13, v13\n"
"v_exp_f32 v14, v14\n"
"v_exp_f32 v15, v15\n"
"s_nop 0\n"
"v_cvt_pk_bf16_f32 v86, v12, v13\n"
"v_cvt_pk_bf16_f32 v87, v14, v15\n"
"s_nop 1\n"
"ds_read_b64_tr_b16 v[24:25], v181 offset:16640\n"
"ds_read_b64_tr_b16 v[26:27], v181 offset:20736\n"
"ds_read_b64_tr_b16 v[28:29], v181 offset:24832\n"
"ds_read_b64_tr_b16 v[30:31], v181 offset:28928\n"
"ds_read_b64_tr_b16 v[32:33], v182 offset:16640\n"
"ds_read_b64_tr_b16 v[34:35], v182 offset:20736\n"
"ds_read_b64_tr_b16 v[36:37], v182 offset:24832\n"
"ds_read_b64_tr_b16 v[38:39], v182 offset:28928\n"
"ds_read_b64_tr_b16 v[40:41], v181 offset:16896\n"
"ds_read_b64_tr_b16 v[42:43], v181 offset:20992\n"
"ds_read_b64_tr_b16 v[44:45], v181 offset:25088\n"
"ds_read_b64_tr_b16 v[46:47], v181 offset:29184\n"
"ds_read_b64_tr_b16 v[48:49], v182 offset:16896\n"
"ds_read_b64_tr_b16 v[50:51], v182 offset:20992\n"
"ds_read_b64_tr_b16 v[52:53], v182 offset:25088\n"
"ds_read_b64_tr_b16 v[54:55], v182 offset:29184\n"
"ds_read_b64_tr_b16 v[96:97], v181 offset:17152\n"
"ds_read_b64_tr_b16 v[98:99], v181 offset:21248\n"
"ds_read_b64_tr_b16 v[100:101], v181 offset:25344\n"
"ds_read_b64_tr_b16 v[102:103], v181 offset:29440\n"
"ds_read_b64_tr_b16 v[104:105], v182 offset:17152\n"
"ds_read_b64_tr_b16 v[106:107], v182 offset:21248\n"
"ds_read_b64_tr_b16 v[108:109], v182 offset:25344\n"
"ds_read_b64_tr_b16 v[110:111], v182 offset:29440\n"
"v_mbcnt_lo_u32_b32 v0, -1, 0\n"
"v_mbcnt_hi_u32_b32 v0, -1, v0\n"
"v_and_b32 v1, 15, v0\n"
"v_lshrrev_b32 v2, 4, v0\n"
"v_and_b32 v3, 1, v2\n"
"v_lshrrev_b32 v4, 1, v2\n"
"v_lshl_add_u32 v3, v3, 1, v4\n"
"v_mov_b32 v7, 1.0\n"
"s_lshl_b32 s62, s64, 13\n"
"s_add_u32 s62, s62, 0x20000\n"
"v_add_u32 v8, v1, v3\n"
"v_and_b32 v8, 3, v8\n"
"v_lshlrev_b32 v8, 4, v8\n"
"v_lshl_add_u32 v8, v1, 8, v8\n"
"v_bfe_u32 v9, v1, 2, 1\n"
"v_lshl_add_u32 v186, v9, 6, v8\n"
"v_add_u32 v186, s62, v186\n"
"v_xor_b32 v187, 64, v186\n"
"v_lshlrev_b32 v188, 4, v0\n"
"v_add_u32 v188, s62, v188\n"
"v_sub_u32 v8, v1, v2\n"
"v_and_b32 v8, 3, v8\n"
"v_and_b32 v9, 12, v1\n"
"v_or_b32 v8, v8, v9\n"
"v_lshlrev_b32 v8, 4, v8\n"
"v_lshl_add_u32 v190, v2, 8, v8\n"
"v_xor_b32 v191, 64, v190\n"
"s_waitcnt lgkmcnt(0)\n"
"v_mfma_f32_16x16x32_bf16 v[156:159], v[176:179], v[56:59], v[156:159]\n"
"v_mfma_f32_16x16x32_bf16 v[156:159], v[176:179], v[60:63], v[156:159]\n"
"v_mfma_f32_16x16x32_bf16 a[0:3], v[120:123], v[56:59], a[0:3]\n"
"v_mfma_f32_16x16x32_bf16 a[0:3], v[124:127], v[60:63], a[0:3]\n"
"v_mfma_f32_16x16x32_bf16 a[4:7], v[16:19], v[56:59], a[4:7]\n"
"v_mfma_f32_16x16x32_bf16 a[4:7], v[20:23], v[60:63], a[4:7]\n"
"v_mfma_f32_16x16x32_bf16 a[8:11], v[24:27], v[56:59], a[8:11]\n"
"v_mfma_f32_16x16x32_bf16 a[8:11], v[28:31], v[60:63], a[8:11]\n"
"v_mfma_f32_16x16x32_bf16 a[12:15], v[32:35], v[56:59], a[12:15]\n"
"v_mfma_f32_16x16x32_bf16 a[12:15], v[36:39], v[60:63], a[12:15]\n"
"v_mfma_f32_16x16x4_f32 v[136:139], v7, v156, 0\n"
"v_cmp_ngt_f32 vcc, s65, v156\n"
"s_or_b64 s[60:61], s[60:61], vcc\n"
"v_mfma_f32_16x16x32_bf16 a[16:19], v[40:43], v[56:59], a[16:19]\n"
"v_mfma_f32_16x16x32_bf16 a[16:19], v[44:47], v[60:63], a[16:19]\n"
"v_mfma_f32_16x16x32_bf16 a[20:23], v[48:51], v[56:59], a[20:23]\n"
"v_mfma_f32_16x16x32_bf16 a[20:23], v[52:55], v[60:63], a[20:23]\n"
"v_mfma_f32_16x16x32_bf16 a[24:27], v[96:99], v[56:59], a[24:27]\n"
"v_mfma_f32_16x16x32_bf16 a[24:27], v[100:103], v[60:63], a[24:27]\n"
"v_mfma_f32_16x16x32_bf16 a[28:31], v[104:107], v[56:59], a[28:31]\n"
"v_mfma_f32_16x16x32_bf16 a[28:31], v[108:111], v[60:63], a[28:31]\n"
"s_nop 3\n"
"v_rcp_f32 v136, v136\n"
"v_accvgpr_read_b32 v192, a0\n"
"v_accvgpr_read_b32 v193, a1\n"
"v_accvgpr_read_b32 v194, a2\n"
"v_accvgpr_read_b32 v195, a3\n"
"v_accvgpr_read_b32 v196, a4\n"
"v_accvgpr_read_b32 v197, a5\n"
"v_accvgpr_read_b32 v198, a6\n"
"v_accvgpr_read_b32 v199, a7\n"
"v_pk_mul_f32 v[192:193], v[192:193], v[136:137] op_sel_hi:[1,0]\n"
"v_pk_mul_f32 v[194:195], v[194:195], v[136:137] op_sel_hi:[1,0]\n"
"v_pk_mul_f32 v[196:197], v[196:197], v[136:137] op_sel_hi:[1,0]\n"
"v_pk_mul_f32 v[198:199], v[198:199], v[136:137] op_sel_hi:[1,0]\n"
"v_cvt_pk_bf16_f32 v208, v192, v193\n"
"v_cvt_pk_bf16_f32 v209, v194, v195\n"
"v_cvt_pk_bf16_f32 v210, v196, v197\n"
"v_cvt_pk_bf16_f32 v211, v198, v199\n"
"v_accvgpr_read_b32 v200, a8\n"
"v_accvgpr_read_b32 v201, a9\n"
"v_accvgpr_read_b32 v202, a10\n"
"v_permlane16_swap_b32 v208, v210\n"
"v_permlane16_swap_b32 v209, v211\n"
"v_accvgpr_read_b32 v203, a11\n"
"v_accvgpr_read_b32 v204, a12\n"
"v_accvgpr_read_b32 v205, a13\n"
"v_accvgpr_read_b32 v206, a14\n"
"v_accvgpr_read_b32 v207, a15\n"
"v_pk_mul_f32 v[200:201], v[200:201], v[136:137] op_sel_hi:[1,0]\n"
"v_pk_mul_f32 v[202:203], v[202:203], v[136:137] op_sel_hi:[1,0]\n"
"v_pk_mul_f32 v[204:205], v[204:205], v[136:137] op_sel_hi:[1,0]\n"
"v_pk_mul_f32 v[206:207], v[206:207], v[136:137] op_sel_hi:[1,0]\n"
"v_cvt_pk_bf16_f32 v212, v200, v201\n"
"v_cvt_pk_bf16_f32 v213, v202, v203\n"
"v_cvt_pk_bf16_f32 v214, v204, v205\n"
"v_cvt_pk_bf16_f32 v215, v206, v207\n"
"ds_write_b128 v186, v[208:211] offset:0\n"
"v_accvgpr_read_b32 v192, a16\n"
"v_accvgpr_read_b32 v193, a17\n"
"v_accvgpr_read_b32 v194, a18\n"
"v_permlane16_swap_b32 v212, v214\n"
"v_permlane16_swap_b32 v213, v215\n"
"v_accvgpr_read_b32 v195, a19\n"
"v_accvgpr_read_b32 v196, a20\n"
"v_accvgpr_read_b32 v197, a21\n"
"v_accvgpr_read_b32 v198, a22\n"
"v_accvgpr_read_b32 v199, a23\n"
"v_pk_mul_f32 v[192:193], v[192:193], v[136:137] op_sel_hi:[1,0]\n"
"v_pk_mul_f32 v[194:195], v[194:195], v[136:137] op_sel_hi:[1,0]\n"
"v_pk_mul_f32 v[196:197], v[196:197], v[136:137] op_sel_hi:[1,0]\n"
"v_pk_mul_f32 v[198:199], v[198:199], v[136:137] op_sel_hi:[1,0]\n"
"v_cvt_pk_bf16_f32 v216, v192, v193\n"
"v_cvt_pk_bf16_f32 v217, v194, v195\n"
"v_cvt_pk_bf16_f32 v218, v196, v197\n"
"v_cvt_pk_bf16_f32 v219, v198, v199\n"
"ds_write_b128 v187, v[212:215] offset:0\n"
"v_accvgpr_read_b32 v200, a24\n"
"v_accvgpr_read_b32 v201, a25\n"
"v_accvgpr_read_b32 v202, a26\n"
"v_permlane16_swap_b32 v216, v218\n"
"v_permlane16_swap_b32 v217, v219\n"
"v_accvgpr_read_b32 v203, a27\n"
"v_accvgpr_read_b32 v204, a28\n"
"v_accvgpr_read_b32 v205, a29\n"
"v_accvgpr_read_b32 v206, a30\n"
"v_accvgpr_read_b32 v207, a31\n"
"v_pk_mul_f32 v[200:201], v[200:201], v[136:137] op_sel_hi:[1,0]\n"
"v_pk_mul_f32 v[202:203], v[202:203], v[136:137] op_sel_hi:[1,0]\n"
"v_pk_mul_f32 v[204:205], v[204:205], v[136:137] op_sel_hi:[1,0]\n"
"v_pk_mul_f32 v[206:207], v[206:207], v[136:137] op_sel_hi:[1,0]\n"
"v_cvt_pk_bf16_f32 v220, v200, v201\n"
"v_cvt_pk_bf16_f32 v221, v202, v203\n"
"v_cvt_pk_bf16_f32 v222, v204, v205\n"
"v_cvt_pk_bf16_f32 v223, v206, v207\n"
"ds_write_b128 v186, v[216:219] offset:128\n"
"s_nop 1\n"
"v_permlane16_swap_b32 v220, v222\n"
"v_permlane16_swap_b32 v221, v223\n"
"ds_write_b128 v187, v[220:223] offset:128\n"
"ds_read_b128 v[240:243], v188 offset:0\n"
"ds_read_b128 v[244:247], v188 offset:1024\n"
"ds_read_b128 v[248:251], v188 offset:2048\n"
"ds_read_b128 v[252:255], v188 offset:3072\n"
"v_mfma_f32_16x16x32_bf16 v[160:163], v[176:179], v[64:67], v[160:163]\n"
"v_mfma_f32_16x16x32_bf16 v[160:163], v[176:179], v[68:71], v[160:163]\n"
"v_mfma_f32_16x16x32_bf16 a[32:35], v[120:123], v[64:67], a[32:35]\n"
"v_mfma_f32_16x16x32_bf16 a[32:35], v[124:127], v[68:71], a[32:35]\n"
"v_mfma_f32_16x16x32_bf16 a[36:39], v[16:19], v[64:67], a[36:39]\n"
"v_mfma_f32_16x16x32_bf16 a[36:39], v[20:23], v[68:71], a[36:39]\n"
"v_mfma_f32_16x16x32_bf16 a[40:43], v[24:27], v[64:67], a[40:43]\n"
"v_mfma_f32_16x16x32_bf16 a[40:43], v[28:31], v[68:71], a[40:43]\n"
"v_mfma_f32_16x16x32_bf16 a[44:47], v[32:35], v[64:67], a[44:47]\n"
"v_mfma_f32_16x16x32_bf16 a[44:47], v[36:39], v[68:71], a[44:47]\n"
"v_mfma_f32_16x16x4_f32 v[140:143], v7, v160, 0\n"
"v_cmp_ngt_f32 vcc, s65, v160\n"
"s_or_b64 s[60:61], s[60:61], vcc\n"
"v_mfma_f32_16x16x32_bf16 a[48:51], v[40:43], v[64:67], a[48:51]\n"
"v_mfma_f32_16x16x32_bf16 a[48:51], v[44:47], v[68:71], a[48:51]\n"
"v_mfma_f32_16x16x32_bf16 a[52:55], v[48:51], v[64:67], a[52:55]\n"
"v_mfma_f32_16x16x32_bf16 a[52:55], v[52:55], v[68:71], a[52:55]\n"
"v_mfma_f32_16x16x32_bf16 a[56:59], v[96:99], v[64:67], a[56:59]\n"
"v_mfma_f32_16x16x32_bf16 a[56:59], v[100:103], v[68:71], a[56:59]\n"
"v_mfma_f32_16x16x32_bf16 a[60:63], v[104:107], v[64:67], a[60:63]\n"
"v_mfma_f32_16x16x32_bf16 a[60:63], v[108:111], v[68:71], a[60:63]\n"
"s_nop 3\n"
"v_rcp_f32 v140, v140\n"
"v_accvgpr_read_b32 v192, a32\n"
"v_accvgpr_read_b32 v193, a33\n"
"v_accvgpr_read_b32 v194, a34\n"
"v_accvgpr_read_b32 v195, a35\n"
"v_accvgpr_read_b32 v196, a36\n"
"v_accvgpr_read_b32 v197, a37\n"
"v_accvgpr_read_b32 v198, a38\n"
"v_accvgpr_read_b32 v199, a39\n"
"v_pk_mul_f32 v[192:193], v[192:193], v[140:141] op_sel_hi:[1,0]\n"
"v_pk_mul_f32 v[194:195], v[194:195], v[140:141] op_sel_hi:[1,0]\n"
"v_pk_mul_f32 v[196:197], v[196:197], v[140:141] op_sel_hi:[1,0]\n"
"v_pk_mul_f32 v[198:199], v[198:199], v[140:141] op_sel_hi:[1,0]\n"
"v_cvt_pk_bf16_f32 v224, v192, v193\n"
"v_cvt_pk_bf16_f32 v225, v194, v195\n"
"v_cvt_pk_bf16_f32 v226, v196, v197\n"
"v_cvt_pk_bf16_f32 v227, v198, v199\n"
"v_accvgpr_read_b32 v200, a40\n"
"v_accvgpr_read_b32 v201, a41\n"
"v_accvgpr_read_b32 v202, a42\n"
"v_permlane16_swap_b32 v224, v226\n"
"v_permlane16_swap_b32 v225, v227\n"
"v_accvgpr_read_b32 v203, a43\n"
"v_accvgpr_read_b32 v204, a44\n"
"v_accvgpr_read_b32 v205, a45\n"
"v_accvgpr_read_b32 v206, a46\n"
"v_accvgpr_read_b32 v207, a47\n"
"v_pk_mul_f32 v[200:201], v[200:201], v[140:141] op_sel_hi:[1,0]\n"
"v_pk_mul_f32 v[202:203], v[202:203], v[140:141] op_sel_hi:[1,0]\n"
"v_pk_mul_f32 v[204:205], v[204:205], v[140:141] op_sel_hi:[1,0]\n"
"v_pk_mul_f32 v[206:207], v[206:207], v[140:141] op_sel_hi:[1,0]\n"
"v_cvt_pk_bf16_f32 v228, v200, v201\n"
"v_cvt_pk_bf16_f32 v229, v202, v203\n"
"v_cvt_pk_bf16_f32 v230, v204, v205\n"
"v_cvt_pk_bf16_f32 v231, v206, v207\n"
"ds_write_b128 v186, v[224:227] offset:4096\n"
"s_waitcnt lgkmcnt(1)\n"
"buffer_store_dwordx4 v[240:243], v190, s[48:51], s70 offen offset:0 sc1\n"
"v_accvgpr_read_b32 v192, a48\n"
"v_accvgpr_read_b32 v193, a49\n"
"v_accvgpr_read_b32 v194, a50\n"
"v_permlane16_swap_b32 v228, v230\n"
"v_permlane16_swap_b32 v229, v231\n"
"v_accvgpr_read_b32 v195, a51\n"
"v_accvgpr_read_b32 v196, a52\n"
"v_accvgpr_read_b32 v197, a53\n"
"v_accvgpr_read_b32 v198, a54\n"
"v_accvgpr_read_b32 v199, a55\n"
"v_pk_mul_f32 v[192:193], v[192:193], v[140:141] op_sel_hi:[1,0]\n"
"v_pk_mul_f32 v[194:195], v[194:195], v[140:141] op_sel_hi:[1,0]\n"
"v_pk_mul_f32 v[196:197], v[196:197], v[140:141] op_sel_hi:[1,0]\n"
"v_pk_mul_f32 v[198:199], v[198:199], v[140:141] op_sel_hi:[1,0]\n"
"v_cvt_pk_bf16_f32 v232, v192, v193\n"
"v_cvt_pk_bf16_f32 v233, v194, v195\n"
"v_cvt_pk_bf16_f32 v234, v196, v197\n"
"v_cvt_pk_bf16_f32 v235, v198, v199\n"
"ds_write_b128 v187, v[228:231] offset:4096\n"
"buffer_store_dwordx4 v[244:247], v191, s[48:51], s70 offen offset:1024 sc1\n"
"v_accvgpr_read_b32 v200, a56\n"
"v_accvgpr_read_b32 v201, a57\n"
"v_accvgpr_read_b32 v202, a58\n"
"v_permlane16_swap_b32 v232, v234\n"
"v_permlane16_swap_b32 v233, v235\n"
"v_accvgpr_read_b32 v203, a59\n"
"v_accvgpr_read_b32 v204, a60\n"
"v_accvgpr_read_b32 v205, a61\n"
"v_accvgpr_read_b32 v206, a62\n"
"v_accvgpr_read_b32 v207, a63\n"
"v_pk_mul_f32 v[200:201], v[200:201], v[140:141] op_sel_hi:[1,0]\n"
"v_pk_mul_f32 v[202:203], v[202:203], v[140:141] op_sel_hi:[1,0]\n"
"v_pk_mul_f32 v[204:205], v[204:205], v[140:141] op_sel_hi:[1,0]\n"
"v_pk_mul_f32 v[206:207], v[206:207], v[140:141] op_sel_hi:[1,0]\n"
"v_cvt_pk_bf16_f32 v236, v200, v201\n"
"v_cvt_pk_bf16_f32 v237, v202, v203\n"
"v_cvt_pk_bf16_f32 v238, v204, v205\n"
"v_cvt_pk_bf16_f32 v239, v206, v207\n"
"ds_write_b128 v186, v[232:235] offset:4224\n"
"buffer_store_dwordx4 v[248:251], v190, s[48:51], s70 offen offset:2048 sc1\n"
"s_nop 1\n"
"v_permlane16_swap_b32 v236, v238\n"
"v_permlane16_swap_b32 v237, v239\n"
"ds_write_b128 v187, v[236:239] offset:4224\n"
"buffer_store_dwordx4 v[252:255], v191, s[48:51], s70 offen offset:3072 sc1\n"
"ds_read_b128 a[240:243], v188 offset:4096\n"
"ds_read_b128 a[244:247], v188 offset:5120\n"
"ds_read_b128 a[248:251], v188 offset:6144\n"
"ds_read_b128 a[252:255], v188 offset:7168\n"
"v_mfma_f32_16x16x32_bf16 v[164:167], v[176:179], v[72:75], v[164:167]\n"
"v_mfma_f32_16x16x32_bf16 v[164:167], v[176:179], v[76:79], v[164:167]\n"
"v_mfma_f32_16x16x32_bf16 a[64:67], v[120:123], v[72:75], a[64:67]\n"
"v_mfma_f32_16x16x32_bf16 a[64:67], v[124:127], v[76:79], a[64:67]\n"
"v_mfma_f32_16x16x32_bf16 a[68:71], v[16:19], v[72:75], a[68:71]\n"
"v_mfma_f32_16x16x32_bf16 a[68:71], v[20:23], v[76:79], a[68:71]\n"
"v_mfma_f32_16x16x32_bf16 a[72:75], v[24:27], v[72:75], a[72:75]\n"
"v_mfma_f32_16x16x32_bf16 a[72:75], v[28:31], v[76:79], a[72:75]\n"
"v_mfma_f32_16x16x32_bf16 a[76:79], v[32:35], v[72:75], a[76:79]\n"
"v_mfma_f32_16x16x32_bf16 a[76:79], v[36:39], v[76:79], a[76:79]\n"
"v_mfma_f32_16x16x4_f32 v[144:147], v7, v164, 0\n"
"v_cmp_ngt_f32 vcc, s65, v164\n"
"s_or_b64 s[60:61], s[60:61], vcc\n"
"v_mfma_f32_16x16x32_bf16 a[80:83], v[40:43], v[72:75], a[80:83]\n"
"v_mfma_f32_16x16x32_bf16 a[80:83], v[44:47], v[76:79], a[80:83]\n"
"v_mfma_f32_16x16x32_bf16 a[84:87], v[48:51], v[72:75], a[84:87]\n"
"v_mfma_f32_16x16x32_bf16 a[84:87], v[52:55], v[76:79], a[84:87]\n"
"v_mfma_f32_16x16x32_bf16 a[88:91], v[96:99], v[72:75], a[88:91]\n"
"v_mfma_f32_16x16x32_bf16 a[88:91], v[100:103], v[76:79], a[88:91]\n"
"v_mfma_f32_16x16x32_bf16 a[92:95], v[104:107], v[72:75], a[92:95]\n"
"v_mfma_f32_16x16x32_bf16 a[92:95], v[108:111], v[76:79], a[92:95]\n"
"s_nop 3\n"
"v_rcp_f32 v144, v144\n"
"v_accvgpr_read_b32 v192, a64\n"
"v_accvgpr_read_b32 v193, a65\n"
"v_accvgpr_read_b32 v194, a66\n"
"v_accvgpr_read_b32 v195, a67\n"
"v_accvgpr_read_b32 v196, a68\n"
"v_accvgpr_read_b32 v197, a69\n"
"v_accvgpr_read_b32 v198, a70\n"
"v_accvgpr_read_b32 v199, a71\n"
"v_pk_mul_f32 v[192:193], v[192:193], v[144:145] op_sel_hi:[1,0]\n"
"v_pk_mul_f32 v[194:195], v[194:195], v[144:145] op_sel_hi:[1,0]\n"
"v_pk_mul_f32 v[196:197], v[196:197], v[144:145] op_sel_hi:[1,0]\n"
"v_pk_mul_f32 v[198:199], v[198:199], v[144:145] op_sel_hi:[1,0]\n"
"v_cvt_pk_bf16_f32 v208, v192, v193\n"
"v_cvt_pk_bf16_f32 v209, v194, v195\n"
"v_cvt_pk_bf16_f32 v210, v196, v197\n"
"v_cvt_pk_bf16_f32 v211, v198, v199\n"
"v_accvgpr_read_b32 v200, a72\n"
"v_accvgpr_read_b32 v201, a73\n"
"v_accvgpr_read_b32 v202, a74\n"
"v_permlane16_swap_b32 v208, v210\n"
"v_permlane16_swap_b32 v209, v211\n"
"v_accvgpr_read_b32 v203, a75\n"
"v_accvgpr_read_b32 v204, a76\n"
"v_accvgpr_read_b32 v205, a77\n"
"v_accvgpr_read_b32 v206, a78\n"
"v_accvgpr_read_b32 v207, a79\n"
"v_pk_mul_f32 v[200:201], v[200:201], v[144:145] op_sel_hi:[1,0]\n"
"v_pk_mul_f32 v[202:203], v[202:203], v[144:145] op_sel_hi:[1,0]\n"
"v_pk_mul_f32 v[204:205], v[204:205], v[144:145] op_sel_hi:[1,0]\n"
"v_pk_mul_f32 v[206:207], v[206:207], v[144:145] op_sel_hi:[1,0]\n"
"v_cvt_pk_bf16_f32 v212, v200, v201\n"
"v_cvt_pk_bf16_f32 v213, v202, v203\n"
"v_cvt_pk_bf16_f32 v214, v204, v205\n"
"v_cvt_pk_bf16_f32 v215, v206, v207\n"
"ds_write_b128 v186, v[208:211] offset:0\n"
"s_waitcnt lgkmcnt(1)\n"
"buffer_store_dwordx4 a[240:243], v190, s[48:51], s71 offen offset:0 sc1\n"
"v_accvgpr_read_b32 v192, a80\n"
"v_accvgpr_read_b32 v193, a81\n"
"v_accvgpr_read_b32 v194, a82\n"
"v_permlane16_swap_b32 v212, v214\n"
"v_permlane16_swap_b32 v213, v215\n"
"v_accvgpr_read_b32 v195, a83\n"
"v_accvgpr_read_b32 v196, a84\n"
"v_accvgpr_read_b32 v197, a85\n"
"v_accvgpr_read_b32 v198, a86\n"
"v_accvgpr_read_b32 v199, a87\n"
"v_pk_mul_f32 v[192:193], v[192:193], v[144:145] op_sel_hi:[1,0]\n"
"v_pk_mul_f32 v[194:195], v[194:195], v[144:145] op_sel_hi:[1,0]\n"
"v_pk_mul_f32 v[196:197], v[196:197], v[144:145] op_sel_hi:[1,0]\n"
"v_pk_mul_f32 v[198:199], v[198:199], v[144:145] op_sel_hi:[1,0]\n"
"v_cvt_pk_bf16_f32 v216, v192, v193\n"
"v_cvt_pk_bf16_f32 v217, v194, v195\n"
"v_cvt_pk_bf16_f32 v218, v196, v197\n"
"v_cvt_pk_bf16_f32 v219, v198, v199\n"
"ds_write_b128 v187, v[212:215] offset:0\n"
"buffer_store_dwordx4 a[244:247], v191, s[48:51], s71 offen offset:1024 sc1\n"
"v_accvgpr_read_b32 v200, a88\n"
"v_accvgpr_read_b32 v201, a89\n"
"v_accvgpr_read_b32 v202, a90\n"
"v_permlane16_swap_b32 v216, v218\n"
"v_permlane16_swap_b32 v217, v219\n"
"v_accvgpr_read_b32 v203, a91\n"
"v_accvgpr_read_b32 v204, a92\n"
"v_accvgpr_read_b32 v205, a93\n"
"v_accvgpr_read_b32 v206, a94\n"
"v_accvgpr_read_b32 v207, a95\n"
"v_pk_mul_f32 v[200:201], v[200:201], v[144:145] op_sel_hi:[1,0]\n"
"v_pk_mul_f32 v[202:203], v[202:203], v[144:145] op_sel_hi:[1,0]\n"
"v_pk_mul_f32 v[204:205], v[204:205], v[144:145] op_sel_hi:[1,0]\n"
"v_pk_mul_f32 v[206:207], v[206:207], v[144:145] op_sel_hi:[1,0]\n"
"v_cvt_pk_bf16_f32 v220, v200, v201\n"
"v_cvt_pk_bf16_f32 v221, v202, v203\n"
"v_cvt_pk_bf16_f32 v222, v204, v205\n"
"v_cvt_pk_bf16_f32 v223, v206, v207\n"
"ds_write_b128 v186, v[216:219] offset:128\n"
"buffer_store_dwordx4 a[248:251], v190, s[48:51], s71 offen offset:2048 sc1\n"
"s_nop 1\n"
"v_permlane16_swap_b32 v220, v222\n"
"v_permlane16_swap_b32 v221, v223\n"
"ds_write_b128 v187, v[220:223] offset:128\n"
"buffer_store_dwordx4 a[252:255], v191, s[48:51], s71 offen offset:3072 sc1\n"
"ds_read_b128 v[240:243], v188 offset:0\n"
"ds_read_b128 v[244:247], v188 offset:1024\n"
"ds_read_b128 v[248:251], v188 offset:2048\n"
"ds_read_b128 v[252:255], v188 offset:3072\n"
"v_mfma_f32_16x16x32_bf16 v[168:171], v[176:179], v[80:83], v[168:171]\n"
"v_mfma_f32_16x16x32_bf16 v[168:171], v[176:179], v[84:87], v[168:171]\n"
"v_mfma_f32_16x16x32_bf16 a[96:99], v[120:123], v[80:83], a[96:99]\n"
"v_mfma_f32_16x16x32_bf16 a[96:99], v[124:127], v[84:87], a[96:99]\n"
"v_mfma_f32_16x16x32_bf16 a[100:103], v[16:19], v[80:83], a[100:103]\n"
"v_mfma_f32_16x16x32_bf16 a[100:103], v[20:23], v[84:87], a[100:103]\n"
"v_mfma_f32_16x16x32_bf16 a[104:107], v[24:27], v[80:83], a[104:107]\n"
"v_mfma_f32_16x16x32_bf16 a[104:107], v[28:31], v[84:87], a[104:107]\n"
"v_mfma_f32_16x16x32_bf16 a[108:111], v[32:35], v[80:83], a[108:111]\n"
"v_mfma_f32_16x16x32_bf16 a[108:111], v[36:39], v[84:87], a[108:111]\n"
"v_mfma_f32_16x16x4_f32 v[148:151], v7, v168, 0\n"
"v_cmp_ngt_f32 vcc, s65, v168\n"
"s_or_b64 s[60:61], s[60:61], vcc\n"
"v_mfma_f32_16x16x32_bf16 a[112:115], v[40:43], v[80:83], a[112:115]\n"
"v_mfma_f32_16x16x32_bf16 a[112:115], v[44:47], v[84:87], a[112:115]\n"
"v_mfma_f32_16x16x32_bf16 a[116:119], v[48:51], v[80:83], a[116:119]\n"
"v_mfma_f32_16x16x32_bf16 a[116:119], v[52:55], v[84:87], a[116:119]\n"
"v_mfma_f32_16x16x32_bf16 a[120:123], v[96:99], v[80:83], a[120:123]\n"
"v_mfma_f32_16x16x32_bf16 a[120:123], v[100:103], v[84:87], a[120:123]\n"
"v_mfma_f32_16x16x32_bf16 a[124:127], v[104:107], v[80:83], a[124:127]\n"
"v_mfma_f32_16x16x32_bf16 a[124:127], v[108:111], v[84:87], a[124:127]\n"
"s_nop 3\n"
"v_rcp_f32 v148, v148\n"
"v_accvgpr_read_b32 v192, a96\n"
"v_accvgpr_read_b32 v193, a97\n"
"v_accvgpr_read_b32 v194, a98\n"
"v_accvgpr_read_b32 v195, a99\n"
"v_accvgpr_read_b32 v196, a100\n"
"v_accvgpr_read_b32 v197, a101\n"
"v_accvgpr_read_b32 v198, a102\n"
"v_accvgpr_read_b32 v199, a103\n"
"v_pk_mul_f32 v[192:193], v[192:193], v[148:149] op_sel_hi:[1,0]\n"
"v_pk_mul_f32 v[194:195], v[194:195], v[148:149] op_sel_hi:[1,0]\n"
"v_pk_mul_f32 v[196:197], v[196:197], v[148:149] op_sel_hi:[1,0]\n"
"v_pk_mul_f32 v[198:199], v[198:199], v[148:149] op_sel_hi:[1,0]\n"
"v_cvt_pk_bf16_f32 v224, v192, v193\n"
"v_cvt_pk_bf16_f32 v225, v194, v195\n"
"v_cvt_pk_bf16_f32 v226, v196, v197\n"
"v_cvt_pk_bf16_f32 v227, v198, v199\n"
"v_accvgpr_read_b32 v200, a104\n"
"v_accvgpr_read_b32 v201, a105\n"
"v_accvgpr_read_b32 v202, a106\n"
"v_permlane16_swap_b32 v224, v226\n"
"v_permlane16_swap_b32 v225, v227\n"
"v_accvgpr_read_b32 v203, a107\n"
"v_accvgpr_read_b32 v204, a108\n"
"v_accvgpr_read_b32 v205, a109\n"
"v_accvgpr_read_b32 v206, a110\n"
"v_accvgpr_read_b32 v207, a111\n"
"v_pk_mul_f32 v[200:201], v[200:201], v[148:149] op_sel_hi:[1,0]\n"
"v_pk_mul_f32 v[202:203], v[202:203], v[148:149] op_sel_hi:[1,0]\n"
"v_pk_mul_f32 v[204:205], v[204:205], v[148:149] op_sel_hi:[1,0]\n"
"v_pk_mul_f32 v[206:207], v[206:207], v[148:149] op_sel_hi:[1,0]\n"
"v_cvt_pk_bf16_f32 v228, v200, v201\n"
"v_cvt_pk_bf16_f32 v229, v202, v203\n"
"v_cvt_pk_bf16_f32 v230, v204, v205\n"
"v_cvt_pk_bf16_f32 v231, v206, v207\n"
"ds_write_b128 v186, v[224:227] offset:4096\n"
"s_waitcnt lgkmcnt(1)\n"
"buffer_store_dwordx4 v[240:243], v190, s[48:51], s72 offen offset:0 sc1\n"
"v_accvgpr_read_b32 v192, a112\n"
"v_accvgpr_read_b32 v193, a113\n"
"v_accvgpr_read_b32 v194, a114\n"
"v_permlane16_swap_b32 v228, v230\n"
"v_permlane16_swap_b32 v229, v231\n"
"v_accvgpr_read_b32 v195, a115\n"
"v_accvgpr_read_b32 v196, a116\n"
"v_accvgpr_read_b32 v197, a117\n"
"v_accvgpr_read_b32 v198, a118\n"
"v_accvgpr_read_b32 v199, a119\n"
"v_pk_mul_f32 v[192:193], v[192:193], v[148:149] op_sel_hi:[1,0]\n"
"v_pk_mul_f32 v[194:195], v[194:195], v[148:149] op_sel_hi:[1,0]\n"
"v_pk_mul_f32 v[196:197], v[196:197], v[148:149] op_sel_hi:[1,0]\n"
"v_pk_mul_f32 v[198:199], v[198:199], v[148:149] op_sel_hi:[1,0]\n"
"v_cvt_pk_bf16_f32 v232, v192, v193\n"
"v_cvt_pk_bf16_f32 v233, v194, v195\n"
"v_cvt_pk_bf16_f32 v234, v196, v197\n"
"v_cvt_pk_bf16_f32 v235, v198, v199\n"
"ds_write_b128 v187, v[228:231] offset:4096\n"
"buffer_store_dwordx4 v[244:247], v191, s[48:51], s72 offen offset:1024 sc1\n"
"v_accvgpr_read_b32 v200, a120\n"
"v_accvgpr_read_b32 v201, a121\n"
"v_accvgpr_read_b32 v202, a122\n"
"v_permlane16_swap_b32 v232, v234\n"
"v_permlane16_swap_b32 v233, v235\n"
"v_accvgpr_read_b32 v203, a123\n"
"v_accvgpr_read_b32 v204, a124\n"
"v_accvgpr_read_b32 v205, a125\n"
"v_accvgpr_read_b32 v206, a126\n"
"v_accvgpr_read_b32 v207, a127\n"
"v_pk_mul_f32 v[200:201], v[200:201], v[148:149] op_sel_hi:[1,0]\n"
"v_pk_mul_f32 v[202:203], v[202:203], v[148:149] op_sel_hi:[1,0]\n"
"v_pk_mul_f32 v[204:205], v[204:205], v[148:149] op_sel_hi:[1,0]\n"
"v_pk_mul_f32 v[206:207], v[206:207], v[148:149] op_sel_hi:[1,0]\n"
"v_cvt_pk_bf16_f32 v236, v200, v201\n"
"v_cvt_pk_bf16_f32 v237, v202, v203\n"
"v_cvt_pk_bf16_f32 v238, v204, v205\n"
"v_cvt_pk_bf16_f32 v239, v206, v207\n"
"ds_write_b128 v186, v[232:235] offset:4224\n"
"buffer_store_dwordx4 v[248:251], v190, s[48:51], s72 offen offset:2048 sc1\n"
"s_nop 1\n"
"v_permlane16_swap_b32 v236, v238\n"
"v_permlane16_swap_b32 v237, v239\n"
"ds_write_b128 v187, v[236:239] offset:4224\n"
"buffer_store_dwordx4 v[252:255], v191, s[48:51], s72 offen offset:3072 sc1\n"
"ds_read_b128 a[240:243], v188 offset:4096\n"
"ds_read_b128 a[244:247], v188 offset:5120\n"
"ds_read_b128 a[248:251], v188 offset:6144\n"
"ds_read_b128 a[252:255], v188 offset:7168\n"
"s_waitcnt lgkmcnt(0)\n"
"buffer_store_dwordx4 a[240:243], v190, s[48:51], s73 offen offset:0 sc1\n"
"buffer_store_dwordx4 a[244:247], v191, s[48:51], s73 offen offset:1024 sc1\n"
"buffer_store_dwordx4 a[248:251], v190, s[48:51], s73 offen offset:2048 sc1\n"
"buffer_store_dwordx4 a[252:255], v191, s[48:51], s73 offen offset:3072 sc1\n"
"s_branch L_done_%=\n"
"L_done_%=:\n"
"s_or_b32 s62, s60, s61\n"
"v_mov_b32 v0, s53\n"
"v_mov_b32 v1, s62\n"
"v_mov_b32 v2, 0\n"
"ds_write_b32 v0, v1\n"
"s_waitcnt lgkmcnt(0)\n"
"s_barrier\n"
"ds_read_b32 v4, v2 offset:0\n"
"ds_read_b32 v5, v2 offset:4096\n"
"ds_read_b32 v6, v2 offset:8192\n"
"ds_read_b32 v7, v2 offset:12288\n"
"s_waitcnt lgkmcnt(0)\n"
"v_or_b32 v4, v4, v5\n"
"v_or3_b32 v4, v4, v6, v7\n"
"s_nop 0\n"
"v_readfirstlane_b32 s62, v4\n"
"s_nop 3\n"
"s_cmp_lg_u32 s62, 0\n"
"s_cbranch_scc1 L_redo_%=\n"
"s_endpgm\n"
"L_redo_%=:\n"
"s_waitcnt vmcnt(0) lgkmcnt(0)\n"
"s_mov_b32 %0, s62\n"
      : "=s"(fail) : "s"(Kp), "s"(Vp), "s"(Qp), "s"(Op), "s"(wave), "s"(mode), "s"(wsw), "s"(slot)
      : "memory", "vcc", "scc", "s36", "s37", "s38", "s39", "s40", "s41", "s42", "s43", "s44", "s45", "s46", "s47", "s48", "s49", "s50", "s51",
        "s52", "s53", "s54", "s55", "s56", "s57", "s58", "s59", "s60", "s61", "s62", "s63", "s64", "s65", "s66", "s67",
        "s68", "s69", "s70", "s71", "s72", "s73", "s74", "s75", "v0", "v1", "v2", "v3", "v4", "v5", "v6", "v7",
        "v8", "v9", "v10", "v11", "v12", "v13", "v14", "v15", "v16", "v17", "v18", "v19", "v20", "v21", "v22", "v23",
        "v24", "v25", "v26", "v27", "v28", "v29", "v30", "v31", "v32", "v33", "v34", "v35", "v36", "v37", "v38", "v39",
        "v40", "v41", "v42", "v43", "v44", "v45", "v46", "v47", "v48", "v49", "v50", "v51", "v52", "v53", "v54", "v55",
        "v56", "v57", "v58", "v59", "v60", "v61", "v62", "v63", "v64", "v65", "v66", "v67", "v68", "v69", "v70", "v71",
        "v72", "v73", "v74", "v75", "v76", "v77", "v78", "v79", "v80", "v81", "v82", "v83", "v84", "v85", "v86", "v87",
        "v88", "v89", "v90", "v91", "v92", "v93", "v94", "v95", "v96", "v97", "v98", "v99", "v100", "v101", "v102", "v103",
        "v104", "v105", "v106", "v107", "v108", "v109", "v110", "v111", "v112", "v113", "v114", "v115", "v116", "v117", "v118", "v119",
        "v120", "v121", "v122", "v123", "v124", "v125", "v126", "v127", "v128", "v129", "v130", "v131", "v132", "v133", "v134", "v135",
        "v136", "v137", "v138", "v139", "v140", "v141", "v142", "v143", "v144", "v145", "v146", "v147", "v148", "v149", "v150", "v151",
        "v152", "v153", "v154", "v155", "v156", "v157", "v158", "v159", "v160", "v161", "v162", "v163", "v164", "v165", "v166", "v167",
        "v168", "v169", "v170", "v171", "v172", "v173", "v174", "v175", "v176", "v177", "v178", "v179", "v180", "v181", "v182", "v183",
        "v184", "v185", "v186", "v187", "v188", "v189", "v190", "v191", "v192", "v193", "v194", "v195", "v196", "v197", "v198", "v199",
        "v200", "v201", "v202", "v203", "v204", "v205", "v206", "v207", "v208", "v209", "v210", "v211", "v212", "v213", "v214", "v215",
        "v216", "v217", "v218", "v219", "v220", "v221", "v222", "v223", "v224", "v225", "v226", "v227", "v228", "v229", "v230", "v231",
        "v232", "v233", "v234", "v235", "v236", "v237", "v238", "v239", "v240", "v241", "v242", "v243", "v244", "v245", "v246", "v247",
        "v248", "v249", "v250", "v251", "v252", "v253", "v254", "v255", "a0", "a1", "a2", "a3", "a4", "a5", "a6", "a7",
        "a8", "a9", "a10", "a11", "a12", "a13", "a14", "a15", "a16", "a17", "a18", "a19", "a20", "a21", "a22", "a23",
        "a24", "a25", "a26", "a27", "a28", "a29", "a30", "a31", "a32", "a33", "a34", "a35", "a36", "a37", "a38", "a39",
        "a40", "a41", "a42", "a43", "a44", "a45", "a46", "a47", "a48", "a49", "a50", "a51", "a52", "a53", "a54", "a55",
        "a56", "a57", "a58", "a59", "a60", "a61", "a62", "a63", "a64", "a65", "a66", "a67", "a68", "a69", "a70", "a71",
        "a72", "a73", "a74", "a75", "a76", "a77", "a78", "a79", "a80", "a81", "a82", "a83", "a84", "a85", "a86", "a87",
        "a88", "a89", "a90", "a91", "a92", "a93", "a94", "a95", "a96", "a97", "a98", "a99", "a100", "a101", "a102", "a103",
        "a104", "a105", "a106", "a107", "a108", "a109", "a110", "a111", "a112", "a113", "a114", "a115", "a116", "a117", "a118", "a119",
        "a120", "a121", "a122", "a123", "a124", "a125", "a126", "a127", "a128", "a129", "a130", "a131", "a132", "a133", "a134", "a135",
        "a136", "a137", "a138", "a139", "a140", "a141", "a142", "a143", "a144", "a145", "a146", "a147", "a148", "a149", "a150", "a151",
        "a152", "a153", "a154", "a155", "a156", "a157", "a158", "a159", "a160", "a161", "a162", "a163", "a164", "a165", "a166", "a167",
        "a168", "a169", "a170", "a171", "a172", "a173", "a174", "a175", "a176", "a177", "a178", "a179", "a180", "a181", "a182", "a183",
        "a184", "a185", "a186", "a187", "a188", "a189", "a190", "a191", "a192", "a193", "a194", "a195", "a196", "a197", "a198", "a199",
        "a200", "a201", "a202", "a203", "a204", "a205", "a206", "a207", "a208", "a209", "a210", "a211", "a212", "a213", "a214", "a215",
        "a216", "a217", "a218", "a219", "a220", "a221", "a222", "a223", "a224", "a225", "a226", "a227", "a228", "a229", "a230", "a231",
        "a232", "a233", "a234", "a235", "a236", "a237", "a238", "a239", "a240", "a241", "a242", "a243", "a244", "a245", "a246", "a247",
        "a248", "a249", "a250", "a251", "a252", "a253", "a254", "a255");
  return fail;
}

extern "C" __global__ __launch_bounds__(256, 1) void attn_fwd_kernel(const unsigned short* __restrict__ Qg, const unsigned short* __restrict__ Kg,
                                                                    const unsigned short* __restrict__ Vg, unsigned short* __restrict__ Og, float* __restrict__ ws) {
  const int bid = blockIdx.x, xcd = bid & 7, idx = bid >> 3, pair = idx >> 1, half = idx & 1;
  const int h0 = (xcd * 16 + pair) * 2;
  const unsigned wave = __builtin_amdgcn_readfirstlane(threadIdx.x >> 6);
  const unsigned short* Kp = Kg + (long)h0 * SK * HD;
  const unsigned short* Vp = Vg + (long)h0 * SK * HD;
  const unsigned short* Qp = Qg + ((long)h0 * SQ + half * 304) * HD;
  unsigned short* Op = Og + ((long)h0 * SQ + half * 304) * HD;
  float* wsw = ws + ((long)bid * 4 + wave) * 640;
  (void)attn_body(Kp, Vp, Qp, Op, wave, 0u, wsw, idx * 4 + wave);
  unsigned lane; asm volatile("v_mbcnt_lo_u32_b32 %0, -1, 0\n\tv_mbcnt_hi_u32_b32 %0, -1, %0" : "=v"(lane));
  true_max(Qp, Kp, wsw, wave, lane);
  asm volatile("s_waitcnt vmcnt(0)" ::: "memory");
  __syncthreads();
  (void)attn_body(Kp, Vp, Qp, Op, wave, 1u, wsw, 0u);
}

extern "C" void kernel_launch(void* const* d_in, const int* in_sizes, int n_in, void* d_out, int out_size, void* d_ws, size_t ws_size, hipStream_t stream) {
  (void)hipFuncSetAttribute((const void*)attn_fwd_kernel, hipFuncAttributeMaxDynamicSharedMemorySize, LDS_BYTES);
  hipLaunchKernelGGL(attn_fwd_kernel, dim3(256), dim3(256), LDS_BYTES, stream, (const unsigned short*)d_in[0], (const unsigned short*)d_in[1],
                     (const unsigned short*)d_in[2], (unsigned short*)d_out, (float*)d_ws);
}
```
